```python
import torch

def _softmax(x, axis=-1):
    return torch.softmax(x, dim=axis)
def _gelu(x):
    return torch.nn.functional.gelu(x, approximate="tanh")
def _silu(x):
    return torch.nn.functional.silu(x)
def _sigmoid(x):
    return torch.sigmoid(x)
def _layernorm(x, axis=-1, eps=1e-5):
    m = x.mean(dim=axis, keepdim=True)
    v = x.var(dim=axis, keepdim=True, unbiased=False)
    return (x - m) / torch.sqrt(v + eps)
def _scatter_add(tgt, idx, src, axis=0):
    return tgt.clone().index_add_(axis, idx, src)
def _segment_sum(src, seg, n):
    out = torch.zeros((n,) + src.shape[1:], dtype=src.dtype)
    return out.index_add_(0, seg, src)
def _pad(x, pw):
    flat = tuple(v for pair in reversed(pw) for v in pair)
    return torch.nn.functional.pad(x, flat)

kernel_name = 'standalone__mha_softmax__near_ridge'

def setup_inputs():

    _w = (lambda a: (lambda t: t.to(torch.bfloat16) if t.is_floating_point() else t)(torch.as_tensor(a)))
    B, H, Sq, Sk, D = 8, 16, 384, 2048, 256
    return {
        "q": _w((lambda *s: torch.randn(*s, dtype=torch.bfloat16) * 0.8)(B, H, Sq, D)),
        "k": _w((lambda *s: torch.randn(*s, dtype=torch.bfloat16) * 0.8)(B, H, Sk, D)),
        "v": _w((lambda *s: torch.randn(*s, dtype=torch.bfloat16) * 0.05)(B, H, Sk, D)),
    }

def reference(q, k, v):
    scale = q.shape[-1] ** -0.5
    scores = torch.einsum("bhqd,bhkd->bhqk", q, k) * scale
    p = _softmax(scores, axis=-1)
    return torch.einsum("bhqk,bhkd->bhqd", p, v)
```

```cpp
#include <hip/hip_runtime.h>
#include <hip/hip_bf16.h>

typedef __bf16 bf16x8 __attribute__((ext_vector_type(8)));
typedef float f32x4 __attribute__((ext_vector_type(4)));
typedef unsigned short u16;

constexpr int NB = 8, NH = 16, SQ = 384, SK = 2048, D = 256;

__global__ __launch_bounds__(256) void attn_base(const u16* __restrict__ Q, const u16* __restrict__ K,
                                                 const u16* __restrict__ V, u16* __restrict__ O) {
  __shared__ __attribute__((aligned(16))) u16 Ks[32][D + 8];
  __shared__ __attribute__((aligned(16))) u16 Vs[32][D + 8];
  const int tid = threadIdx.x, lane = tid & 63, w = tid >> 6;
  const int head = blockIdx.x / 6, qb = blockIdx.x % 6;
  const int c = lane & 15, g = lane >> 4;
  const int qrow = qb * 64 + w * 16 + c;
  const u16* Qp = Q + ((size_t)head * SQ + qrow) * D;
  bf16x8 qf[8];
#pragma unroll
  for (int s = 0; s < 8; ++s) qf[s] = *(const bf16x8*)(Qp + 32 * s + 8 * g);
  f32x4 o[16];
#pragma unroll
  for (int i = 0; i < 16; ++i) o[i] = f32x4{0, 0, 0, 0};
  float m = -INFINITY, l = 0.f;
  const float cs = 0.0625f * 1.4426950408889634f;
  for (int t = 0; t < SK / 32; ++t) {
    __syncthreads();
    for (int i = tid; i < 32 * 32; i += 256) {
      int r = i >> 5, ch = i & 31;
      *(bf16x8*)&Ks[r][ch * 8] = *(const bf16x8*)(K + ((size_t)head * SK + t * 32 + r) * D + ch * 8);
      *(bf16x8*)&Vs[r][ch * 8] = *(const bf16x8*)(V + ((size_t)head * SK + t * 32 + r) * D + ch * 8);
    }
    __syncthreads();
    f32x4 s0 = {0, 0, 0, 0}, s1 = {0, 0, 0, 0};
#pragma unroll
    for (int s = 0; s < 8; ++s) {
      bf16x8 k0 = *(const bf16x8*)&Ks[c][32 * s + 8 * g];
      bf16x8 k1 = *(const bf16x8*)&Ks[16 + c][32 * s + 8 * g];
      s0 = __builtin_amdgcn_mfma_f32_16x16x32_bf16(k0, qf[s], s0, 0, 0, 0);
      s1 = __builtin_amdgcn_mfma_f32_16x16x32_bf16(k1, qf[s], s1, 0, 0, 0);
    }
    float mx = m;
#pragma unroll
    for (int i = 0; i < 4; ++i) mx = fmaxf(mx, fmaxf(s0[i], s1[i]));
    mx = fmaxf(mx, __shfl_xor(mx, 16));
    mx = fmaxf(mx, __shfl_xor(mx, 32));
    const float alpha = exp2f((m - mx) * cs);
    m = mx;
    float p[8], ps = 0.f;
#pragma unroll
    for (int i = 0; i < 4; ++i) { p[i] = exp2f((s0[i] - mx) * cs); p[4 + i] = exp2f((s1[i] - mx) * cs); }
    bf16x8 pf;
#pragma unroll
    for (int i = 0; i < 8; ++i) { pf[i] = (__bf16)p[i]; ps += p[i]; }
    l = l * alpha + ps;
#pragma unroll
    for (int db = 0; db < 16; ++db) {
      bf16x8 vf;
#pragma unroll
      for (int j = 0; j < 8; ++j) {
        int key = (j < 4) ? (4 * g + j) : (16 + 4 * g + j - 4);
        vf[j] = __builtin_bit_cast(__bf16, Vs[key][16 * db + c]);
      }
      o[db] *= alpha;
      o[db] = __builtin_amdgcn_mfma_f32_16x16x32_bf16(vf, pf, o[db], 0, 0, 0);
    }
  }
  l += __shfl_xor(l, 16);
  l += __shfl_xor(l, 32);
  const float il = 1.f / l;
  u16* Op = O + ((size_t)head * SQ + qrow) * D;
#pragma unroll
  for (int db = 0; db < 16; ++db)
#pragma unroll
    for (int i = 0; i < 4; ++i) Op[16 * db + 4 * g + i] = __builtin_bit_cast(u16, (__bf16)(o[db][i] * il));
}

extern "C" void kernel_launch(void* const* d_in, const int* in_sizes, int n_in, void* d_out, int out_size,
                              void* d_ws, size_t ws_size, hipStream_t stream) {
  attn_base<<<NB * NH * 6, 256, 0, stream>>>((const u16*)d_in[0], (const u16*)d_in[1], (const u16*)d_in[2], (u16*)d_out);
}
```

```cpp
#include <hip/hip_runtime.h>
#include <hip/hip_bf16.h>

__global__ __launch_bounds__(256, 1) void attn_fwd_d256(const void* q, const void* k, const void* v, void* o) {
  __shared__ __attribute__((aligned(1024))) char lds[163840];
  const unsigned tid = threadIdx.x, bid = blockIdx.x;
  const unsigned long long args = (unsigned long long)__builtin_amdgcn_kernarg_segment_ptr();
  asm volatile("" ::"s"((unsigned)(size_t)lds));
  asm volatile(
      "s_load_dwordx8 s[8:15], %[args], 0x0\n"
      "L_entry_%=:\n"
      "s_getpc_b64 s[34:35]\n"
      "s_lshr_b32 s29, %[bid], 3\n"
      "v_lshlrev_b32 v22, 4, v0\n"
      "s_lshl_b32 s29, s29, 12\n"
      "v_add_u32 v22, s29, v22\n"
      "v_min_u32 v22, (L_done_%= - L_entry_%=), v22\n"
      "global_load_dword v23, v22, s[34:35]\n"
      "s_mov_b32 s21, 0\n"
      "v_mov_b32 v7, 0xff800000\n"
      "v_mov_b32 v9, 0xff800000\n"
      "v_mov_b32 v11, 0xff800000\n"
      "L_start_%=:\n"
      "v_readfirstlane_b32 s22, v0\n"
      "s_and_b32 s29, %[bid], 7\n"
      "s_lshr_b32 s30, %[bid], 4\n"
      "s_lshl_b32 s30, s30, 3\n"
      "s_add_u32 s29, s29, s30\n"
      "s_lshl_b32 s31, s29, 20\n"
      "s_lshr_b32 s22, s22, 6\n"
      "s_lshl_b32 s23, s22, 13\n"
      "s_mov_b32 s52, 4096\n"
      "v_and_b32 v16, 63, v0\n"
      "v_lshrrev_b32 v17, 3, v16\n"
      "v_and_b32 v5, 6, v17\n"
      "v_and_b32 v16, 7, v16\n"
      "v_xor_b32 v5, v5, v16\n"
      "v_lshlrev_b32 v5, 4, v5\n"
      "v_lshl_add_u32 v5, v17, 9, v5\n"
      "v_add_u32 v1, s23, v5\n"
      "v_mov_b32 v2, v1\n"
      "v_and_b32 v16, 63, v0\n"
      "v_lshrrev_b32 v17, 4, v16\n"
      "v_and_b32 v18, 6, v16\n"
      "v_xor_b32 v18, v18, v17\n"
      "v_lshlrev_b32 v18, 4, v18\n"
      "v_and_b32 v19, 15, v16\n"
      "v_lshl_add_u32 v3, v19, 7, v18\n"
      "v_xor_b32 v4, 64, v3\n"
      "v_lshrrev_b32 v18, 2, v16\n"
      "v_and_b32 v19, 6, v18\n"
      "v_bfe_u32 v17, v16, 1, 1\n"
      "v_xor_b32 v19, v19, v17\n"
      "v_lshlrev_b32 v19, 4, v19\n"
      "v_lshl_add_u32 v18, v18, 7, v19\n"
      "v_and_b32 v19, 1, v16\n"
      "v_lshl_add_u32 v18, v19, 3, v18\n"
      "v_add_u32 v12, 0x10000, v18\n"
      "v_xor_b32 v13, 32, v12\n"
      "v_xor_b32 v14, 64, v12\n"
      "v_xor_b32 v15, 96, v12\n"
      "s_waitcnt lgkmcnt(0)\n"
      "s_add_u32 s36, s10, s31\n"
      "s_addc_u32 s37, s11, 0\n"
      "s_mov_b32 s38, 1048576\n"
      "s_mov_b32 s39, 0x20000\n"
      "s_add_u32 m0, s23, 0\n"
      "s_nop 0\n"
      "buffer_load_dwordx4 v1, s[36:39], 0 offen offset:0 nt lds\n"
      "s_add_u32 m0, s23, 1024\n"
      "s_nop 0\n"
      "buffer_load_dwordx4 v1, s[36:39], s52 offen offset:0 nt lds\n"
      "s_add_u32 m0, s23, 1920\n"
      "s_nop 0\n"
      "buffer_load_dwordx4 v1, s[36:39], 0 offen offset:128 nt lds\n"
      "s_add_u32 m0, s23, 2944\n"
      "s_nop 0\n"
      "buffer_load_dwordx4 v1, s[36:39], s52 offen offset:128 nt lds\n"
      "s_add_u32 s40, s12, s31\n"
      "s_addc_u32 s41, s13, 0\n"
      "s_mov_b32 s42, 1048576\n"
      "s_mov_b32 s43, 0x20000\n"
      "s_lshr_b32 s30, %[bid], 3\n"
      "s_and_b32 s30, s30, 1\n"
      "s_lshl_b32 s29, s29, 3\n"
      "s_lshl_b32 s30, s30, 2\n"
      "s_add_u32 s29, s29, s30\n"
      "s_add_u32 s29, s29, s22\n"
      "s_mul_i32 s29, s29, 24576\n"
      "s_add_u32 s44, s8, s29\n"
      "s_addc_u32 s45, s9, 0\n"
      "s_mov_b32 s46, 24576\n"
      "s_mov_b32 s47, 0x20000\n"
      "s_add_u32 s48, s14, s29\n"
      "s_addc_u32 s49, s15, 0\n"
      "s_mov_b32 s50, 24576\n"
      "s_mov_b32 s51, 0x20000\n"
      "s_mov_b32 s53, 8192\n"
      "s_mov_b32 s54, 12288\n"
      "s_mov_b32 s55, 16384\n"
      "s_mov_b32 s56, 20480\n"
      "s_mov_b32 s24, 0x3db8aa3b\n"
      "s_add_u32 m0, s23, 3840\n"
      "s_nop 0\n"
      "buffer_load_dwordx4 v1, s[36:39], 0 offen offset:256 nt lds\n"
      "s_add_u32 m0, s23, 4864\n"
      "s_nop 0\n"
      "buffer_load_dwordx4 v1, s[36:39], s52 offen offset:256 nt lds\n"
      "s_add_u32 m0, s23, 5760\n"
      "s_nop 0\n"
      "buffer_load_dwordx4 v1, s[36:39], 0 offen offset:384 nt lds\n"
      "s_add_u32 m0, s23, 6784\n"
      "s_nop 0\n"
      "buffer_load_dwordx4 v1, s[36:39], s52 offen offset:384 nt lds\n"
      "v_add_u32 v1, 0x8000, v1\n"
      "s_add_u32 m0, s23, 32768\n"
      "s_nop 0\n"
      "buffer_load_dwordx4 v5, s[44:47], 0 offen offset:0 nt lds\n"
      "s_add_u32 m0, s23, 33792\n"
      "s_nop 0\n"
      "buffer_load_dwordx4 v5, s[44:47], s52 offen offset:0 nt lds\n"
      "s_add_u32 m0, s23, 34688\n"
      "s_nop 0\n"
      "buffer_load_dwordx4 v5, s[44:47], 0 offen offset:128 nt lds\n"
      "s_add_u32 m0, s23, 35712\n"
      "s_nop 0\n"
      "buffer_load_dwordx4 v5, s[44:47], s52 offen offset:128 nt lds\n"
      "v_mov_b32 v16, 0x00000000\n"
      "v_accvgpr_write_b32 a200, v16\n"
      "v_accvgpr_write_b32 a201, v16\n"
      "v_accvgpr_write_b32 a202, v16\n"
      "v_accvgpr_write_b32 a203, v16\n"
      "v_accvgpr_write_b32 a204, v16\n"
      "v_accvgpr_write_b32 a205, v16\n"
      "v_accvgpr_write_b32 a206, v16\n"
      "v_accvgpr_write_b32 a207, v16\n"
      "v_accvgpr_write_b32 a208, v16\n"
      "v_accvgpr_write_b32 a209, v16\n"
      "v_accvgpr_write_b32 a210, v16\n"
      "v_accvgpr_write_b32 a211, v16\n"
      "v_accvgpr_write_b32 a212, v16\n"
      "v_accvgpr_write_b32 a213, v16\n"
      "v_accvgpr_write_b32 a214, v16\n"
      "v_accvgpr_write_b32 a215, v16\n"
      "v_accvgpr_write_b32 a216, v16\n"
      "v_accvgpr_write_b32 a217, v16\n"
      "v_accvgpr_write_b32 a218, v16\n"
      "v_accvgpr_write_b32 a219, v16\n"
      "v_accvgpr_write_b32 a220, v16\n"
      "v_accvgpr_write_b32 a221, v16\n"
      "v_accvgpr_write_b32 a222, v16\n"
      "v_accvgpr_write_b32 a223, v16\n"
      "v_accvgpr_write_b32 a224, v16\n"
      "v_accvgpr_write_b32 a225, v16\n"
      "v_accvgpr_write_b32 a226, v16\n"
      "v_accvgpr_write_b32 a227, v16\n"
      "v_accvgpr_write_b32 a228, v16\n"
      "v_accvgpr_write_b32 a229, v16\n"
      "v_accvgpr_write_b32 a230, v16\n"
      "v_accvgpr_write_b32 a231, v16\n"
      "v_accvgpr_write_b32 a232, v16\n"
      "v_accvgpr_write_b32 a233, v16\n"
      "v_accvgpr_write_b32 a234, v16\n"
      "v_accvgpr_write_b32 a235, v16\n"
      "v_accvgpr_write_b32 a236, v16\n"
      "v_accvgpr_write_b32 a237, v16\n"
      "v_accvgpr_write_b32 a238, v16\n"
      "v_accvgpr_write_b32 a239, v16\n"
      "v_accvgpr_write_b32 a240, v16\n"
      "v_accvgpr_write_b32 a241, v16\n"
      "v_accvgpr_write_b32 a242, v16\n"
      "v_accvgpr_write_b32 a243, v16\n"
      "v_mov_b32 v132, 0x00000000\n"
      "v_mov_b32 v133, 0x00000000\n"
      "v_mov_b32 v134, 0x00000000\n"
      "v_mov_b32 v135, 0x00000000\n"
      "v_mov_b32 v136, 0x00000000\n"
      "v_mov_b32 v137, 0x00000000\n"
      "v_mov_b32 v138, 0x00000000\n"
      "v_mov_b32 v139, 0x00000000\n"
      "v_mov_b32 v140, 0x00000000\n"
      "v_mov_b32 v141, 0x00000000\n"
      "v_mov_b32 v142, 0x00000000\n"
      "v_mov_b32 v143, 0x00000000\n"
      "v_mov_b32 v144, 0x00000000\n"
      "v_mov_b32 v145, 0x00000000\n"
      "v_mov_b32 v146, 0x00000000\n"
      "v_mov_b32 v147, 0x00000000\n"
      "v_mov_b32 v148, 0x00000000\n"
      "v_mov_b32 v149, 0x00000000\n"
      "v_mov_b32 v150, 0x00000000\n"
      "v_mov_b32 v151, 0x00000000\n"
      "v_mov_b32 v152, 0x00000000\n"
      "v_mov_b32 v153, 0x00000000\n"
      "v_mov_b32 v154, 0x00000000\n"
      "v_mov_b32 v155, 0x00000000\n"
      "v_mov_b32 v156, 0x00000000\n"
      "v_mov_b32 v157, 0x00000000\n"
      "v_mov_b32 v158, 0x00000000\n"
      "v_mov_b32 v159, 0x00000000\n"
      "v_mov_b32 v160, 0x00000000\n"
      "v_mov_b32 v161, 0x00000000\n"
      "v_mov_b32 v162, 0x00000000\n"
      "v_mov_b32 v163, 0x00000000\n"
      "v_mov_b32 v164, 0x00000000\n"
      "v_mov_b32 v165, 0x00000000\n"
      "v_mov_b32 v166, 0x00000000\n"
      "v_mov_b32 v167, 0x00000000\n"
      "v_mov_b32 v168, 0x00000000\n"
      "v_mov_b32 v169, 0x00000000\n"
      "v_mov_b32 v170, 0x00000000\n"
      "v_mov_b32 v171, 0x00000000\n"
      "v_mov_b32 v172, 0x00000000\n"
      "v_mov_b32 v173, 0x00000000\n"
      "v_mov_b32 v174, 0x00000000\n"
      "v_mov_b32 v175, 0x00000000\n"
      "v_mov_b32 v176, 0x00000000\n"
      "v_mov_b32 v177, 0x00000000\n"
      "v_mov_b32 v178, 0x00000000\n"
      "v_mov_b32 v179, 0x00000000\n"
      "v_mov_b32 v180, 0x00000000\n"
      "v_mov_b32 v181, 0x00000000\n"
      "v_mov_b32 v182, 0x00000000\n"
      "v_mov_b32 v183, 0x00000000\n"
      "v_mov_b32 v184, 0x00000000\n"
      "v_mov_b32 v185, 0x00000000\n"
      "v_mov_b32 v186, 0x00000000\n"
      "v_mov_b32 v187, 0x00000000\n"
      "v_mov_b32 v188, 0x00000000\n"
      "v_mov_b32 v189, 0x00000000\n"
      "v_mov_b32 v190, 0x00000000\n"
      "v_mov_b32 v191, 0x00000000\n"
      "v_mov_b32 v192, 0x00000000\n"
      "v_mov_b32 v193, 0x00000000\n"
      "v_mov_b32 v194, 0x00000000\n"
      "v_mov_b32 v195, 0x00000000\n"
      "v_mov_b32 v196, 0x00000000\n"
      "v_mov_b32 v197, 0x00000000\n"
      "v_mov_b32 v198, 0x00000000\n"
      "v_mov_b32 v199, 0x00000000\n"
      "v_mov_b32 v200, 0x00000000\n"
      "v_mov_b32 v201, 0x00000000\n"
      "v_mov_b32 v202, 0x00000000\n"
      "v_mov_b32 v203, 0x00000000\n"
      "v_mov_b32 v204, 0x00000000\n"
      "v_mov_b32 v205, 0x00000000\n"
      "v_mov_b32 v206, 0x00000000\n"
      "v_mov_b32 v207, 0x00000000\n"
      "v_mov_b32 v208, 0x00000000\n"
      "v_mov_b32 v209, 0x00000000\n"
      "v_mov_b32 v210, 0x00000000\n"
      "v_mov_b32 v211, 0x00000000\n"
      "v_mov_b32 v212, 0x00000000\n"
      "v_mov_b32 v213, 0x00000000\n"
      "v_mov_b32 v214, 0x00000000\n"
      "v_mov_b32 v215, 0x00000000\n"
      "v_mov_b32 v216, 0x00000000\n"
      "v_mov_b32 v217, 0x00000000\n"
      "v_mov_b32 v218, 0x00000000\n"
      "v_mov_b32 v219, 0x00000000\n"
      "v_mov_b32 v220, 0x00000000\n"
      "v_mov_b32 v221, 0x00000000\n"
      "v_mov_b32 v222, 0x00000000\n"
      "v_mov_b32 v223, 0x00000000\n"
      "v_mov_b32 v224, 0x00000000\n"
      "v_mov_b32 v225, 0x00000000\n"
      "v_mov_b32 v226, 0x00000000\n"
      "v_mov_b32 v227, 0x00000000\n"
      "v_mov_b32 v228, 0x00000000\n"
      "v_mov_b32 v229, 0x00000000\n"
      "v_mov_b32 v230, 0x00000000\n"
      "v_mov_b32 v231, 0x00000000\n"
      "v_mov_b32 v232, 0x00000000\n"
      "v_mov_b32 v233, 0x00000000\n"
      "v_mov_b32 v234, 0x00000000\n"
      "v_mov_b32 v235, 0x00000000\n"
      "v_mov_b32 v236, 0x00000000\n"
      "v_mov_b32 v237, 0x00000000\n"
      "v_mov_b32 v238, 0x00000000\n"
      "v_mov_b32 v239, 0x00000000\n"
      "v_mov_b32 v240, 0x00000000\n"
      "v_mov_b32 v241, 0x00000000\n"
      "v_mov_b32 v242, 0x00000000\n"
      "v_mov_b32 v243, 0x00000000\n"
      "v_mov_b32 v244, 0x00000000\n"
      "v_mov_b32 v245, 0x00000000\n"
      "v_mov_b32 v246, 0x00000000\n"
      "v_mov_b32 v247, 0x00000000\n"
      "v_mov_b32 v248, 0x00000000\n"
      "v_mov_b32 v249, 0x00000000\n"
      "v_mov_b32 v250, 0x00000000\n"
      "v_mov_b32 v251, 0x00000000\n"
      "v_mov_b32 v252, 0x00000000\n"
      "v_mov_b32 v253, 0x00000000\n"
      "v_mov_b32 v254, 0x00000000\n"
      "v_mov_b32 v255, 0x00000000\n"
      "v_mov_b32 v6, 0\n"
      "v_mov_b32 v8, 0\n"
      "v_mov_b32 v10, 0\n"
      "s_mov_b32 s20, 15\n"
      "s_add_u32 m0, s23, 36608\n"
      "s_nop 0\n"
      "buffer_load_dwordx4 v5, s[44:47], 0 offen offset:256 nt lds\n"
      "s_add_u32 m0, s23, 37632\n"
      "s_nop 0\n"
      "buffer_load_dwordx4 v5, s[44:47], s52 offen offset:256 nt lds\n"
      "s_add_u32 m0, s23, 38528\n"
      "s_nop 0\n"
      "buffer_load_dwordx4 v5, s[44:47], 0 offen offset:384 nt lds\n"
      "s_add_u32 m0, s23, 39552\n"
      "s_nop 0\n"
      "buffer_load_dwordx4 v5, s[44:47], s52 offen offset:384 nt lds\n"
      "s_add_u32 m0, s23, 98304\n"
      "s_nop 0\n"
      "buffer_load_dwordx4 v5, s[44:47], s53 offen offset:0 nt lds\n"
      "s_add_u32 m0, s23, 99328\n"
      "s_nop 0\n"
      "buffer_load_dwordx4 v5, s[44:47], s54 offen offset:0 nt lds\n"
      "s_add_u32 m0, s23, 100224\n"
      "s_nop 0\n"
      "buffer_load_dwordx4 v5, s[44:47], s53 offen offset:128 nt lds\n"
      "s_waitcnt vmcnt(11)\n"
      "s_barrier\n"
      "s_add_u32 s29, s23, 32768\n"
      "v_add_u32 v20, s29, v3\n"
      "v_add_u32 v21, s29, v4\n"
      "ds_read_b128 a[0:3], v3 offset:0\n"
      "ds_read_b128 a[4:7], v3 offset:8192\n"
      "ds_read_b128 a[8:11], v3 offset:16384\n"
      "ds_read_b128 a[12:15], v3 offset:24576\n"
      "ds_read_b128 a[16:19], v4 offset:0\n"
      "ds_read_b128 a[20:23], v4 offset:8192\n"
      "ds_read_b128 a[24:27], v4 offset:16384\n"
      "ds_read_b128 a[28:31], v4 offset:24576\n"
      "s_add_u32 m0, s23, 101248\n"
      "s_nop 0\n"
      "buffer_load_dwordx4 v5, s[44:47], s54 offen offset:128 nt lds\n"
      "s_add_u32 m0, s23, 102144\n"
      "s_nop 0\n"
      "buffer_load_dwordx4 v5, s[44:47], s53 offen offset:256 nt lds\n"
      "s_waitcnt vmcnt(11)\n"
      "ds_read_b128 v[108:111], v20 offset:0\n"
      "ds_read_b128 v[112:115], v21 offset:0\n"
      "s_waitcnt lgkmcnt(1)\n"
      "v_lshlrev_b32 v116, 16, v108\n"
      "v_and_b32 v117, 0xffff0000, v108\n"
      "v_lshlrev_b32 v118, 16, v109\n"
      "v_and_b32 v119, 0xffff0000, v109\n"
      "v_lshlrev_b32 v120, 16, v110\n"
      "v_and_b32 v121, 0xffff0000, v110\n"
      "v_lshlrev_b32 v122, 16, v111\n"
      "v_and_b32 v123, 0xffff0000, v111\n"
      "s_waitcnt lgkmcnt(0)\n"
      "v_pk_mul_f32 v[116:117], v[116:117], s[24:25] op_sel_hi:[1,0]\n"
      "v_pk_mul_f32 v[118:119], v[118:119], s[24:25] op_sel_hi:[1,0]\n"
      "v_pk_mul_f32 v[120:121], v[120:121], s[24:25] op_sel_hi:[1,0]\n"
      "v_pk_mul_f32 v[122:123], v[122:123], s[24:25] op_sel_hi:[1,0]\n"
      "v_cvt_pk_bf16_f32 v116, v116, v117\n"
      "v_cvt_pk_bf16_f32 v117, v118, v119\n"
      "v_cvt_pk_bf16_f32 v118, v120, v121\n"
      "v_cvt_pk_bf16_f32 v119, v122, v123\n"
      "v_accvgpr_write_b32 a104, v116\n"
      "v_accvgpr_write_b32 a105, v117\n"
      "v_accvgpr_write_b32 a106, v118\n"
      "v_accvgpr_write_b32 a107, v119\n"
      "v_lshlrev_b32 v124, 16, v112\n"
      "v_and_b32 v125, 0xffff0000, v112\n"
      "v_lshlrev_b32 v126, 16, v113\n"
      "v_and_b32 v127, 0xffff0000, v113\n"
      "v_lshlrev_b32 v128, 16, v114\n"
      "v_and_b32 v129, 0xffff0000, v114\n"
      "v_lshlrev_b32 v130, 16, v115\n"
      "v_and_b32 v131, 0xffff0000, v115\n"
      "v_pk_mul_f32 v[124:125], v[124:125], s[24:25] op_sel_hi:[1,0]\n"
      "v_pk_mul_f32 v[126:127], v[126:127], s[24:25] op_sel_hi:[1,0]\n"
      "v_pk_mul_f32 v[128:129], v[128:129], s[24:25] op_sel_hi:[1,0]\n"
      "v_pk_mul_f32 v[130:131], v[130:131], s[24:25] op_sel_hi:[1,0]\n"
      "v_cvt_pk_bf16_f32 v124, v124, v125\n"
      "v_cvt_pk_bf16_f32 v125, v126, v127\n"
      "v_cvt_pk_bf16_f32 v126, v128, v129\n"
      "v_cvt_pk_bf16_f32 v127, v130, v131\n"
      "v_accvgpr_write_b32 a108, v124\n"
      "v_accvgpr_write_b32 a109, v125\n"
      "v_accvgpr_write_b32 a110, v126\n"
      "v_accvgpr_write_b32 a111, v127\n"
      "ds_read_b128 a[32:35], v3 offset:2048\n"
      "ds_read_b128 a[36:39], v3 offset:10240\n"
      "ds_read_b128 a[40:43], v3 offset:18432\n"
      "ds_read_b128 a[44:47], v3 offset:26624\n"
      "ds_read_b128 a[48:51], v4 offset:2048\n"
      "ds_read_b128 a[52:55], v4 offset:10240\n"
      "ds_read_b128 a[56:59], v4 offset:18432\n"
      "ds_read_b128 a[60:63], v4 offset:26624\n"
      "s_add_u32 m0, s23, 103168\n"
      "s_nop 0\n"
      "buffer_load_dwordx4 v5, s[44:47], s54 offen offset:256 nt lds\n"
      "s_add_u32 m0, s23, 104064\n"
      "s_nop 0\n"
      "buffer_load_dwordx4 v5, s[44:47], s53 offen offset:384 nt lds\n"
      "s_waitcnt vmcnt(11)\n"
      "ds_read_b128 v[108:111], v20 offset:2048\n"
      "ds_read_b128 v[112:115], v21 offset:2048\n"
      "v_mfma_f32_16x16x32_bf16 v[36:39], a[0:3], a[104:107], 0\n"
      "v_mfma_f32_16x16x32_bf16 v[48:51], a[4:7], a[104:107], 0\n"
      "v_mfma_f32_16x16x32_bf16 v[60:63], a[8:11], a[104:107], 0\n"
      "v_mfma_f32_16x16x32_bf16 v[72:75], a[12:15], a[104:107], 0\n"
      "s_waitcnt lgkmcnt(1)\n"
      "v_mfma_f32_16x16x32_bf16 v[36:39], a[16:19], a[108:111], v[36:39]\n"
      "v_mfma_f32_16x16x32_bf16 v[48:51], a[20:23], a[108:111], v[48:51]\n"
      "v_lshlrev_b32 v116, 16, v108\n"
      "v_and_b32 v117, 0xffff0000, v108\n"
      "v_lshlrev_b32 v118, 16, v109\n"
      "v_and_b32 v119, 0xffff0000, v109\n"
      "v_lshlrev_b32 v120, 16, v110\n"
      "v_and_b32 v121, 0xffff0000, v110\n"
      "v_lshlrev_b32 v122, 16, v111\n"
      "v_and_b32 v123, 0xffff0000, v111\n"
      "s_waitcnt lgkmcnt(0)\n"
      "v_pk_mul_f32 v[116:117], v[116:117], s[24:25] op_sel_hi:[1,0]\n"
      "v_pk_mul_f32 v[118:119], v[118:119], s[24:25] op_sel_hi:[1,0]\n"
      "v_pk_mul_f32 v[120:121], v[120:121], s[24:25] op_sel_hi:[1,0]\n"
      "v_pk_mul_f32 v[122:123], v[122:123], s[24:25] op_sel_hi:[1,0]\n"
      "v_cvt_pk_bf16_f32 v116, v116, v117\n"
      "v_cvt_pk_bf16_f32 v117, v118, v119\n"
      "v_cvt_pk_bf16_f32 v118, v120, v121\n"
      "v_cvt_pk_bf16_f32 v119, v122, v123\n"
      "v_accvgpr_write_b32 a112, v116\n"
      "v_accvgpr_write_b32 a113, v117\n"
      "v_accvgpr_write_b32 a114, v118\n"
      "v_accvgpr_write_b32 a115, v119\n"
      "v_mfma_f32_16x16x32_bf16 v[60:63], a[24:27], a[108:111], v[60:63]\n"
      "v_mfma_f32_16x16x32_bf16 v[72:75], a[28:31], a[108:111], v[72:75]\n"
      "v_lshlrev_b32 v124, 16, v112\n"
      "v_and_b32 v125, 0xffff0000, v112\n"
      "v_lshlrev_b32 v126, 16, v113\n"
      "v_and_b32 v127, 0xffff0000, v113\n"
      "v_lshlrev_b32 v128, 16, v114\n"
      "v_and_b32 v129, 0xffff0000, v114\n"
      "v_lshlrev_b32 v130, 16, v115\n"
      "v_and_b32 v131, 0xffff0000, v115\n"
      "v_pk_mul_f32 v[124:125], v[124:125], s[24:25] op_sel_hi:[1,0]\n"
      "v_pk_mul_f32 v[126:127], v[126:127], s[24:25] op_sel_hi:[1,0]\n"
      "v_pk_mul_f32 v[128:129], v[128:129], s[24:25] op_sel_hi:[1,0]\n"
      "v_pk_mul_f32 v[130:131], v[130:131], s[24:25] op_sel_hi:[1,0]\n"
      "v_cvt_pk_bf16_f32 v124, v124, v125\n"
      "v_cvt_pk_bf16_f32 v125, v126, v127\n"
      "v_cvt_pk_bf16_f32 v126, v128, v129\n"
      "v_cvt_pk_bf16_f32 v127, v130, v131\n"
      "v_accvgpr_write_b32 a116, v124\n"
      "v_accvgpr_write_b32 a117, v125\n"
      "v_accvgpr_write_b32 a118, v126\n"
      "v_accvgpr_write_b32 a119, v127\n"
      "ds_read_b128 a[0:3], v3 offset:4096\n"
      "ds_read_b128 a[4:7], v3 offset:12288\n"
      "ds_read_b128 a[8:11], v3 offset:20480\n"
      "ds_read_b128 a[12:15], v3 offset:28672\n"
      "ds_read_b128 a[16:19], v4 offset:4096\n"
      "ds_read_b128 a[20:23], v4 offset:12288\n"
      "ds_read_b128 a[24:27], v4 offset:20480\n"
      "ds_read_b128 a[28:31], v4 offset:28672\n"
      "s_add_u32 m0, s23, 105088\n"
      "s_nop 0\n"
      "buffer_load_dwordx4 v5, s[44:47], s54 offen offset:384 nt lds\n"
      "s_add_u32 m0, s23, 131072\n"
      "s_nop 0\n"
      "buffer_load_dwordx4 v5, s[44:47], s55 offen offset:0 nt lds\n"
      "s_waitcnt vmcnt(11)\n"
      "ds_read_b128 v[108:111], v20 offset:4096\n"
      "ds_read_b128 v[112:115], v21 offset:4096\n"
      "v_mfma_f32_16x16x32_bf16 v[36:39], a[32:35], a[112:115], v[36:39]\n"
      "v_mfma_f32_16x16x32_bf16 v[48:51], a[36:39], a[112:115], v[48:51]\n"
      "v_mfma_f32_16x16x32_bf16 v[60:63], a[40:43], a[112:115], v[60:63]\n"
      "v_mfma_f32_16x16x32_bf16 v[72:75], a[44:47], a[112:115], v[72:75]\n"
      "s_waitcnt lgkmcnt(1)\n"
      "v_mfma_f32_16x16x32_bf16 v[36:39], a[48:51], a[116:119], v[36:39]\n"
      "v_mfma_f32_16x16x32_bf16 v[48:51], a[52:55], a[116:119], v[48:51]\n"
      "v_lshlrev_b32 v116, 16, v108\n"
      "v_and_b32 v117, 0xffff0000, v108\n"
      "v_lshlrev_b32 v118, 16, v109\n"
      "v_and_b32 v119, 0xffff0000, v109\n"
      "v_lshlrev_b32 v120, 16, v110\n"
      "v_and_b32 v121, 0xffff0000, v110\n"
      "v_lshlrev_b32 v122, 16, v111\n"
      "v_and_b32 v123, 0xffff0000, v111\n"
      "s_waitcnt lgkmcnt(0)\n"
      "v_pk_mul_f32 v[116:117], v[116:117], s[24:25] op_sel_hi:[1,0]\n"
      "v_pk_mul_f32 v[118:119], v[118:119], s[24:25] op_sel_hi:[1,0]\n"
      "v_pk_mul_f32 v[120:121], v[120:121], s[24:25] op_sel_hi:[1,0]\n"
      "v_pk_mul_f32 v[122:123], v[122:123], s[24:25] op_sel_hi:[1,0]\n"
      "v_cvt_pk_bf16_f32 v116, v116, v117\n"
      "v_cvt_pk_bf16_f32 v117, v118, v119\n"
      "v_cvt_pk_bf16_f32 v118, v120, v121\n"
      "v_cvt_pk_bf16_f32 v119, v122, v123\n"
      "v_accvgpr_write_b32 a120, v116\n"
      "v_accvgpr_write_b32 a121, v117\n"
      "v_accvgpr_write_b32 a122, v118\n"
      "v_accvgpr_write_b32 a123, v119\n"
      "v_mfma_f32_16x16x32_bf16 v[60:63], a[56:59], a[116:119], v[60:63]\n"
      "v_mfma_f32_16x16x32_bf16 v[72:75], a[60:63], a[116:119], v[72:75]\n"
      "v_lshlrev_b32 v124, 16, v112\n"
      "v_and_b32 v125, 0xffff0000, v112\n"
      "v_lshlrev_b32 v126, 16, v113\n"
      "v_and_b32 v127, 0xffff0000, v113\n"
      "v_lshlrev_b32 v128, 16, v114\n"
      "v_and_b32 v129, 0xffff0000, v114\n"
      "v_lshlrev_b32 v130, 16, v115\n"
      "v_and_b32 v131, 0xffff0000, v115\n"
      "v_pk_mul_f32 v[124:125], v[124:125], s[24:25] op_sel_hi:[1,0]\n"
      "v_pk_mul_f32 v[126:127], v[126:127], s[24:25] op_sel_hi:[1,0]\n"
      "v_pk_mul_f32 v[128:129], v[128:129], s[24:25] op_sel_hi:[1,0]\n"
      "v_pk_mul_f32 v[130:131], v[130:131], s[24:25] op_sel_hi:[1,0]\n"
      "v_cvt_pk_bf16_f32 v124, v124, v125\n"
      "v_cvt_pk_bf16_f32 v125, v126, v127\n"
      "v_cvt_pk_bf16_f32 v126, v128, v129\n"
      "v_cvt_pk_bf16_f32 v127, v130, v131\n"
      "v_accvgpr_write_b32 a124, v124\n"
      "v_accvgpr_write_b32 a125, v125\n"
      "v_accvgpr_write_b32 a126, v126\n"
      "v_accvgpr_write_b32 a127, v127\n"
      "ds_read_b128 a[32:35], v3 offset:6144\n"
      "ds_read_b128 a[36:39], v3 offset:14336\n"
      "ds_read_b128 a[40:43], v3 offset:22528\n"
      "ds_read_b128 a[44:47], v3 offset:30720\n"
      "ds_read_b128 a[48:51], v4 offset:6144\n"
      "ds_read_b128 a[52:55], v4 offset:14336\n"
      "ds_read_b128 a[56:59], v4 offset:22528\n"
      "ds_read_b128 a[60:63], v4 offset:30720\n"
      "s_add_u32 m0, s23, 132096\n"
      "s_nop 0\n"
      "buffer_load_dwordx4 v5, s[44:47], s56 offen offset:0 nt lds\n"
      "s_add_u32 m0, s23, 132992\n"
      "s_nop 0\n"
      "buffer_load_dwordx4 v5, s[44:47], s55 offen offset:128 nt lds\n"
      "s_waitcnt vmcnt(11)\n"
      "ds_read_b128 v[108:111], v20 offset:6144\n"
      "ds_read_b128 v[112:115], v21 offset:6144\n"
      "v_mfma_f32_16x16x32_bf16 v[36:39], a[0:3], a[120:123], v[36:39]\n"
      "v_mfma_f32_16x16x32_bf16 v[48:51], a[4:7], a[120:123], v[48:51]\n"
      "v_mfma_f32_16x16x32_bf16 v[60:63], a[8:11], a[120:123], v[60:63]\n"
      "v_mfma_f32_16x16x32_bf16 v[72:75], a[12:15], a[120:123], v[72:75]\n"
      "s_waitcnt lgkmcnt(1)\n"
      "v_mfma_f32_16x16x32_bf16 v[36:39], a[16:19], a[124:127], v[36:39]\n"
      "v_mfma_f32_16x16x32_bf16 v[48:51], a[20:23], a[124:127], v[48:51]\n"
      "v_lshlrev_b32 v116, 16, v108\n"
      "v_and_b32 v117, 0xffff0000, v108\n"
      "v_lshlrev_b32 v118, 16, v109\n"
      "v_and_b32 v119, 0xffff0000, v109\n"
      "v_lshlrev_b32 v120, 16, v110\n"
      "v_and_b32 v121, 0xffff0000, v110\n"
      "v_lshlrev_b32 v122, 16, v111\n"
      "v_and_b32 v123, 0xffff0000, v111\n"
      "s_waitcnt lgkmcnt(0)\n"
      "v_pk_mul_f32 v[116:117], v[116:117], s[24:25] op_sel_hi:[1,0]\n"
      "v_pk_mul_f32 v[118:119], v[118:119], s[24:25] op_sel_hi:[1,0]\n"
      "v_pk_mul_f32 v[120:121], v[120:121], s[24:25] op_sel_hi:[1,0]\n"
      "v_pk_mul_f32 v[122:123], v[122:123], s[24:25] op_sel_hi:[1,0]\n"
      "v_cvt_pk_bf16_f32 v116, v116, v117\n"
      "v_cvt_pk_bf16_f32 v117, v118, v119\n"
      "v_cvt_pk_bf16_f32 v118, v120, v121\n"
      "v_cvt_pk_bf16_f32 v119, v122, v123\n"
      "v_accvgpr_write_b32 a128, v116\n"
      "v_accvgpr_write_b32 a129, v117\n"
      "v_accvgpr_write_b32 a130, v118\n"
      "v_accvgpr_write_b32 a131, v119\n"
      "v_mfma_f32_16x16x32_bf16 v[60:63], a[24:27], a[124:127], v[60:63]\n"
      "v_mfma_f32_16x16x32_bf16 v[72:75], a[28:31], a[124:127], v[72:75]\n"
      "v_lshlrev_b32 v124, 16, v112\n"
      "v_and_b32 v125, 0xffff0000, v112\n"
      "v_lshlrev_b32 v126, 16, v113\n"
      "v_and_b32 v127, 0xffff0000, v113\n"
      "v_lshlrev_b32 v128, 16, v114\n"
      "v_and_b32 v129, 0xffff0000, v114\n"
      "v_lshlrev_b32 v130, 16, v115\n"
      "v_and_b32 v131, 0xffff0000, v115\n"
      "v_pk_mul_f32 v[124:125], v[124:125], s[24:25] op_sel_hi:[1,0]\n"
      "v_pk_mul_f32 v[126:127], v[126:127], s[24:25] op_sel_hi:[1,0]\n"
      "v_pk_mul_f32 v[128:129], v[128:129], s[24:25] op_sel_hi:[1,0]\n"
      "v_pk_mul_f32 v[130:131], v[130:131], s[24:25] op_sel_hi:[1,0]\n"
      "v_cvt_pk_bf16_f32 v124, v124, v125\n"
      "v_cvt_pk_bf16_f32 v125, v126, v127\n"
      "v_cvt_pk_bf16_f32 v126, v128, v129\n"
      "v_cvt_pk_bf16_f32 v127, v130, v131\n"
      "v_accvgpr_write_b32 a132, v124\n"
      "v_accvgpr_write_b32 a133, v125\n"
      "v_accvgpr_write_b32 a134, v126\n"
      "v_accvgpr_write_b32 a135, v127\n"
      "s_nop 1\n"
      "v_mfma_f32_16x16x32_bf16 v[36:39], a[32:35], a[128:131], v[36:39]\n"
      "v_mfma_f32_16x16x32_bf16 v[48:51], a[36:39], a[128:131], v[48:51]\n"
      "v_mfma_f32_16x16x32_bf16 v[60:63], a[40:43], a[128:131], v[60:63]\n"
      "v_mfma_f32_16x16x32_bf16 v[72:75], a[44:47], a[128:131], v[72:75]\n"
      "v_mfma_f32_16x16x32_bf16 v[36:39], a[48:51], a[132:135], v[36:39]\n"
      "v_mfma_f32_16x16x32_bf16 v[48:51], a[52:55], a[132:135], v[48:51]\n"
      "v_mfma_f32_16x16x32_bf16 v[60:63], a[56:59], a[132:135], v[60:63]\n"
      "v_mfma_f32_16x16x32_bf16 v[72:75], a[60:63], a[132:135], v[72:75]\n"
      "s_nop 7\n"
      "v_max3_f32 v22, v7, v36, v37\n"
      "v_max3_f32 v22, v22, v38, v39\n"
      "v_max3_f32 v22, v22, v48, v49\n"
      "v_max3_f32 v22, v22, v50, v51\n"
      "v_max3_f32 v22, v22, v60, v61\n"
      "v_max3_f32 v22, v22, v62, v63\n"
      "v_max3_f32 v22, v22, v72, v73\n"
      "v_max3_f32 v22, v22, v74, v75\n"
      "v_mov_b32 v23, v22\n"
      "s_nop 1\n"
      "v_permlane16_swap_b32 v22, v23\n"
      "v_max_f32 v22, v22, v23\n"
      "v_mov_b32 v23, v22\n"
      "s_nop 1\n"
      "v_permlane32_swap_b32 v22, v23\n"
      "v_max_f32 v22, v22, v23\n"
      "v_add_f32 v22, 35.0, v22\n"
      "v_sub_f32 v36, v36, v22\n"
      "v_sub_f32 v37, v37, v22\n"
      "v_sub_f32 v38, v38, v22\n"
      "v_sub_f32 v39, v39, v22\n"
      "v_sub_f32 v48, v48, v22\n"
      "v_sub_f32 v49, v49, v22\n"
      "v_sub_f32 v50, v50, v22\n"
      "v_sub_f32 v51, v51, v22\n"
      "v_sub_f32 v60, v60, v22\n"
      "v_sub_f32 v61, v61, v22\n"
      "v_sub_f32 v62, v62, v22\n"
      "v_sub_f32 v63, v63, v22\n"
      "v_sub_f32 v72, v72, v22\n"
      "v_sub_f32 v73, v73, v22\n"
      "v_sub_f32 v74, v74, v22\n"
      "v_sub_f32 v75, v75, v22\n"
      "v_exp_f32 v36, v36\n"
      "v_exp_f32 v37, v37\n"
      "v_exp_f32 v38, v38\n"
      "v_exp_f32 v39, v39\n"
      "v_exp_f32 v48, v48\n"
      "v_exp_f32 v49, v49\n"
      "v_exp_f32 v50, v50\n"
      "v_exp_f32 v51, v51\n"
      "v_exp_f32 v60, v60\n"
      "v_exp_f32 v61, v61\n"
      "v_exp_f32 v62, v62\n"
      "v_exp_f32 v63, v63\n"
      "v_exp_f32 v72, v72\n"
      "v_exp_f32 v73, v73\n"
      "v_exp_f32 v74, v74\n"
      "v_exp_f32 v75, v75\n"
      "v_sub_f32 v24, 0, v22\n"
      "v_sub_f32 v25, 0, v22\n"
      "v_sub_f32 v26, 0, v22\n"
      "v_sub_f32 v27, 0, v22\n"
      "v_add_f32 v6, v6, v36\n"
      "v_cvt_pk_bf16_f32 v84, v36, v37\n"
      "v_add_f32 v6, v6, v37\n"
      "v_cvt_pk_bf16_f32 v85, v38, v39\n"
      "v_add_f32 v6, v6, v38\n"
      "v_add_f32 v6, v6, v39\n"
      "v_cvt_pk_bf16_f32 v86, v48, v49\n"
      "v_cvt_pk_bf16_f32 v87, v50, v51\n"
      "v_cvt_pk_bf16_f32 v88, v60, v61\n"
      "v_add_f32 v6, v6, v48\n"
      "v_cvt_pk_bf16_f32 v89, v62, v63\n"
      "v_add_f32 v6, v6, v49\n"
      "v_add_f32 v6, v6, v50\n"
      "v_add_f32 v6, v6, v51\n"
      "v_cvt_pk_bf16_f32 v90, v72, v73\n"
      "v_cvt_pk_bf16_f32 v91, v74, v75\n"
      "s_add_u32 s29, s23, 98304\n"
      "v_add_u32 v20, s29, v3\n"
      "v_add_u32 v21, s29, v4\n"
      "ds_read_b128 a[0:3], v3 offset:0\n"
      "ds_read_b128 a[4:7], v3 offset:8192\n"
      "ds_read_b128 a[8:11], v3 offset:16384\n"
      "ds_read_b128 a[12:15], v3 offset:24576\n"
      "ds_read_b128 a[16:19], v4 offset:0\n"
      "ds_read_b128 a[20:23], v4 offset:8192\n"
      "ds_read_b128 a[24:27], v4 offset:16384\n"
      "ds_read_b128 a[28:31], v4 offset:24576\n"
      "s_add_u32 m0, s23, 134016\n"
      "s_nop 0\n"
      "buffer_load_dwordx4 v5, s[44:47], s56 offen offset:128 nt lds\n"
      "s_add_u32 m0, s23, 134912\n"
      "s_nop 0\n"
      "buffer_load_dwordx4 v5, s[44:47], s55 offen offset:256 nt lds\n"
      "s_waitcnt vmcnt(11)\n"
      "ds_read_b128 v[108:111], v20 offset:0\n"
      "ds_read_b128 v[112:115], v21 offset:0\n"
      "s_waitcnt lgkmcnt(1)\n"
      "v_lshlrev_b32 v116, 16, v108\n"
      "v_and_b32 v117, 0xffff0000, v108\n"
      "v_lshlrev_b32 v118, 16, v109\n"
      "v_and_b32 v119, 0xffff0000, v109\n"
      "v_lshlrev_b32 v120, 16, v110\n"
      "v_and_b32 v121, 0xffff0000, v110\n"
      "v_lshlrev_b32 v122, 16, v111\n"
      "v_and_b32 v123, 0xffff0000, v111\n"
      "s_waitcnt lgkmcnt(0)\n"
      "v_pk_mul_f32 v[116:117], v[116:117], s[24:25] op_sel_hi:[1,0]\n"
      "v_pk_mul_f32 v[118:119], v[118:119], s[24:25] op_sel_hi:[1,0]\n"
      "v_pk_mul_f32 v[120:121], v[120:121], s[24:25] op_sel_hi:[1,0]\n"
      "v_pk_mul_f32 v[122:123], v[122:123], s[24:25] op_sel_hi:[1,0]\n"
      "v_cvt_pk_bf16_f32 v116, v116, v117\n"
      "v_cvt_pk_bf16_f32 v117, v118, v119\n"
      "v_cvt_pk_bf16_f32 v118, v120, v121\n"
      "v_cvt_pk_bf16_f32 v119, v122, v123\n"
      "v_accvgpr_write_b32 a136, v116\n"
      "v_accvgpr_write_b32 a137, v117\n"
      "v_accvgpr_write_b32 a138, v118\n"
      "v_accvgpr_write_b32 a139, v119\n"
      "v_lshlrev_b32 v124, 16, v112\n"
      "v_and_b32 v125, 0xffff0000, v112\n"
      "v_lshlrev_b32 v126, 16, v113\n"
      "v_and_b32 v127, 0xffff0000, v113\n"
      "v_lshlrev_b32 v128, 16, v114\n"
      "v_and_b32 v129, 0xffff0000, v114\n"
      "v_lshlrev_b32 v130, 16, v115\n"
      "v_and_b32 v131, 0xffff0000, v115\n"
      "v_pk_mul_f32 v[124:125], v[124:125], s[24:25] op_sel_hi:[1,0]\n"
      "v_pk_mul_f32 v[126:127], v[126:127], s[24:25] op_sel_hi:[1,0]\n"
      "v_pk_mul_f32 v[128:129], v[128:129], s[24:25] op_sel_hi:[1,0]\n"
      "v_pk_mul_f32 v[130:131], v[130:131], s[24:25] op_sel_hi:[1,0]\n"
      "v_cvt_pk_bf16_f32 v124, v124, v125\n"
      "v_cvt_pk_bf16_f32 v125, v126, v127\n"
      "v_cvt_pk_bf16_f32 v126, v128, v129\n"
      "v_cvt_pk_bf16_f32 v127, v130, v131\n"
      "v_accvgpr_write_b32 a140, v124\n"
      "v_accvgpr_write_b32 a141, v125\n"
      "v_accvgpr_write_b32 a142, v126\n"
      "v_accvgpr_write_b32 a143, v127\n"
      "ds_read_b128 a[32:35], v3 offset:2048\n"
      "ds_read_b128 a[36:39], v3 offset:10240\n"
      "ds_read_b128 a[40:43], v3 offset:18432\n"
      "ds_read_b128 a[44:47], v3 offset:26624\n"
      "ds_read_b128 a[48:51], v4 offset:2048\n"
      "ds_read_b128 a[52:55], v4 offset:10240\n"
      "ds_read_b128 a[56:59], v4 offset:18432\n"
      "ds_read_b128 a[60:63], v4 offset:26624\n"
      "s_add_u32 m0, s23, 135936\n"
      "s_nop 0\n"
      "buffer_load_dwordx4 v5, s[44:47], s56 offen offset:256 nt lds\n"
      "s_add_u32 m0, s23, 136832\n"
      "s_nop 0\n"
      "buffer_load_dwordx4 v5, s[44:47], s55 offen offset:384 nt lds\n"
      "s_waitcnt vmcnt(11)\n"
      "ds_read_b128 v[108:111], v20 offset:2048\n"
      "ds_read_b128 v[112:115], v21 offset:2048\n"
      "v_mfma_f32_16x16x32_bf16 v[40:43], a[0:3], a[136:139], 0\n"
      "v_mfma_f32_16x16x32_bf16 v[52:55], a[4:7], a[136:139], 0\n"
      "v_mfma_f32_16x16x32_bf16 v[64:67], a[8:11], a[136:139], 0\n"
      "v_mfma_f32_16x16x32_bf16 v[76:79], a[12:15], a[136:139], 0\n"
      "s_waitcnt lgkmcnt(1)\n"
      "v_mfma_f32_16x16x32_bf16 v[40:43], a[16:19], a[140:143], v[40:43]\n"
      "v_mfma_f32_16x16x32_bf16 v[52:55], a[20:23], a[140:143], v[52:55]\n"
      "v_lshlrev_b32 v116, 16, v108\n"
      "v_and_b32 v117, 0xffff0000, v108\n"
      "v_lshlrev_b32 v118, 16, v109\n"
      "v_and_b32 v119, 0xffff0000, v109\n"
      "v_lshlrev_b32 v120, 16, v110\n"
      "v_and_b32 v121, 0xffff0000, v110\n"
      "v_lshlrev_b32 v122, 16, v111\n"
      "v_and_b32 v123, 0xffff0000, v111\n"
      "s_waitcnt lgkmcnt(0)\n"
      "v_pk_mul_f32 v[116:117], v[116:117], s[24:25] op_sel_hi:[1,0]\n"
      "v_pk_mul_f32 v[118:119], v[118:119], s[24:25] op_sel_hi:[1,0]\n"
      "v_pk_mul_f32 v[120:121], v[120:121], s[24:25] op_sel_hi:[1,0]\n"
      "v_pk_mul_f32 v[122:123], v[122:123], s[24:25] op_sel_hi:[1,0]\n"
      "v_cvt_pk_bf16_f32 v116, v116, v117\n"
      "v_cvt_pk_bf16_f32 v117, v118, v119\n"
      "v_cvt_pk_bf16_f32 v118, v120, v121\n"
      "v_cvt_pk_bf16_f32 v119, v122, v123\n"
      "v_accvgpr_write_b32 a144, v116\n"
      "v_accvgpr_write_b32 a145, v117\n"
      "v_accvgpr_write_b32 a146, v118\n"
      "v_accvgpr_write_b32 a147, v119\n"
      "v_mfma_f32_16x16x32_bf16 v[64:67], a[24:27], a[140:143], v[64:67]\n"
      "v_mfma_f32_16x16x32_bf16 v[76:79], a[28:31], a[140:143], v[76:79]\n"
      "v_lshlrev_b32 v124, 16, v112\n"
      "v_and_b32 v125, 0xffff0000, v112\n"
      "v_lshlrev_b32 v126, 16, v113\n"
      "v_and_b32 v127, 0xffff0000, v113\n"
      "v_lshlrev_b32 v128, 16, v114\n"
      "v_and_b32 v129, 0xffff0000, v114\n"
      "v_lshlrev_b32 v130, 16, v115\n"
      "v_and_b32 v131, 0xffff0000, v115\n"
      "v_pk_mul_f32 v[124:125], v[124:125], s[24:25] op_sel_hi:[1,0]\n"
      "v_pk_mul_f32 v[126:127], v[126:127], s[24:25] op_sel_hi:[1,0]\n"
      "v_pk_mul_f32 v[128:129], v[128:129], s[24:25] op_sel_hi:[1,0]\n"
      "v_pk_mul_f32 v[130:131], v[130:131], s[24:25] op_sel_hi:[1,0]\n"
      "v_cvt_pk_bf16_f32 v124, v124, v125\n"
      "v_cvt_pk_bf16_f32 v125, v126, v127\n"
      "v_cvt_pk_bf16_f32 v126, v128, v129\n"
      "v_cvt_pk_bf16_f32 v127, v130, v131\n"
      "v_accvgpr_write_b32 a148, v124\n"
      "v_accvgpr_write_b32 a149, v125\n"
      "v_accvgpr_write_b32 a150, v126\n"
      "v_accvgpr_write_b32 a151, v127\n"
      "ds_read_b128 a[0:3], v3 offset:4096\n"
      "ds_read_b128 a[4:7], v3 offset:12288\n"
      "ds_read_b128 a[8:11], v3 offset:20480\n"
      "ds_read_b128 a[12:15], v3 offset:28672\n"
      "ds_read_b128 a[16:19], v4 offset:4096\n"
      "ds_read_b128 a[20:23], v4 offset:12288\n"
      "ds_read_b128 a[24:27], v4 offset:20480\n"
      "ds_read_b128 a[28:31], v4 offset:28672\n"
      "s_add_u32 m0, s23, 137856\n"
      "s_nop 0\n"
      "buffer_load_dwordx4 v5, s[44:47], s56 offen offset:384 nt lds\n"
      "s_add_u32 m0, s23, 65536\n"
      "s_nop 0\n"
      "buffer_load_dwordx4 v2, s[40:43], 0 offen offset:0 nt lds\n"
      "s_waitcnt vmcnt(11)\n"
      "ds_read_b128 v[108:111], v20 offset:4096\n"
      "ds_read_b128 v[112:115], v21 offset:4096\n"
      "v_mfma_f32_16x16x32_bf16 v[40:43], a[32:35], a[144:147], v[40:43]\n"
      "v_mfma_f32_16x16x32_bf16 v[52:55], a[36:39], a[144:147], v[52:55]\n"
      "v_mfma_f32_16x16x32_bf16 v[64:67], a[40:43], a[144:147], v[64:67]\n"
      "v_mfma_f32_16x16x32_bf16 v[76:79], a[44:47], a[144:147], v[76:79]\n"
      "s_waitcnt lgkmcnt(1)\n"
      "v_mfma_f32_16x16x32_bf16 v[40:43], a[48:51], a[148:151], v[40:43]\n"
      "v_mfma_f32_16x16x32_bf16 v[52:55], a[52:55], a[148:151], v[52:55]\n"
      "v_lshlrev_b32 v116, 16, v108\n"
      "v_and_b32 v117, 0xffff0000, v108\n"
      "v_lshlrev_b32 v118, 16, v109\n"
      "v_and_b32 v119, 0xffff0000, v109\n"
      "v_lshlrev_b32 v120, 16, v110\n"
      "v_and_b32 v121, 0xffff0000, v110\n"
      "v_lshlrev_b32 v122, 16, v111\n"
      "v_and_b32 v123, 0xffff0000, v111\n"
      "s_waitcnt lgkmcnt(0)\n"
      "v_pk_mul_f32 v[116:117], v[116:117], s[24:25] op_sel_hi:[1,0]\n"
      "v_pk_mul_f32 v[118:119], v[118:119], s[24:25] op_sel_hi:[1,0]\n"
      "v_pk_mul_f32 v[120:121], v[120:121], s[24:25] op_sel_hi:[1,0]\n"
      "v_pk_mul_f32 v[122:123], v[122:123], s[24:25] op_sel_hi:[1,0]\n"
      "v_cvt_pk_bf16_f32 v116, v116, v117\n"
      "v_cvt_pk_bf16_f32 v117, v118, v119\n"
      "v_cvt_pk_bf16_f32 v118, v120, v121\n"
      "v_cvt_pk_bf16_f32 v119, v122, v123\n"
      "v_accvgpr_write_b32 a152, v116\n"
      "v_accvgpr_write_b32 a153, v117\n"
      "v_accvgpr_write_b32 a154, v118\n"
      "v_accvgpr_write_b32 a155, v119\n"
      "v_mfma_f32_16x16x32_bf16 v[64:67], a[56:59], a[148:151], v[64:67]\n"
      "v_mfma_f32_16x16x32_bf16 v[76:79], a[60:63], a[148:151], v[76:79]\n"
      "v_lshlrev_b32 v124, 16, v112\n"
      "v_and_b32 v125, 0xffff0000, v112\n"
      "v_lshlrev_b32 v126, 16, v113\n"
      "v_and_b32 v127, 0xffff0000, v113\n"
      "v_lshlrev_b32 v128, 16, v114\n"
      "v_and_b32 v129, 0xffff0000, v114\n"
      "v_lshlrev_b32 v130, 16, v115\n"
      "v_and_b32 v131, 0xffff0000, v115\n"
      "v_pk_mul_f32 v[124:125], v[124:125], s[24:25] op_sel_hi:[1,0]\n"
      "v_pk_mul_f32 v[126:127], v[126:127], s[24:25] op_sel_hi:[1,0]\n"
      "v_pk_mul_f32 v[128:129], v[128:129], s[24:25] op_sel_hi:[1,0]\n"
      "v_pk_mul_f32 v[130:131], v[130:131], s[24:25] op_sel_hi:[1,0]\n"
      "v_cvt_pk_bf16_f32 v124, v124, v125\n"
      "v_cvt_pk_bf16_f32 v125, v126, v127\n"
      "v_cvt_pk_bf16_f32 v126, v128, v129\n"
      "v_cvt_pk_bf16_f32 v127, v130, v131\n"
      "v_accvgpr_write_b32 a156, v124\n"
      "v_accvgpr_write_b32 a157, v125\n"
      "v_accvgpr_write_b32 a158, v126\n"
      "v_accvgpr_write_b32 a159, v127\n"
      "ds_read_b128 a[32:35], v3 offset:6144\n"
      "ds_read_b128 a[36:39], v3 offset:14336\n"
      "ds_read_b128 a[40:43], v3 offset:22528\n"
      "ds_read_b128 a[44:47], v3 offset:30720\n"
      "ds_read_b128 a[48:51], v4 offset:6144\n"
      "ds_read_b128 a[52:55], v4 offset:14336\n"
      "ds_read_b128 a[56:59], v4 offset:22528\n"
      "ds_read_b128 a[60:63], v4 offset:30720\n"
      "s_add_u32 m0, s23, 66560\n"
      "s_nop 0\n"
      "buffer_load_dwordx4 v2, s[40:43], s52 offen offset:0 nt lds\n"
      "s_add_u32 m0, s23, 67456\n"
      "s_nop 0\n"
      "buffer_load_dwordx4 v2, s[40:43], 0 offen offset:128 nt lds\n"
      "s_waitcnt vmcnt(11)\n"
      "ds_read_b128 v[108:111], v20 offset:6144\n"
      "ds_read_b128 v[112:115], v21 offset:6144\n"
      "v_mfma_f32_16x16x32_bf16 v[40:43], a[0:3], a[152:155], v[40:43]\n"
      "v_mfma_f32_16x16x32_bf16 v[52:55], a[4:7], a[152:155], v[52:55]\n"
      "v_mfma_f32_16x16x32_bf16 v[64:67], a[8:11], a[152:155], v[64:67]\n"
      "v_mfma_f32_16x16x32_bf16 v[76:79], a[12:15], a[152:155], v[76:79]\n"
      "s_waitcnt lgkmcnt(1)\n"
      "v_mfma_f32_16x16x32_bf16 v[40:43], a[16:19], a[156:159], v[40:43]\n"
      "v_mfma_f32_16x16x32_bf16 v[52:55], a[20:23], a[156:159], v[52:55]\n"
      "v_lshlrev_b32 v116, 16, v108\n"
      "v_and_b32 v117, 0xffff0000, v108\n"
      "v_lshlrev_b32 v118, 16, v109\n"
      "v_and_b32 v119, 0xffff0000, v109\n"
      "v_lshlrev_b32 v120, 16, v110\n"
      "v_and_b32 v121, 0xffff0000, v110\n"
      "v_lshlrev_b32 v122, 16, v111\n"
      "v_and_b32 v123, 0xffff0000, v111\n"
      "s_waitcnt lgkmcnt(0)\n"
      "v_pk_mul_f32 v[116:117], v[116:117], s[24:25] op_sel_hi:[1,0]\n"
      "v_pk_mul_f32 v[118:119], v[118:119], s[24:25] op_sel_hi:[1,0]\n"
      "v_pk_mul_f32 v[120:121], v[120:121], s[24:25] op_sel_hi:[1,0]\n"
      "v_pk_mul_f32 v[122:123], v[122:123], s[24:25] op_sel_hi:[1,0]\n"
      "v_cvt_pk_bf16_f32 v116, v116, v117\n"
      "v_cvt_pk_bf16_f32 v117, v118, v119\n"
      "v_cvt_pk_bf16_f32 v118, v120, v121\n"
      "v_cvt_pk_bf16_f32 v119, v122, v123\n"
      "v_accvgpr_write_b32 a160, v116\n"
      "v_accvgpr_write_b32 a161, v117\n"
      "v_accvgpr_write_b32 a162, v118\n"
      "v_accvgpr_write_b32 a163, v119\n"
      "v_mfma_f32_16x16x32_bf16 v[64:67], a[24:27], a[156:159], v[64:67]\n"
      "v_mfma_f32_16x16x32_bf16 v[76:79], a[28:31], a[156:159], v[76:79]\n"
      "v_lshlrev_b32 v124, 16, v112\n"
      "v_and_b32 v125, 0xffff0000, v112\n"
      "v_lshlrev_b32 v126, 16, v113\n"
      "v_and_b32 v127, 0xffff0000, v113\n"
      "v_lshlrev_b32 v128, 16, v114\n"
      "v_and_b32 v129, 0xffff0000, v114\n"
      "v_lshlrev_b32 v130, 16, v115\n"
      "v_and_b32 v131, 0xffff0000, v115\n"
      "v_pk_mul_f32 v[124:125], v[124:125], s[24:25] op_sel_hi:[1,0]\n"
      "v_pk_mul_f32 v[126:127], v[126:127], s[24:25] op_sel_hi:[1,0]\n"
      "v_pk_mul_f32 v[128:129], v[128:129], s[24:25] op_sel_hi:[1,0]\n"
      "v_pk_mul_f32 v[130:131], v[130:131], s[24:25] op_sel_hi:[1,0]\n"
      "v_cvt_pk_bf16_f32 v124, v124, v125\n"
      "v_cvt_pk_bf16_f32 v125, v126, v127\n"
      "v_cvt_pk_bf16_f32 v126, v128, v129\n"
      "v_cvt_pk_bf16_f32 v127, v130, v131\n"
      "v_accvgpr_write_b32 a164, v124\n"
      "v_accvgpr_write_b32 a165, v125\n"
      "v_accvgpr_write_b32 a166, v126\n"
      "v_accvgpr_write_b32 a167, v127\n"
      "s_nop 1\n"
      "v_mfma_f32_16x16x32_bf16 v[40:43], a[32:35], a[160:163], v[40:43]\n"
      "v_mfma_f32_16x16x32_bf16 v[52:55], a[36:39], a[160:163], v[52:55]\n"
      "v_mfma_f32_16x16x32_bf16 v[64:67], a[40:43], a[160:163], v[64:67]\n"
      "v_mfma_f32_16x16x32_bf16 v[76:79], a[44:47], a[160:163], v[76:79]\n"
      "v_mfma_f32_16x16x32_bf16 v[40:43], a[48:51], a[164:167], v[40:43]\n"
      "v_mfma_f32_16x16x32_bf16 v[52:55], a[52:55], a[164:167], v[52:55]\n"
      "v_mfma_f32_16x16x32_bf16 v[64:67], a[56:59], a[164:167], v[64:67]\n"
      "v_mfma_f32_16x16x32_bf16 v[76:79], a[60:63], a[164:167], v[76:79]\n"
      "s_nop 7\n"
      "v_max3_f32 v22, v9, v40, v41\n"
      "v_max3_f32 v22, v22, v42, v43\n"
      "v_max3_f32 v22, v22, v52, v53\n"
      "v_max3_f32 v22, v22, v54, v55\n"
      "v_max3_f32 v22, v22, v64, v65\n"
      "v_max3_f32 v22, v22, v66, v67\n"
      "v_max3_f32 v22, v22, v76, v77\n"
      "v_max3_f32 v22, v22, v78, v79\n"
      "v_mov_b32 v23, v22\n"
      "s_nop 1\n"
      "v_permlane16_swap_b32 v22, v23\n"
      "v_max_f32 v22, v22, v23\n"
      "v_mov_b32 v23, v22\n"
      "s_nop 1\n"
      "v_permlane32_swap_b32 v22, v23\n"
      "v_max_f32 v22, v22, v23\n"
      "v_add_f32 v22, 35.0, v22\n"
      "v_sub_f32 v40, v40, v22\n"
      "v_sub_f32 v41, v41, v22\n"
      "v_sub_f32 v42, v42, v22\n"
      "v_sub_f32 v43, v43, v22\n"
      "v_sub_f32 v52, v52, v22\n"
      "v_sub_f32 v53, v53, v22\n"
      "v_sub_f32 v54, v54, v22\n"
      "v_sub_f32 v55, v55, v22\n"
      "v_sub_f32 v64, v64, v22\n"
      "v_sub_f32 v65, v65, v22\n"
      "v_sub_f32 v66, v66, v22\n"
      "v_sub_f32 v67, v67, v22\n"
      "v_sub_f32 v76, v76, v22\n"
      "v_sub_f32 v77, v77, v22\n"
      "v_sub_f32 v78, v78, v22\n"
      "v_sub_f32 v79, v79, v22\n"
      "v_exp_f32 v40, v40\n"
      "v_exp_f32 v41, v41\n"
      "v_exp_f32 v42, v42\n"
      "v_exp_f32 v43, v43\n"
      "v_exp_f32 v52, v52\n"
      "v_exp_f32 v53, v53\n"
      "v_exp_f32 v54, v54\n"
      "v_exp_f32 v55, v55\n"
      "v_exp_f32 v64, v64\n"
      "v_exp_f32 v65, v65\n"
      "v_exp_f32 v66, v66\n"
      "v_exp_f32 v67, v67\n"
      "v_exp_f32 v76, v76\n"
      "v_exp_f32 v77, v77\n"
      "v_exp_f32 v78, v78\n"
      "v_sub_f32 v28, 0, v22\n"
      "v_sub_f32 v29, 0, v22\n"
      "v_sub_f32 v30, 0, v22\n"
      "v_sub_f32 v31, 0, v22\n"
      "v_cvt_pk_bf16_f32 v92, v40, v41\n"
      "v_cvt_pk_bf16_f32 v93, v42, v43\n"
      "v_add_f32 v8, v8, v40\n"
      "v_add_f32 v8, v8, v41\n"
      "v_add_f32 v8, v8, v42\n"
      "v_add_f32 v8, v8, v43\n"
      "v_cvt_pk_bf16_f32 v94, v52, v53\n"
      "v_cvt_pk_bf16_f32 v95, v54, v55\n"
      "v_cvt_pk_bf16_f32 v96, v64, v65\n"
      "v_cvt_pk_bf16_f32 v97, v66, v67\n"
      "v_add_f32 v8, v8, v52\n"
      "v_add_f32 v8, v8, v53\n"
      "v_add_f32 v8, v8, v54\n"
      "v_add_f32 v8, v8, v55\n"
      "v_cvt_pk_bf16_f32 v98, v76, v77\n"
      "s_add_u32 s29, s23, 131072\n"
      "v_add_u32 v20, s29, v3\n"
      "v_add_u32 v21, s29, v4\n"
      "ds_read_b128 a[0:3], v3 offset:0\n"
      "ds_read_b128 a[4:7], v3 offset:8192\n"
      "ds_read_b128 a[8:11], v3 offset:16384\n"
      "ds_read_b128 a[12:15], v3 offset:24576\n"
      "ds_read_b128 a[16:19], v4 offset:0\n"
      "ds_read_b128 a[20:23], v4 offset:8192\n"
      "ds_read_b128 a[24:27], v4 offset:16384\n"
      "ds_read_b128 a[28:31], v4 offset:24576\n"
      "s_add_u32 m0, s23, 68480\n"
      "s_nop 0\n"
      "buffer_load_dwordx4 v2, s[40:43], s52 offen offset:128 nt lds\n"
      "s_add_u32 m0, s23, 69376\n"
      "s_nop 0\n"
      "buffer_load_dwordx4 v2, s[40:43], 0 offen offset:256 nt lds\n"
      "s_waitcnt vmcnt(11)\n"
      "ds_read_b128 v[108:111], v20 offset:0\n"
      "ds_read_b128 v[112:115], v21 offset:0\n"
      "s_waitcnt lgkmcnt(1)\n"
      "v_lshlrev_b32 v116, 16, v108\n"
      "v_and_b32 v117, 0xffff0000, v108\n"
      "v_lshlrev_b32 v118, 16, v109\n"
      "v_and_b32 v119, 0xffff0000, v109\n"
      "v_lshlrev_b32 v120, 16, v110\n"
      "v_and_b32 v121, 0xffff0000, v110\n"
      "v_lshlrev_b32 v122, 16, v111\n"
      "v_and_b32 v123, 0xffff0000, v111\n"
      "s_waitcnt lgkmcnt(0)\n"
      "v_pk_mul_f32 v[116:117], v[116:117], s[24:25] op_sel_hi:[1,0]\n"
      "v_pk_mul_f32 v[118:119], v[118:119], s[24:25] op_sel_hi:[1,0]\n"
      "v_pk_mul_f32 v[120:121], v[120:121], s[24:25] op_sel_hi:[1,0]\n"
      "v_pk_mul_f32 v[122:123], v[122:123], s[24:25] op_sel_hi:[1,0]\n"
      "v_cvt_pk_bf16_f32 v116, v116, v117\n"
      "v_cvt_pk_bf16_f32 v117, v118, v119\n"
      "v_cvt_pk_bf16_f32 v118, v120, v121\n"
      "v_cvt_pk_bf16_f32 v119, v122, v123\n"
      "v_accvgpr_write_b32 a168, v116\n"
      "v_accvgpr_write_b32 a169, v117\n"
      "v_accvgpr_write_b32 a170, v118\n"
      "v_accvgpr_write_b32 a171, v119\n"
      "v_lshlrev_b32 v124, 16, v112\n"
      "v_and_b32 v125, 0xffff0000, v112\n"
      "v_lshlrev_b32 v126, 16, v113\n"
      "v_and_b32 v127, 0xffff0000, v113\n"
      "v_lshlrev_b32 v128, 16, v114\n"
      "v_and_b32 v129, 0xffff0000, v114\n"
      "v_lshlrev_b32 v130, 16, v115\n"
      "v_and_b32 v131, 0xffff0000, v115\n"
      "v_pk_mul_f32 v[124:125], v[124:125], s[24:25] op_sel_hi:[1,0]\n"
      "v_pk_mul_f32 v[126:127], v[126:127], s[24:25] op_sel_hi:[1,0]\n"
      "v_pk_mul_f32 v[128:129], v[128:129], s[24:25] op_sel_hi:[1,0]\n"
      "v_pk_mul_f32 v[130:131], v[130:131], s[24:25] op_sel_hi:[1,0]\n"
      "v_cvt_pk_bf16_f32 v124, v124, v125\n"
      "v_cvt_pk_bf16_f32 v125, v126, v127\n"
      "v_cvt_pk_bf16_f32 v126, v128, v129\n"
      "v_cvt_pk_bf16_f32 v127, v130, v131\n"
      "v_accvgpr_write_b32 a172, v124\n"
      "v_accvgpr_write_b32 a173, v125\n"
      "v_accvgpr_write_b32 a174, v126\n"
      "v_accvgpr_write_b32 a175, v127\n"
      "ds_read_b128 a[32:35], v3 offset:2048\n"
      "ds_read_b128 a[36:39], v3 offset:10240\n"
      "ds_read_b128 a[40:43], v3 offset:18432\n"
      "ds_read_b128 a[44:47], v3 offset:26624\n"
      "ds_read_b128 a[48:51], v4 offset:2048\n"
      "ds_read_b128 a[52:55], v4 offset:10240\n"
      "ds_read_b128 a[56:59], v4 offset:18432\n"
      "ds_read_b128 a[60:63], v4 offset:26624\n"
      "s_add_u32 m0, s23, 70400\n"
      "s_nop 0\n"
      "buffer_load_dwordx4 v2, s[40:43], s52 offen offset:256 nt lds\n"
      "s_add_u32 m0, s23, 71296\n"
      "s_nop 0\n"
      "buffer_load_dwordx4 v2, s[40:43], 0 offen offset:384 nt lds\n"
      "s_waitcnt vmcnt(11)\n"
      "ds_read_b128 v[108:111], v20 offset:2048\n"
      "ds_read_b128 v[112:115], v21 offset:2048\n"
      "v_mfma_f32_16x16x32_bf16 v[44:47], a[0:3], a[168:171], 0\n"
      "v_mfma_f32_16x16x32_bf16 v[56:59], a[4:7], a[168:171], 0\n"
      "v_mfma_f32_16x16x32_bf16 v[68:71], a[8:11], a[168:171], 0\n"
      "v_mfma_f32_16x16x32_bf16 v[80:83], a[12:15], a[168:171], 0\n"
      "s_waitcnt lgkmcnt(1)\n"
      "v_mfma_f32_16x16x32_bf16 v[44:47], a[16:19], a[172:175], v[44:47]\n"
      "v_mfma_f32_16x16x32_bf16 v[56:59], a[20:23], a[172:175], v[56:59]\n"
      "v_lshlrev_b32 v116, 16, v108\n"
      "v_and_b32 v117, 0xffff0000, v108\n"
      "v_lshlrev_b32 v118, 16, v109\n"
      "v_and_b32 v119, 0xffff0000, v109\n"
      "v_lshlrev_b32 v120, 16, v110\n"
      "v_and_b32 v121, 0xffff0000, v110\n"
      "v_lshlrev_b32 v122, 16, v111\n"
      "v_and_b32 v123, 0xffff0000, v111\n"
      "s_waitcnt lgkmcnt(0)\n"
      "v_pk_mul_f32 v[116:117], v[116:117], s[24:25] op_sel_hi:[1,0]\n"
      "v_pk_mul_f32 v[118:119], v[118:119], s[24:25] op_sel_hi:[1,0]\n"
      "v_pk_mul_f32 v[120:121], v[120:121], s[24:25] op_sel_hi:[1,0]\n"
      "v_pk_mul_f32 v[122:123], v[122:123], s[24:25] op_sel_hi:[1,0]\n"
      "v_cvt_pk_bf16_f32 v116, v116, v117\n"
      "v_cvt_pk_bf16_f32 v117, v118, v119\n"
      "v_cvt_pk_bf16_f32 v118, v120, v121\n"
      "v_cvt_pk_bf16_f32 v119, v122, v123\n"
      "v_accvgpr_write_b32 a176, v116\n"
      "v_accvgpr_write_b32 a177, v117\n"
      "v_accvgpr_write_b32 a178, v118\n"
      "v_accvgpr_write_b32 a179, v119\n"
      "v_mfma_f32_16x16x32_bf16 v[68:71], a[24:27], a[172:175], v[68:71]\n"
      "v_mfma_f32_16x16x32_bf16 v[80:83], a[28:31], a[172:175], v[80:83]\n"
      "v_lshlrev_b32 v124, 16, v112\n"
      "v_and_b32 v125, 0xffff0000, v112\n"
      "v_lshlrev_b32 v126, 16, v113\n"
      "v_and_b32 v127, 0xffff0000, v113\n"
      "v_lshlrev_b32 v128, 16, v114\n"
      "v_and_b32 v129, 0xffff0000, v114\n"
      "v_lshlrev_b32 v130, 16, v115\n"
      "v_and_b32 v131, 0xffff0000, v115\n"
      "v_pk_mul_f32 v[124:125], v[124:125], s[24:25] op_sel_hi:[1,0]\n"
      "v_pk_mul_f32 v[126:127], v[126:127], s[24:25] op_sel_hi:[1,0]\n"
      "v_pk_mul_f32 v[128:129], v[128:129], s[24:25] op_sel_hi:[1,0]\n"
      "v_pk_mul_f32 v[130:131], v[130:131], s[24:25] op_sel_hi:[1,0]\n"
      "v_cvt_pk_bf16_f32 v124, v124, v125\n"
      "v_cvt_pk_bf16_f32 v125, v126, v127\n"
      "v_cvt_pk_bf16_f32 v126, v128, v129\n"
      "v_cvt_pk_bf16_f32 v127, v130, v131\n"
      "v_accvgpr_write_b32 a180, v124\n"
      "v_accvgpr_write_b32 a181, v125\n"
      "v_accvgpr_write_b32 a182, v126\n"
      "v_accvgpr_write_b32 a183, v127\n"
      "ds_read_b128 a[0:3], v3 offset:4096\n"
      "ds_read_b128 a[4:7], v3 offset:12288\n"
      "ds_read_b128 a[8:11], v3 offset:20480\n"
      "ds_read_b128 a[12:15], v3 offset:28672\n"
      "ds_read_b128 a[16:19], v4 offset:4096\n"
      "ds_read_b128 a[20:23], v4 offset:12288\n"
      "ds_read_b128 a[24:27], v4 offset:20480\n"
      "ds_read_b128 a[28:31], v4 offset:28672\n"
      "s_add_u32 m0, s23, 72320\n"
      "s_nop 0\n"
      "buffer_load_dwordx4 v2, s[40:43], s52 offen offset:384 nt lds\n"
      "v_add_u32 v2, 0x8000, v2\n"
      "s_add_u32 m0, s23, 32768\n"
      "s_nop 0\n"
      "buffer_load_dwordx4 v1, s[36:39], 0 offen offset:0 nt lds\n"
      "s_waitcnt vmcnt(11)\n"
      "ds_read_b128 v[108:111], v20 offset:4096\n"
      "ds_read_b128 v[112:115], v21 offset:4096\n"
      "v_mfma_f32_16x16x32_bf16 v[44:47], a[32:35], a[176:179], v[44:47]\n"
      "v_mfma_f32_16x16x32_bf16 v[56:59], a[36:39], a[176:179], v[56:59]\n"
      "v_mfma_f32_16x16x32_bf16 v[68:71], a[40:43], a[176:179], v[68:71]\n"
      "v_mfma_f32_16x16x32_bf16 v[80:83], a[44:47], a[176:179], v[80:83]\n"
      "s_waitcnt lgkmcnt(1)\n"
      "v_mfma_f32_16x16x32_bf16 v[44:47], a[48:51], a[180:183], v[44:47]\n"
      "v_mfma_f32_16x16x32_bf16 v[56:59], a[52:55], a[180:183], v[56:59]\n"
      "v_lshlrev_b32 v116, 16, v108\n"
      "v_and_b32 v117, 0xffff0000, v108\n"
      "v_lshlrev_b32 v118, 16, v109\n"
      "v_and_b32 v119, 0xffff0000, v109\n"
      "v_lshlrev_b32 v120, 16, v110\n"
      "v_and_b32 v121, 0xffff0000, v110\n"
      "v_lshlrev_b32 v122, 16, v111\n"
      "v_and_b32 v123, 0xffff0000, v111\n"
      "s_waitcnt lgkmcnt(0)\n"
      "v_pk_mul_f32 v[116:117], v[116:117], s[24:25] op_sel_hi:[1,0]\n"
      "v_pk_mul_f32 v[118:119], v[118:119], s[24:25] op_sel_hi:[1,0]\n"
      "v_pk_mul_f32 v[120:121], v[120:121], s[24:25] op_sel_hi:[1,0]\n"
      "v_pk_mul_f32 v[122:123], v[122:123], s[24:25] op_sel_hi:[1,0]\n"
      "v_cvt_pk_bf16_f32 v116, v116, v117\n"
      "v_cvt_pk_bf16_f32 v117, v118, v119\n"
      "v_cvt_pk_bf16_f32 v118, v120, v121\n"
      "v_cvt_pk_bf16_f32 v119, v122, v123\n"
      "v_accvgpr_write_b32 a184, v116\n"
      "v_accvgpr_write_b32 a185, v117\n"
      "v_accvgpr_write_b32 a186, v118\n"
      "v_accvgpr_write_b32 a187, v119\n"
      "v_mfma_f32_16x16x32_bf16 v[68:71], a[56:59], a[180:183], v[68:71]\n"
      "v_mfma_f32_16x16x32_bf16 v[80:83], a[60:63], a[180:183], v[80:83]\n"
      "v_lshlrev_b32 v124, 16, v112\n"
      "v_and_b32 v125, 0xffff0000, v112\n"
      "v_lshlrev_b32 v126, 16, v113\n"
      "v_and_b32 v127, 0xffff0000, v113\n"
      "v_lshlrev_b32 v128, 16, v114\n"
      "v_and_b32 v129, 0xffff0000, v114\n"
      "v_lshlrev_b32 v130, 16, v115\n"
      "v_and_b32 v131, 0xffff0000, v115\n"
      "v_pk_mul_f32 v[124:125], v[124:125], s[24:25] op_sel_hi:[1,0]\n"
      "v_pk_mul_f32 v[126:127], v[126:127], s[24:25] op_sel_hi:[1,0]\n"
      "v_pk_mul_f32 v[128:129], v[128:129], s[24:25] op_sel_hi:[1,0]\n"
      "v_pk_mul_f32 v[130:131], v[130:131], s[24:25] op_sel_hi:[1,0]\n"
      "v_cvt_pk_bf16_f32 v124, v124, v125\n"
      "v_cvt_pk_bf16_f32 v125, v126, v127\n"
      "v_cvt_pk_bf16_f32 v126, v128, v129\n"
      "v_cvt_pk_bf16_f32 v127, v130, v131\n"
      "v_accvgpr_write_b32 a188, v124\n"
      "v_accvgpr_write_b32 a189, v125\n"
      "v_accvgpr_write_b32 a190, v126\n"
      "v_accvgpr_write_b32 a191, v127\n"
      "ds_read_b128 a[32:35], v3 offset:6144\n"
      "ds_read_b128 a[36:39], v3 offset:14336\n"
      "ds_read_b128 a[40:43], v3 offset:22528\n"
      "ds_read_b128 a[44:47], v3 offset:30720\n"
      "ds_read_b128 a[48:51], v4 offset:6144\n"
      "ds_read_b128 a[52:55], v4 offset:14336\n"
      "ds_read_b128 a[56:59], v4 offset:22528\n"
      "ds_read_b128 a[60:63], v4 offset:30720\n"
      "s_add_u32 m0, s23, 33792\n"
      "s_nop 0\n"
      "buffer_load_dwordx4 v1, s[36:39], s52 offen offset:0 nt lds\n"
      "s_add_u32 m0, s23, 34688\n"
      "s_nop 0\n"
      "buffer_load_dwordx4 v1, s[36:39], 0 offen offset:128 nt lds\n"
      "s_waitcnt vmcnt(11)\n"
      "ds_read_b128 v[108:111], v20 offset:6144\n"
      "ds_read_b128 v[112:115], v21 offset:6144\n"
      "v_mfma_f32_16x16x32_bf16 v[44:47], a[0:3], a[184:187], v[44:47]\n"
      "v_mfma_f32_16x16x32_bf16 v[56:59], a[4:7], a[184:187], v[56:59]\n"
      "v_mfma_f32_16x16x32_bf16 v[68:71], a[8:11], a[184:187], v[68:71]\n"
      "v_mfma_f32_16x16x32_bf16 v[80:83], a[12:15], a[184:187], v[80:83]\n"
      "s_waitcnt lgkmcnt(1)\n"
      "v_mfma_f32_16x16x32_bf16 v[44:47], a[16:19], a[188:191], v[44:47]\n"
      "v_mfma_f32_16x16x32_bf16 v[56:59], a[20:23], a[188:191], v[56:59]\n"
      "v_lshlrev_b32 v116, 16, v108\n"
      "v_and_b32 v117, 0xffff0000, v108\n"
      "v_lshlrev_b32 v118, 16, v109\n"
      "v_and_b32 v119, 0xffff0000, v109\n"
      "v_lshlrev_b32 v120, 16, v110\n"
      "v_and_b32 v121, 0xffff0000, v110\n"
      "v_lshlrev_b32 v122, 16, v111\n"
      "v_and_b32 v123, 0xffff0000, v111\n"
      "s_waitcnt lgkmcnt(0)\n"
      "v_pk_mul_f32 v[116:117], v[116:117], s[24:25] op_sel_hi:[1,0]\n"
      "v_pk_mul_f32 v[118:119], v[118:119], s[24:25] op_sel_hi:[1,0]\n"
      "v_pk_mul_f32 v[120:121], v[120:121], s[24:25] op_sel_hi:[1,0]\n"
      "v_pk_mul_f32 v[122:123], v[122:123], s[24:25] op_sel_hi:[1,0]\n"
      "v_cvt_pk_bf16_f32 v116, v116, v117\n"
      "v_cvt_pk_bf16_f32 v117, v118, v119\n"
      "v_cvt_pk_bf16_f32 v118, v120, v121\n"
      "v_cvt_pk_bf16_f32 v119, v122, v123\n"
      "v_accvgpr_write_b32 a192, v116\n"
      "v_accvgpr_write_b32 a193, v117\n"
      "v_accvgpr_write_b32 a194, v118\n"
      "v_accvgpr_write_b32 a195, v119\n"
      "v_mfma_f32_16x16x32_bf16 v[68:71], a[24:27], a[188:191], v[68:71]\n"
      "v_mfma_f32_16x16x32_bf16 v[80:83], a[28:31], a[188:191], v[80:83]\n"
      "v_lshlrev_b32 v124, 16, v112\n"
      "v_and_b32 v125, 0xffff0000, v112\n"
      "v_lshlrev_b32 v126, 16, v113\n"
      "v_and_b32 v127, 0xffff0000, v113\n"
      "v_lshlrev_b32 v128, 16, v114\n"
      "v_and_b32 v129, 0xffff0000, v114\n"
      "v_lshlrev_b32 v130, 16, v115\n"
      "v_and_b32 v131, 0xffff0000, v115\n"
      "v_pk_mul_f32 v[124:125], v[124:125], s[24:25] op_sel_hi:[1,0]\n"
      "v_pk_mul_f32 v[126:127], v[126:127], s[24:25] op_sel_hi:[1,0]\n"
      "v_pk_mul_f32 v[128:129], v[128:129], s[24:25] op_sel_hi:[1,0]\n"
      "v_pk_mul_f32 v[130:131], v[130:131], s[24:25] op_sel_hi:[1,0]\n"
      "v_cvt_pk_bf16_f32 v124, v124, v125\n"
      "v_cvt_pk_bf16_f32 v125, v126, v127\n"
      "v_cvt_pk_bf16_f32 v126, v128, v129\n"
      "v_cvt_pk_bf16_f32 v127, v130, v131\n"
      "v_accvgpr_write_b32 a196, v124\n"
      "v_accvgpr_write_b32 a197, v125\n"
      "v_accvgpr_write_b32 a198, v126\n"
      "v_accvgpr_write_b32 a199, v127\n"
      "s_nop 1\n"
      "v_mfma_f32_16x16x32_bf16 v[44:47], a[32:35], a[192:195], v[44:47]\n"
      "v_mfma_f32_16x16x32_bf16 v[56:59], a[36:39], a[192:195], v[56:59]\n"
      "v_mfma_f32_16x16x32_bf16 v[68:71], a[40:43], a[192:195], v[68:71]\n"
      "v_mfma_f32_16x16x32_bf16 v[80:83], a[44:47], a[192:195], v[80:83]\n"
      "v_mfma_f32_16x16x32_bf16 v[44:47], a[48:51], a[196:199], v[44:47]\n"
      "v_mfma_f32_16x16x32_bf16 v[56:59], a[52:55], a[196:199], v[56:59]\n"
      "v_mfma_f32_16x16x32_bf16 v[68:71], a[56:59], a[196:199], v[68:71]\n"
      "v_mfma_f32_16x16x32_bf16 v[80:83], a[60:63], a[196:199], v[80:83]\n"
      "s_add_u32 m0, s23, 35712\n"
      "s_nop 0\n"
      "buffer_load_dwordx4 v1, s[36:39], s52 offen offset:128 nt lds\n"
      "s_add_u32 m0, s23, 36608\n"
      "s_nop 0\n"
      "buffer_load_dwordx4 v1, s[36:39], 0 offen offset:256 nt lds\n"
      "s_add_u32 m0, s23, 37632\n"
      "s_nop 0\n"
      "buffer_load_dwordx4 v1, s[36:39], s52 offen offset:256 nt lds\n"
      "s_add_u32 m0, s23, 38528\n"
      "s_nop 0\n"
      "buffer_load_dwordx4 v1, s[36:39], 0 offen offset:384 nt lds\n"
      "s_nop 7\n"
      "v_max3_f32 v22, v11, v44, v45\n"
      "v_max3_f32 v22, v22, v46, v47\n"
      "v_max3_f32 v22, v22, v56, v57\n"
      "v_max3_f32 v22, v22, v58, v59\n"
      "v_max3_f32 v22, v22, v68, v69\n"
      "v_max3_f32 v22, v22, v70, v71\n"
      "v_max3_f32 v22, v22, v80, v81\n"
      "v_max3_f32 v22, v22, v82, v83\n"
      "v_mov_b32 v23, v22\n"
      "s_nop 1\n"
      "v_permlane16_swap_b32 v22, v23\n"
      "v_max_f32 v22, v22, v23\n"
      "v_mov_b32 v23, v22\n"
      "s_nop 1\n"
      "v_permlane32_swap_b32 v22, v23\n"
      "v_max_f32 v22, v22, v23\n"
      "v_add_f32 v22, 35.0, v22\n"
      "v_sub_f32 v44, v44, v22\n"
      "v_sub_f32 v45, v45, v22\n"
      "v_sub_f32 v46, v46, v22\n"
      "v_sub_f32 v47, v47, v22\n"
      "v_sub_f32 v56, v56, v22\n"
      "v_sub_f32 v57, v57, v22\n"
      "v_sub_f32 v58, v58, v22\n"
      "v_sub_f32 v59, v59, v22\n"
      "v_sub_f32 v68, v68, v22\n"
      "v_sub_f32 v69, v69, v22\n"
      "v_sub_f32 v70, v70, v22\n"
      "v_sub_f32 v71, v71, v22\n"
      "v_sub_f32 v80, v80, v22\n"
      "v_sub_f32 v81, v81, v22\n"
      "v_sub_f32 v82, v82, v22\n"
      "v_sub_f32 v83, v83, v22\n"
      "v_exp_f32 v44, v44\n"
      "v_exp_f32 v45, v45\n"
      "v_exp_f32 v46, v46\n"
      "v_exp_f32 v47, v47\n"
      "v_exp_f32 v56, v56\n"
      "v_exp_f32 v57, v57\n"
      "v_exp_f32 v58, v58\n"
      "v_exp_f32 v59, v59\n"
      "v_exp_f32 v68, v68\n"
      "v_exp_f32 v69, v69\n"
      "v_exp_f32 v70, v70\n"
      "v_exp_f32 v71, v71\n"
      "v_sub_f32 v32, 0, v22\n"
      "v_sub_f32 v33, 0, v22\n"
      "v_sub_f32 v34, 0, v22\n"
      "v_sub_f32 v35, 0, v22\n"
      "v_cvt_pk_bf16_f32 v100, v44, v45\n"
      "v_cvt_pk_bf16_f32 v101, v46, v47\n"
      "v_add_f32 v10, v10, v44\n"
      "v_cvt_pk_bf16_f32 v102, v56, v57\n"
      "v_cvt_pk_bf16_f32 v103, v58, v59\n"
      "v_add_f32 v10, v10, v45\n"
      "v_add_f32 v10, v10, v46\n"
      "v_add_f32 v10, v10, v47\n"
      "v_cvt_pk_bf16_f32 v104, v68, v69\n"
      "v_cvt_pk_bf16_f32 v105, v70, v71\n"
      "s_add_u32 m0, s23, 39552\n"
      "s_nop 0\n"
      "buffer_load_dwordx4 v1, s[36:39], s52 offen offset:384 nt lds\n"
      "v_add_u32 v1, 0x8000, v1\n"
      "s_add_u32 m0, s23, 98304\n"
      "s_nop 0\n"
      "buffer_load_dwordx4 v2, s[40:43], 0 offen offset:0 nt lds\n"
      "v_mov_b32 v108, 0x00000000\n"
      "v_mov_b32 v109, 0x00000000\n"
      "v_mov_b32 v110, 0x00000000\n"
      "v_mov_b32 v111, 0x00000000\n"
      "v_mov_b32 v112, 0x00000000\n"
      "v_mov_b32 v113, 0x00000000\n"
      "v_mov_b32 v114, 0x00000000\n"
      "v_mov_b32 v115, 0x00000000\n"
      "v_mov_b32 v116, 0x00000000\n"
      "v_mov_b32 v117, 0x00000000\n"
      "v_mov_b32 v118, 0x00000000\n"
      "v_mov_b32 v119, 0x00000000\n"
      "v_mov_b32 v120, 0x00000000\n"
      "v_mov_b32 v121, 0x00000000\n"
      "v_mov_b32 v122, 0x00000000\n"
      "v_mov_b32 v123, 0x00000000\n"
      "v_mov_b32 v124, 0x00000000\n"
      "v_mov_b32 v125, 0x00000000\n"
      "v_mov_b32 v126, 0x00000000\n"
      "v_mov_b32 v127, 0x00000000\n"
      "v_mov_b32 v128, 0x00000000\n"
      "v_mov_b32 v129, 0x00000000\n"
      "v_mov_b32 v130, 0x00000000\n"
      "v_mov_b32 v131, 0x00000000\n"
      "v_and_b32 v5, 7, v0\n"
      "v_bfe_u32 v22, v0, 4, 1\n"
      "v_lshl_add_u32 v5, v22, 3, v5\n"
      "v_bfe_u32 v22, v0, 5, 1\n"
      "v_lshl_add_u32 v5, v22, 6, v5\n"
      "v_bfe_u32 v22, v0, 3, 1\n"
      "v_lshl_add_u32 v5, v5, 1, v22\n"
      "v_lshlrev_b32 v5, 2, v5\n"
      "v_add_u32 v16, s23, v5\n"
      "v_xor_b32 v17, 160, v16\n"
      "v_xor_b32 v18, 320, v16\n"
      "v_xor_b32 v19, 480, v16\n"
      "v_bfe_u32 v5, v0, 2, 2\n"
      "v_bfe_u32 v22, v0, 1, 1\n"
      "v_mul_u32_u24 v22, 20, v22\n"
      "v_add_u32 v5, v5, v22\n"
      "v_bfe_u32 v22, v0, 4, 1\n"
      "v_bfe_u32 v23, v0, 5, 1\n"
      "v_xor_b32 v23, v22, v23\n"
      "v_lshl_add_u32 v5, v23, 3, v5\n"
      "v_lshl_add_u32 v5, v22, 5, v5\n"
      "v_and_b32 v22, 1, v0\n"
      "v_lshl_add_u32 v5, v22, 7, v5\n"
      "v_lshlrev_b32 v5, 3, v5\n"
      "v_add_u32 v20, s23, v5\n"
      "v_bfe_u32 v5, v0, 3, 3\n"
      "v_bfe_u32 v22, v0, 2, 1\n"
      "v_lshlrev_b32 v5, 9, v5\n"
      "v_lshl_add_u32 v5, v22, 6, v5\n"
      "v_and_b32 v22, 1, v0\n"
      "v_lshl_add_u32 v5, v22, 5, v5\n"
      "v_bfe_u32 v22, v0, 1, 1\n"
      "v_lshl_add_u32 v21, v22, 4, v5\n"
      "s_add_u32 m0, s23, 99328\n"
      "s_nop 0\n"
      "buffer_load_dwordx4 v2, s[40:43], s52 offen offset:0 nt lds\n"
      "s_add_u32 m0, s23, 100224\n"
      "s_nop 0\n"
      "buffer_load_dwordx4 v2, s[40:43], 0 offen offset:128 nt lds\n"
      "s_waitcnt vmcnt(11)\n"
      "s_barrier\n"
      "ds_read_b64_tr_b16 a[64:65], v12 offset:0\n"
      "ds_read_b64_tr_b16 a[66:67], v12 offset:8192\n"
      "ds_read_b64_tr_b16 a[68:69], v12 offset:16384\n"
      "ds_read_b64_tr_b16 a[70:71], v12 offset:24576\n"
      "ds_read_b64_tr_b16 a[72:73], v13 offset:0\n"
      "ds_read_b64_tr_b16 a[74:75], v13 offset:8192\n"
      "ds_read_b64_tr_b16 a[76:77], v13 offset:16384\n"
      "ds_read_b64_tr_b16 a[78:79], v13 offset:24576\n"
      "ds_read_b64_tr_b16 a[80:81], v14 offset:0\n"
      "ds_read_b64_tr_b16 a[82:83], v14 offset:8192\n"
      "ds_read_b64_tr_b16 a[84:85], v14 offset:16384\n"
      "ds_read_b64_tr_b16 a[86:87], v14 offset:24576\n"
      "s_lshr_b32 s29, %[bid], 2\n"
      "s_and_b32 s29, s29, 2\n"
      "s_and_b32 s30, s22, 1\n"
      "s_add_u32 s29, s29, s30\n"
      "s_mul_i32 s29, s29, 16384\n"
      "s_add_u32 s29, s29, 983040\n"
      "s_cmp_lt_u32 s22, 2\n"
      "s_cselect_b32 s60, s36, s40\n"
      "s_cselect_b32 s61, s37, s41\n"
      "s_add_u32 s60, s60, s29\n"
      "s_addc_u32 s61, s61, 0\n"
      "s_mov_b32 s62, 16384\n"
      "s_mov_b32 s63, 0x20000\n"
      "v_and_b32 v22, 63, v0\n"
      "v_lshlrev_b32 v22, 4, v22\n"
      "v_subrev_u32 v22, 3072, v22\n"
      "s_add_u32 m0, s23, 101248\n"
      "s_nop 0\n"
      "buffer_load_dwordx4 v2, s[40:43], s52 offen offset:128 nt lds\n"
      "s_add_u32 m0, s23, 102144\n"
      "s_nop 0\n"
      "buffer_load_dwordx4 v2, s[40:43], 0 offen offset:256 nt lds\n"
      "s_add_u32 m0, s23, 103168\n"
      "s_nop 0\n"
      "buffer_load_dwordx4 v2, s[40:43], s52 offen offset:256 nt lds\n"
      "s_add_u32 m0, s23, 104064\n"
      "s_nop 0\n"
      "buffer_load_dwordx4 v2, s[40:43], 0 offen offset:384 nt lds\n"
      "s_add_u32 m0, s23, 105088\n"
      "s_nop 0\n"
      "buffer_load_dwordx4 v2, s[40:43], s52 offen offset:384 nt lds\n"
      "v_add_u32 v2, 0x8000, v2\n"
      "s_waitcnt lgkmcnt(0)\n"
      "s_cmp_eq_u32 s22, 1\n"
      "s_cbranch_scc1 L_loopN1_%=\n"
      "s_cmp_eq_u32 s22, 2\n"
      "s_cbranch_scc1 L_loopN2_%=\n"
      "s_cmp_eq_u32 s22, 3\n"
      "s_cbranch_scc1 L_loopN3_%=\n"
      "s_branch L_loopN0_%=\n"
      ".p2align 6\n"
      "L_topD0_%=:\n"
      "v_mfma_f32_16x16x32_bf16 v[216:219], v[100:103], a[80:83], v[216:219]\n"
      "v_mfma_f32_16x16x32_bf16 v[216:219], v[104:107], a[84:87], v[216:219]\n"
      "s_waitcnt lgkmcnt(8)\n"
      "v_mfma_f32_16x16x32_bf16 v[220:223], v[84:87], a[88:91], v[220:223]\n"
      "s_waitcnt vmcnt(17) lgkmcnt(0)\n"
      "v_mfma_f32_16x16x32_bf16 v[220:223], v[88:91], a[92:95], v[220:223]\n"
      "s_barrier\n"
      "ds_read_b128 a[0:3], v3 offset:0\n"
      "v_mfma_f32_16x16x32_bf16 v[224:227], v[92:95], a[88:91], v[224:227]\n"
      "ds_read_b128 a[4:7], v4 offset:0\n"
      "v_mfma_f32_16x16x32_bf16 v[224:227], v[96:99], a[92:95], v[224:227]\n"
      "ds_read_b128 a[8:11], v3 offset:2048\n"
      "v_mfma_f32_16x16x32_bf16 v[228:231], v[100:103], a[88:91], v[228:231]\n"
      "ds_read_b128 a[12:15], v4 offset:2048\n"
      "v_mfma_f32_16x16x32_bf16 v[228:231], v[104:107], a[92:95], v[228:231]\n"
      "s_add_u32 m0, s23, 98304\n"
      "ds_read_b128 a[16:19], v3 offset:4096\n"
      "v_mfma_f32_16x16x32_bf16 v[232:235], v[84:87], a[96:99], v[232:235]\n"
      "buffer_load_dwordx4 v2, s[40:43], 0 offen offset:0 lds\n"
      "v_mfma_f32_16x16x32_bf16 v[232:235], v[88:91], a[100:103], v[232:235]\n"
      "ds_read_b128 a[20:23], v4 offset:4096\n"
      "v_mfma_f32_16x16x32_bf16 v[236:239], v[92:95], a[96:99], v[236:239]\n"
      "ds_read_b128 a[24:27], v3 offset:6144\n"
      "v_mfma_f32_16x16x32_bf16 v[236:239], v[96:99], a[100:103], v[236:239]\n"
      "ds_read_b128 a[28:31], v4 offset:6144\n"
      "v_mfma_f32_16x16x32_bf16 v[240:243], v[100:103], a[96:99], v[240:243]\n"
      "ds_read_b128 a[32:35], v3 offset:8192\n"
      "v_mfma_f32_16x16x32_bf16 v[240:243], v[104:107], a[100:103], v[240:243]\n"
      "ds_read_b128 a[36:39], v4 offset:8192\n"
      "v_mfma_f32_16x16x32_bf16 v[244:247], v[84:87], a[64:67], v[244:247]\n"
      "ds_read_b128 a[40:43], v3 offset:10240\n"
      "v_mfma_f32_16x16x32_bf16 v[244:247], v[88:91], a[68:71], v[244:247]\n"
      "s_add_u32 m0, s23, 99328\n"
      "ds_read_b128 a[44:47], v4 offset:10240\n"
      "v_mfma_f32_16x16x32_bf16 v[248:251], v[92:95], a[64:67], v[248:251]\n"
      "buffer_load_dwordx4 v2, s[40:43], s52 offen offset:0 lds\n"
      "v_mfma_f32_16x16x32_bf16 v[248:251], v[96:99], a[68:71], v[248:251]\n"
      "ds_read_b128 a[48:51], v3 offset:12288\n"
      "v_mfma_f32_16x16x32_bf16 v[252:255], v[100:103], a[64:67], v[252:255]\n"
      "ds_read_b128 a[52:55], v4 offset:12288\n"
      "v_mfma_f32_16x16x32_bf16 v[252:255], v[104:107], a[68:71], v[252:255]\n"
      "s_waitcnt lgkmcnt(6)\n"
      "ds_read_b128 a[56:59], v3 offset:14336\n"
      "v_mfma_f32_16x16x32_bf16 v[36:39], a[0:3], a[104:107], v[24:27]\n"
      "ds_read_b128 a[60:63], v4 offset:14336\n"
      "v_mfma_f32_16x16x32_bf16 v[36:39], a[4:7], a[108:111], v[36:39]\n"
      "v_mfma_f32_16x16x32_bf16 v[36:39], a[8:11], a[112:115], v[36:39]\n"
      "v_mfma_f32_16x16x32_bf16 v[36:39], a[12:15], a[116:119], v[36:39]\n"
      "v_mfma_f32_16x16x32_bf16 v[36:39], a[16:19], a[120:123], v[36:39]\n"
      "v_mfma_f32_16x16x32_bf16 v[36:39], a[20:23], a[124:127], v[36:39]\n"
      "v_mfma_f32_16x16x32_bf16 v[36:39], a[24:27], a[128:131], v[36:39]\n"
      "v_mfma_f32_16x16x32_bf16 v[36:39], a[28:31], a[132:135], v[36:39]\n"
      "s_add_u32 m0, s23, 100224\n"
      "v_mfma_f32_16x16x32_bf16 v[40:43], a[0:3], a[136:139], v[28:31]\n"
      "buffer_load_dwordx4 v2, s[40:43], 0 offen offset:128 lds\n"
      "v_mfma_f32_16x16x32_bf16 v[40:43], a[4:7], a[140:143], v[40:43]\n"
      "v_mfma_f32_16x16x32_bf16 v[40:43], a[8:11], a[144:147], v[40:43]\n"
      "v_mfma_f32_16x16x32_bf16 v[40:43], a[12:15], a[148:151], v[40:43]\n"
      "v_exp_f32 v36, v36\n"
      "v_mfma_f32_16x16x32_bf16 v[40:43], a[16:19], a[152:155], v[40:43]\n"
      "v_add_f32 v6, v6, v36\n"
      "v_mfma_f32_16x16x32_bf16 v[40:43], a[20:23], a[156:159], v[40:43]\n"
      "v_exp_f32 v37, v37\n"
      "v_mfma_f32_16x16x32_bf16 v[40:43], a[24:27], a[160:163], v[40:43]\n"
      "v_cvt_pk_bf16_f32 v84, v36, v37\n"
      "v_add_f32 v6, v6, v37\n"
      "v_mfma_f32_16x16x32_bf16 v[40:43], a[28:31], a[164:167], v[40:43]\n"
      "s_add_u32 m0, s23, 101248\n"
      "v_exp_f32 v38, v38\n"
      "v_mfma_f32_16x16x32_bf16 v[44:47], a[0:3], a[168:171], v[32:35]\n"
      "buffer_load_dwordx4 v2, s[40:43], s52 offen offset:128 lds\n"
      "v_mfma_f32_16x16x32_bf16 v[44:47], a[4:7], a[172:175], v[44:47]\n"
      "v_exp_f32 v39, v39\n"
      "v_mfma_f32_16x16x32_bf16 v[44:47], a[8:11], a[176:179], v[44:47]\n"
      "ds_read_b128 a[0:3], v3 offset:16384\n"
      "v_cvt_pk_bf16_f32 v85, v38, v39\n"
      "v_mfma_f32_16x16x32_bf16 v[44:47], a[12:15], a[180:183], v[44:47]\n"
      "v_exp_f32 v40, v40\n"
      "v_mfma_f32_16x16x32_bf16 v[44:47], a[16:19], a[184:187], v[44:47]\n"
      "ds_read_b128 a[4:7], v4 offset:16384\n"
      "v_add_f32 v6, v6, v38\n"
      "v_mfma_f32_16x16x32_bf16 v[44:47], a[20:23], a[188:191], v[44:47]\n"
      "v_exp_f32 v41, v41\n"
      "v_mfma_f32_16x16x32_bf16 v[44:47], a[24:27], a[192:195], v[44:47]\n"
      "ds_read_b128 a[8:11], v3 offset:18432\n"
      "v_cvt_pk_bf16_f32 v92, v40, v41\n"
      "v_mfma_f32_16x16x32_bf16 v[44:47], a[28:31], a[196:199], v[44:47]\n"
      "s_waitcnt lgkmcnt(3)\n"
      "v_exp_f32 v42, v42\n"
      "v_mfma_f32_16x16x32_bf16 v[48:51], a[32:35], a[104:107], v[24:27]\n"
      "ds_read_b128 a[12:15], v4 offset:18432\n"
      "v_add_f32 v6, v6, v39\n"
      "v_mfma_f32_16x16x32_bf16 v[48:51], a[36:39], a[108:111], v[48:51]\n"
      "v_exp_f32 v43, v43\n"
      "v_mfma_f32_16x16x32_bf16 v[48:51], a[40:43], a[112:115], v[48:51]\n"
      "ds_read_b128 a[16:19], v3 offset:20480\n"
      "v_cvt_pk_bf16_f32 v93, v42, v43\n"
      "v_mfma_f32_16x16x32_bf16 v[48:51], a[44:47], a[116:119], v[48:51]\n"
      "s_add_u32 m0, s23, 102144\n"
      "v_exp_f32 v44, v44\n"
      "v_mfma_f32_16x16x32_bf16 v[48:51], a[48:51], a[120:123], v[48:51]\n"
      "buffer_load_dwordx4 v2, s[40:43], 0 offen offset:256 lds\n"
      "v_mfma_f32_16x16x32_bf16 v[48:51], a[52:55], a[124:127], v[48:51]\n"
      "v_exp_f32 v45, v45\n"
      "v_mfma_f32_16x16x32_bf16 v[48:51], a[56:59], a[128:131], v[48:51]\n"
      "ds_read_b128 a[20:23], v4 offset:20480\n"
      "v_cvt_pk_bf16_f32 v100, v44, v45\n"
      "v_mfma_f32_16x16x32_bf16 v[48:51], a[60:63], a[132:135], v[48:51]\n"
      "v_exp_f32 v46, v46\n"
      "v_mfma_f32_16x16x32_bf16 v[52:55], a[32:35], a[136:139], v[28:31]\n"
      "ds_read_b128 a[24:27], v3 offset:22528\n"
      "v_add_f32 v8, v8, v40\n"
      "v_mfma_f32_16x16x32_bf16 v[52:55], a[36:39], a[140:143], v[52:55]\n"
      "v_exp_f32 v47, v47\n"
      "v_mfma_f32_16x16x32_bf16 v[52:55], a[40:43], a[144:147], v[52:55]\n"
      "ds_read_b128 a[28:31], v4 offset:22528\n"
      "v_cvt_pk_bf16_f32 v101, v46, v47\n"
      "v_mfma_f32_16x16x32_bf16 v[52:55], a[44:47], a[148:151], v[52:55]\n"
      "s_add_u32 m0, s23, 103168\n"
      "v_exp_f32 v48, v48\n"
      "v_mfma_f32_16x16x32_bf16 v[52:55], a[48:51], a[152:155], v[52:55]\n"
      "buffer_load_dwordx4 v2, s[40:43], s52 offen offset:256 lds\n"
      "v_mfma_f32_16x16x32_bf16 v[52:55], a[52:55], a[156:159], v[52:55]\n"
      "v_exp_f32 v49, v49\n"
      "v_mfma_f32_16x16x32_bf16 v[52:55], a[56:59], a[160:163], v[52:55]\n"
      "v_cvt_pk_bf16_f32 v86, v48, v49\n"
      "v_add_f32 v8, v8, v41\n"
      "v_mfma_f32_16x16x32_bf16 v[52:55], a[60:63], a[164:167], v[52:55]\n"
      "v_exp_f32 v50, v50\n"
      "v_mfma_f32_16x16x32_bf16 v[56:59], a[32:35], a[168:171], v[32:35]\n"
      "ds_read_b128 a[32:35], v3 offset:24576\n"
      "v_add_f32 v8, v8, v42\n"
      "v_mfma_f32_16x16x32_bf16 v[56:59], a[36:39], a[172:175], v[56:59]\n"
      "v_exp_f32 v51, v51\n"
      "v_mfma_f32_16x16x32_bf16 v[56:59], a[40:43], a[176:179], v[56:59]\n"
      "ds_read_b128 a[36:39], v4 offset:24576\n"
      "v_cvt_pk_bf16_f32 v87, v50, v51\n"
      "v_mfma_f32_16x16x32_bf16 v[56:59], a[44:47], a[180:183], v[56:59]\n"
      "v_exp_f32 v52, v52\n"
      "v_mfma_f32_16x16x32_bf16 v[56:59], a[48:51], a[184:187], v[56:59]\n"
      "ds_read_b128 a[40:43], v3 offset:26624\n"
      "v_add_f32 v8, v8, v43\n"
      "v_mfma_f32_16x16x32_bf16 v[56:59], a[52:55], a[188:191], v[56:59]\n"
      "v_exp_f32 v53, v53\n"
      "v_mfma_f32_16x16x32_bf16 v[56:59], a[56:59], a[192:195], v[56:59]\n"
      "s_add_u32 m0, s23, 104064\n"
      "ds_read_b128 a[44:47], v4 offset:26624\n"
      "v_cvt_pk_bf16_f32 v94, v52, v53\n"
      "v_mfma_f32_16x16x32_bf16 v[56:59], a[60:63], a[196:199], v[56:59]\n"
      "s_waitcnt lgkmcnt(4)\n"
      "v_exp_f32 v54, v54\n"
      "v_mfma_f32_16x16x32_bf16 v[60:63], a[0:3], a[104:107], v[24:27]\n"
      "buffer_load_dwordx4 v2, s[40:43], 0 offen offset:384 lds\n"
      "v_mfma_f32_16x16x32_bf16 v[60:63], a[4:7], a[108:111], v[60:63]\n"
      "v_exp_f32 v55, v55\n"
      "v_mfma_f32_16x16x32_bf16 v[60:63], a[8:11], a[112:115], v[60:63]\n"
      "ds_read_b128 a[48:51], v3 offset:28672\n"
      "v_cvt_pk_bf16_f32 v95, v54, v55\n"
      "v_mfma_f32_16x16x32_bf16 v[60:63], a[12:15], a[116:119], v[60:63]\n"
      "v_exp_f32 v56, v56\n"
      "v_mfma_f32_16x16x32_bf16 v[60:63], a[16:19], a[120:123], v[60:63]\n"
      "ds_read_b128 a[52:55], v4 offset:28672\n"
      "v_add_f32 v10, v10, v44\n"
      "v_mfma_f32_16x16x32_bf16 v[60:63], a[20:23], a[124:127], v[60:63]\n"
      "v_exp_f32 v57, v57\n"
      "v_mfma_f32_16x16x32_bf16 v[60:63], a[24:27], a[128:131], v[60:63]\n"
      "ds_read_b128 a[56:59], v3 offset:30720\n"
      "v_cvt_pk_bf16_f32 v102, v56, v57\n"
      "v_mfma_f32_16x16x32_bf16 v[60:63], a[28:31], a[132:135], v[60:63]\n"
      "s_add_u32 m0, s23, 105088\n"
      "v_exp_f32 v58, v58\n"
      "v_mfma_f32_16x16x32_bf16 v[64:67], a[0:3], a[136:139], v[28:31]\n"
      "buffer_load_dwordx4 v2, s[40:43], s52 offen offset:384 lds\n"
      "v_mfma_f32_16x16x32_bf16 v[64:67], a[4:7], a[140:143], v[64:67]\n"
      "v_exp_f32 v59, v59\n"
      "v_mfma_f32_16x16x32_bf16 v[64:67], a[8:11], a[144:147], v[64:67]\n"
      "ds_read_b128 a[60:63], v4 offset:30720\n"
      "v_cvt_pk_bf16_f32 v103, v58, v59\n"
      "v_mfma_f32_16x16x32_bf16 v[64:67], a[12:15], a[148:151], v[64:67]\n"
      "v_exp_f32 v60, v60\n"
      "v_mfma_f32_16x16x32_bf16 v[64:67], a[16:19], a[152:155], v[64:67]\n"
      "v_add_u32 v2, 0x8000, v2\n"
      "v_add_f32 v10, v10, v45\n"
      "v_mfma_f32_16x16x32_bf16 v[64:67], a[20:23], a[156:159], v[64:67]\n"
      "v_exp_f32 v61, v61\n"
      "v_mfma_f32_16x16x32_bf16 v[64:67], a[24:27], a[160:163], v[64:67]\n"
      "v_cvt_pk_bf16_f32 v88, v60, v61\n"
      "v_add_f32 v10, v10, v46\n"
      "v_mfma_f32_16x16x32_bf16 v[64:67], a[28:31], a[164:167], v[64:67]\n"
      "v_exp_f32 v62, v62\n"
      "v_mfma_f32_16x16x32_bf16 v[68:71], a[0:3], a[168:171], v[32:35]\n"
      "v_add_f32 v10, v10, v47\n"
      "v_add_f32 v6, v6, v48\n"
      "v_mfma_f32_16x16x32_bf16 v[68:71], a[4:7], a[172:175], v[68:71]\n"
      "v_exp_f32 v63, v63\n"
      "v_mfma_f32_16x16x32_bf16 v[68:71], a[8:11], a[176:179], v[68:71]\n"
      "s_waitcnt vmcnt(17) lgkmcnt(0)\n"
      "v_cvt_pk_bf16_f32 v89, v62, v63\n"
      "v_add_f32 v6, v6, v49\n"
      "v_mfma_f32_16x16x32_bf16 v[68:71], a[12:15], a[180:183], v[68:71]\n"
      "s_barrier\n"
      "v_exp_f32 v64, v64\n"
      "v_mfma_f32_16x16x32_bf16 v[68:71], a[16:19], a[184:187], v[68:71]\n"
      "ds_read_b64_tr_b16 a[64:65], v12 offset:0\n"
      "v_add_f32 v6, v6, v50\n"
      "v_mfma_f32_16x16x32_bf16 v[68:71], a[20:23], a[188:191], v[68:71]\n"
      "v_exp_f32 v65, v65\n"
      "v_mfma_f32_16x16x32_bf16 v[68:71], a[24:27], a[192:195], v[68:71]\n"
      "ds_read_b64_tr_b16 a[66:67], v12 offset:8192\n"
      "v_cvt_pk_bf16_f32 v96, v64, v65\n"
      "v_mfma_f32_16x16x32_bf16 v[68:71], a[28:31], a[196:199], v[68:71]\n"
      "v_exp_f32 v66, v66\n"
      "v_mfma_f32_16x16x32_bf16 v[72:75], a[32:35], a[104:107], v[24:27]\n"
      "ds_read_b64_tr_b16 a[68:69], v12 offset:16384\n"
      "v_add_f32 v6, v6, v51\n"
      "v_mfma_f32_16x16x32_bf16 v[72:75], a[36:39], a[108:111], v[72:75]\n"
      "v_exp_f32 v67, v67\n"
      "v_mfma_f32_16x16x32_bf16 v[72:75], a[40:43], a[112:115], v[72:75]\n"
      "ds_read_b64_tr_b16 a[70:71], v12 offset:24576\n"
      "v_cvt_pk_bf16_f32 v97, v66, v67\n"
      "v_mfma_f32_16x16x32_bf16 v[72:75], a[44:47], a[116:119], v[72:75]\n"
      "v_exp_f32 v68, v68\n"
      "v_mfma_f32_16x16x32_bf16 v[72:75], a[48:51], a[120:123], v[72:75]\n"
      "ds_read_b64_tr_b16 a[72:73], v13 offset:0\n"
      "v_add_f32 v8, v8, v52\n"
      "v_mfma_f32_16x16x32_bf16 v[72:75], a[52:55], a[124:127], v[72:75]\n"
      "v_exp_f32 v69, v69\n"
      "v_mfma_f32_16x16x32_bf16 v[72:75], a[56:59], a[128:131], v[72:75]\n"
      "ds_read_b64_tr_b16 a[74:75], v13 offset:8192\n"
      "v_cvt_pk_bf16_f32 v104, v68, v69\n"
      "v_mfma_f32_16x16x32_bf16 v[72:75], a[60:63], a[132:135], v[72:75]\n"
      "v_exp_f32 v70, v70\n"
      "v_mfma_f32_16x16x32_bf16 v[76:79], a[32:35], a[136:139], v[28:31]\n"
      "ds_read_b64_tr_b16 a[76:77], v13 offset:16384\n"
      "v_add_f32 v8, v8, v53\n"
      "v_mfma_f32_16x16x32_bf16 v[76:79], a[36:39], a[140:143], v[76:79]\n"
      "v_exp_f32 v71, v71\n"
      "v_mfma_f32_16x16x32_bf16 v[76:79], a[40:43], a[144:147], v[76:79]\n"
      "ds_read_b64_tr_b16 a[78:79], v13 offset:24576\n"
      "v_cvt_pk_bf16_f32 v105, v70, v71\n"
      "v_mfma_f32_16x16x32_bf16 v[76:79], a[44:47], a[148:151], v[76:79]\n"
      "v_exp_f32 v72, v72\n"
      "v_mfma_f32_16x16x32_bf16 v[76:79], a[48:51], a[152:155], v[76:79]\n"
      "ds_read_b64_tr_b16 a[80:81], v14 offset:0\n"
      "v_add_f32 v8, v8, v54\n"
      "v_mfma_f32_16x16x32_bf16 v[76:79], a[52:55], a[156:159], v[76:79]\n"
      "v_exp_f32 v73, v73\n"
      "v_mfma_f32_16x16x32_bf16 v[76:79], a[56:59], a[160:163], v[76:79]\n"
      "ds_read_b64_tr_b16 a[82:83], v14 offset:8192\n"
      "v_cvt_pk_bf16_f32 v90, v72, v73\n"
      "v_mfma_f32_16x16x32_bf16 v[76:79], a[60:63], a[164:167], v[76:79]\n"
      "v_exp_f32 v74, v74\n"
      "v_mfma_f32_16x16x32_bf16 v[80:83], a[32:35], a[168:171], v[32:35]\n"
      "ds_read_b64_tr_b16 a[84:85], v14 offset:16384\n"
      "v_add_f32 v8, v8, v55\n"
      "v_mfma_f32_16x16x32_bf16 v[80:83], a[36:39], a[172:175], v[80:83]\n"
      "v_exp_f32 v75, v75\n"
      "v_mfma_f32_16x16x32_bf16 v[80:83], a[40:43], a[176:179], v[80:83]\n"
      "ds_read_b64_tr_b16 a[86:87], v14 offset:24576\n"
      "v_cvt_pk_bf16_f32 v91, v74, v75\n"
      "v_mfma_f32_16x16x32_bf16 v[80:83], a[44:47], a[180:183], v[80:83]\n"
      "v_exp_f32 v76, v76\n"
      "v_mfma_f32_16x16x32_bf16 v[80:83], a[48:51], a[184:187], v[80:83]\n"
      "v_mfma_f32_16x16x32_bf16 v[80:83], a[52:55], a[188:191], v[80:83]\n"
      "v_exp_f32 v77, v77\n"
      "v_mfma_f32_16x16x32_bf16 v[80:83], a[56:59], a[192:195], v[80:83]\n"
      "v_cvt_pk_bf16_f32 v98, v76, v77\n"
      "v_mfma_f32_16x16x32_bf16 v[80:83], a[60:63], a[196:199], v[80:83]\n"
      "s_waitcnt lgkmcnt(8)\n"
      "v_exp_f32 v78, v78\n"
      "L_loopD0_%=:\n"
      "v_mfma_f32_16x16x32_bf16 a[200:203], v[84:87], a[64:67], a[200:203]\n"
      "ds_read_b64_tr_b16 a[88:89], v15 offset:0\n"
      "v_add_f32 v10, v10, v56\n"
      "v_mfma_f32_16x16x32_bf16 a[200:203], v[88:91], a[68:71], a[200:203]\n"
      "s_waitcnt lgkmcnt(5)\n"
      "v_exp_f32 v79, v79\n"
      "v_mfma_f32_16x16x32_bf16 a[212:215], v[84:87], a[72:75], a[212:215]\n"
      "s_add_u32 m0, s23, 0\n"
      "ds_read_b64_tr_b16 a[90:91], v15 offset:8192\n"
      "v_cvt_pk_bf16_f32 v99, v78, v79\n"
      "v_mfma_f32_16x16x32_bf16 a[212:215], v[88:91], a[76:79], a[212:215]\n"
      "s_waitcnt lgkmcnt(2)\n"
      "v_exp_f32 v80, v80\n"
      "v_mfma_f32_16x16x32_bf16 a[224:227], v[84:87], a[80:83], a[224:227]\n"
      "buffer_load_dwordx4 v1, s[36:39], 0 offen offset:0 lds\n"
      "v_mfma_f32_16x16x32_bf16 a[224:227], v[88:91], a[84:87], a[224:227]\n"
      "v_exp_f32 v81, v81\n"
      "v_mfma_f32_16x16x32_bf16 a[204:207], v[92:95], a[64:67], a[204:207]\n"
      "ds_read_b64_tr_b16 a[92:93], v15 offset:16384\n"
      "v_cvt_pk_bf16_f32 v106, v80, v81\n"
      "v_mfma_f32_16x16x32_bf16 a[204:207], v[96:99], a[68:71], a[204:207]\n"
      "v_exp_f32 v82, v82\n"
      "v_mfma_f32_16x16x32_bf16 a[216:219], v[92:95], a[72:75], a[216:219]\n"
      "ds_read_b64_tr_b16 a[94:95], v15 offset:24576\n"
      "v_add_f32 v10, v10, v57\n"
      "v_mfma_f32_16x16x32_bf16 a[216:219], v[96:99], a[76:79], a[216:219]\n"
      "v_exp_f32 v83, v83\n"
      "v_mfma_f32_16x16x32_bf16 a[228:231], v[92:95], a[80:83], a[228:231]\n"
      "ds_read_b64_tr_b16 a[96:97], v12 offset:2048\n"
      "v_cvt_pk_bf16_f32 v107, v82, v83\n"
      "v_mfma_f32_16x16x32_bf16 a[228:231], v[96:99], a[84:87], a[228:231]\n"
      "s_add_u32 m0, s23, 1024\n"
      "ds_read_b64_tr_b16 a[98:99], v12 offset:10240\n"
      "v_add_f32 v10, v10, v58\n"
      "v_mfma_f32_16x16x32_bf16 a[208:211], v[100:103], a[64:67], a[208:211]\n"
      "buffer_load_dwordx4 v1, s[36:39], s52 offen offset:0 lds\n"
      "v_mfma_f32_16x16x32_bf16 a[208:211], v[104:107], a[68:71], a[208:211]\n"
      "ds_read_b64_tr_b16 a[100:101], v12 offset:18432\n"
      "v_add_f32 v10, v10, v59\n"
      "v_mfma_f32_16x16x32_bf16 a[220:223], v[100:103], a[72:75], a[220:223]\n"
      "ds_read_b64_tr_b16 a[102:103], v12 offset:26624\n"
      "v_add_f32 v6, v6, v60\n"
      "v_mfma_f32_16x16x32_bf16 a[220:223], v[104:107], a[76:79], a[220:223]\n"
      "ds_read_b64_tr_b16 a[64:65], v13 offset:2048\n"
      "v_add_f32 v6, v6, v61\n"
      "v_mfma_f32_16x16x32_bf16 a[232:235], v[100:103], a[80:83], a[232:235]\n"
      "ds_read_b64_tr_b16 a[66:67], v13 offset:10240\n"
      "v_add_f32 v6, v6, v62\n"
      "v_mfma_f32_16x16x32_bf16 a[232:235], v[104:107], a[84:87], a[232:235]\n"
      "s_waitcnt lgkmcnt(6)\n"
      "ds_read_b64_tr_b16 a[68:69], v13 offset:18432\n"
      "v_add_f32 v6, v6, v63\n"
      "v_mfma_f32_16x16x32_bf16 a[236:239], v[84:87], a[88:91], a[236:239]\n"
      "ds_read_b64_tr_b16 a[70:71], v13 offset:26624\n"
      "v_add_f32 v8, v8, v64\n"
      "v_mfma_f32_16x16x32_bf16 a[236:239], v[88:91], a[92:95], a[236:239]\n"
      "s_add_u32 m0, s23, 1920\n"
      "ds_read_b64_tr_b16 a[72:73], v14 offset:2048\n"
      "v_add_f32 v8, v8, v65\n"
      "v_mfma_f32_16x16x32_bf16 a[240:243], v[92:95], a[88:91], a[240:243]\n"
      "buffer_load_dwordx4 v1, s[36:39], 0 offen offset:128 lds\n"
      "v_mfma_f32_16x16x32_bf16 a[240:243], v[96:99], a[92:95], a[240:243]\n"
      "ds_read_b64_tr_b16 a[74:75], v14 offset:10240\n"
      "v_add_f32 v8, v8, v66\n"
      "v_mfma_f32_16x16x32_bf16 v[108:111], v[100:103], a[88:91], v[108:111]\n"
      "ds_read_b64_tr_b16 a[76:77], v14 offset:18432\n"
      "v_add_f32 v8, v8, v67\n"
      "v_mfma_f32_16x16x32_bf16 v[108:111], v[104:107], a[92:95], v[108:111]\n"
      "s_waitcnt lgkmcnt(7)\n"
      "ds_read_b64_tr_b16 a[78:79], v14 offset:26624\n"
      "v_add_f32 v10, v10, v68\n"
      "v_mfma_f32_16x16x32_bf16 v[112:115], v[84:87], a[96:99], v[112:115]\n"
      "ds_read_b64_tr_b16 a[80:81], v15 offset:2048\n"
      "v_add_f32 v10, v10, v69\n"
      "v_mfma_f32_16x16x32_bf16 v[112:115], v[88:91], a[100:103], v[112:115]\n"
      "ds_read_b64_tr_b16 a[82:83], v15 offset:10240\n"
      "v_add_f32 v10, v10, v70\n"
      "v_mfma_f32_16x16x32_bf16 v[116:119], v[92:95], a[96:99], v[116:119]\n"
      "ds_read_b64_tr_b16 a[84:85], v15 offset:18432\n"
      "v_add_f32 v10, v10, v71\n"
      "v_mfma_f32_16x16x32_bf16 v[116:119], v[96:99], a[100:103], v[116:119]\n"
      "s_add_u32 m0, s23, 2944\n"
      "ds_read_b64_tr_b16 a[86:87], v15 offset:26624\n"
      "v_add_f32 v6, v6, v72\n"
      "v_mfma_f32_16x16x32_bf16 v[120:123], v[100:103], a[96:99], v[120:123]\n"
      "buffer_load_dwordx4 v1, s[36:39], s52 offen offset:128 lds\n"
      "v_mfma_f32_16x16x32_bf16 v[120:123], v[104:107], a[100:103], v[120:123]\n"
      "s_waitcnt lgkmcnt(8)\n"
      "ds_read_b64_tr_b16 a[88:89], v12 offset:4096\n"
      "v_add_f32 v6, v6, v73\n"
      "v_mfma_f32_16x16x32_bf16 v[124:127], v[84:87], a[64:67], v[124:127]\n"
      "ds_read_b64_tr_b16 a[90:91], v12 offset:12288\n"
      "v_add_f32 v6, v6, v74\n"
      "v_mfma_f32_16x16x32_bf16 v[124:127], v[88:91], a[68:71], v[124:127]\n"
      "ds_read_b64_tr_b16 a[92:93], v12 offset:20480\n"
      "v_add_f32 v6, v6, v75\n"
      "v_mfma_f32_16x16x32_bf16 v[128:131], v[92:95], a[64:67], v[128:131]\n"
      "ds_read_b64_tr_b16 a[94:95], v12 offset:28672\n"
      "v_add_f32 v8, v8, v76\n"
      "v_mfma_f32_16x16x32_bf16 v[128:131], v[96:99], a[68:71], v[128:131]\n"
      "ds_read_b64_tr_b16 a[96:97], v13 offset:4096\n"
      "v_add_f32 v8, v8, v77\n"
      "v_mfma_f32_16x16x32_bf16 v[132:135], v[100:103], a[64:67], v[132:135]\n"
      "s_add_u32 m0, s23, 3840\n"
      "ds_read_b64_tr_b16 a[98:99], v13 offset:12288\n"
      "v_add_f32 v8, v8, v78\n"
      "v_mfma_f32_16x16x32_bf16 v[132:135], v[104:107], a[68:71], v[132:135]\n"
      "s_waitcnt lgkmcnt(10)\n"
      "ds_read_b64_tr_b16 a[100:101], v13 offset:20480\n"
      "v_add_f32 v8, v8, v79\n"
      "v_mfma_f32_16x16x32_bf16 v[136:139], v[84:87], a[72:75], v[136:139]\n"
      "buffer_load_dwordx4 v1, s[36:39], 0 offen offset:256 lds\n"
      "v_mfma_f32_16x16x32_bf16 v[136:139], v[88:91], a[76:79], v[136:139]\n"
      "ds_read_b64_tr_b16 a[102:103], v13 offset:28672\n"
      "v_add_f32 v10, v10, v80\n"
      "v_mfma_f32_16x16x32_bf16 v[140:143], v[92:95], a[72:75], v[140:143]\n"
      "ds_read_b64_tr_b16 a[64:65], v14 offset:4096\n"
      "v_add_f32 v10, v10, v81\n"
      "v_mfma_f32_16x16x32_bf16 v[140:143], v[96:99], a[76:79], v[140:143]\n"
      "ds_read_b64_tr_b16 a[66:67], v14 offset:12288\n"
      "v_add_f32 v10, v10, v82\n"
      "v_mfma_f32_16x16x32_bf16 v[144:147], v[100:103], a[72:75], v[144:147]\n"
      "ds_read_b64_tr_b16 a[68:69], v14 offset:20480\n"
      "v_add_f32 v10, v10, v83\n"
      "v_mfma_f32_16x16x32_bf16 v[144:147], v[104:107], a[76:79], v[144:147]\n"
      "s_waitcnt lgkmcnt(11)\n"
      "ds_read_b64_tr_b16 a[70:71], v14 offset:28672\n"
      "v_mfma_f32_16x16x32_bf16 v[148:151], v[84:87], a[80:83], v[148:151]\n"
      "ds_read_b64_tr_b16 a[72:73], v15 offset:4096\n"
      "v_mfma_f32_16x16x32_bf16 v[148:151], v[88:91], a[84:87], v[148:151]\n"
      "ds_read_b64_tr_b16 a[74:75], v15 offset:12288\n"
      "v_mfma_f32_16x16x32_bf16 v[152:155], v[92:95], a[80:83], v[152:155]\n"
      "ds_read_b64_tr_b16 a[76:77], v15 offset:20480\n"
      "v_mfma_f32_16x16x32_bf16 v[152:155], v[96:99], a[84:87], v[152:155]\n"
      "ds_read_b64_tr_b16 a[78:79], v15 offset:28672\n"
      "v_mfma_f32_16x16x32_bf16 v[156:159], v[100:103], a[80:83], v[156:159]\n"
      "s_add_u32 m0, s23, 4864\n"
      "v_mfma_f32_16x16x32_bf16 v[156:159], v[104:107], a[84:87], v[156:159]\n"
      "s_waitcnt lgkmcnt(12)\n"
      "ds_read_b64_tr_b16 a[80:81], v12 offset:6144\n"
      "v_mfma_f32_16x16x32_bf16 v[160:163], v[84:87], a[88:91], v[160:163]\n"
      "buffer_load_dwordx4 v1, s[36:39], s52 offen offset:256 lds\n"
      "v_mfma_f32_16x16x32_bf16 v[160:163], v[88:91], a[92:95], v[160:163]\n"
      "ds_read_b64_tr_b16 a[82:83], v12 offset:14336\n"
      "v_mfma_f32_16x16x32_bf16 v[164:167], v[92:95], a[88:91], v[164:167]\n"
      "ds_read_b64_tr_b16 a[84:85], v12 offset:22528\n"
      "v_mfma_f32_16x16x32_bf16 v[164:167], v[96:99], a[92:95], v[164:167]\n"
      "ds_read_b64_tr_b16 a[86:87], v12 offset:30720\n"
      "v_mfma_f32_16x16x32_bf16 v[168:171], v[100:103], a[88:91], v[168:171]\n"
      "v_mfma_f32_16x16x32_bf16 v[168:171], v[104:107], a[92:95], v[168:171]\n"
      "s_waitcnt lgkmcnt(12)\n"
      "ds_read_b64_tr_b16 a[88:89], v13 offset:6144\n"
      "v_mfma_f32_16x16x32_bf16 v[172:175], v[84:87], a[96:99], v[172:175]\n"
      "ds_read_b64_tr_b16 a[90:91], v13 offset:14336\n"
      "v_mfma_f32_16x16x32_bf16 v[172:175], v[88:91], a[100:103], v[172:175]\n"
      "s_add_u32 m0, s23, 5760\n"
      "ds_read_b64_tr_b16 a[92:93], v13 offset:22528\n"
      "v_mfma_f32_16x16x32_bf16 v[176:179], v[92:95], a[96:99], v[176:179]\n"
      "buffer_load_dwordx4 v1, s[36:39], 0 offen offset:384 lds\n"
      "v_mfma_f32_16x16x32_bf16 v[176:179], v[96:99], a[100:103], v[176:179]\n"
      "ds_read_b64_tr_b16 a[94:95], v13 offset:30720\n"
      "v_mfma_f32_16x16x32_bf16 v[180:183], v[100:103], a[96:99], v[180:183]\n"
      "v_mfma_f32_16x16x32_bf16 v[180:183], v[104:107], a[100:103], v[180:183]\n"
      "s_waitcnt lgkmcnt(12)\n"
      "ds_read_b64_tr_b16 a[96:97], v14 offset:6144\n"
      "v_mfma_f32_16x16x32_bf16 v[184:187], v[84:87], a[64:67], v[184:187]\n"
      "ds_read_b64_tr_b16 a[98:99], v14 offset:14336\n"
      "v_mfma_f32_16x16x32_bf16 v[184:187], v[88:91], a[68:71], v[184:187]\n"
      "ds_read_b64_tr_b16 a[100:101], v14 offset:22528\n"
      "v_mfma_f32_16x16x32_bf16 v[188:191], v[92:95], a[64:67], v[188:191]\n"
      "ds_read_b64_tr_b16 a[102:103], v14 offset:30720\n"
      "v_mfma_f32_16x16x32_bf16 v[188:191], v[96:99], a[68:71], v[188:191]\n"
      "s_add_u32 m0, s23, 6784\n"
      "v_mfma_f32_16x16x32_bf16 v[192:195], v[100:103], a[64:67], v[192:195]\n"
      "buffer_load_dwordx4 v1, s[36:39], s52 offen offset:384 lds\n"
      "v_mfma_f32_16x16x32_bf16 v[192:195], v[104:107], a[68:71], v[192:195]\n"
      "s_waitcnt lgkmcnt(12)\n"
      "ds_read_b64_tr_b16 a[64:65], v15 offset:6144\n"
      "v_add_u32 v1, 0x8000, v1\n"
      "v_mfma_f32_16x16x32_bf16 v[196:199], v[84:87], a[72:75], v[196:199]\n"
      "ds_read_b64_tr_b16 a[66:67], v15 offset:14336\n"
      "v_mfma_f32_16x16x32_bf16 v[196:199], v[88:91], a[76:79], v[196:199]\n"
      "ds_read_b64_tr_b16 a[68:69], v15 offset:22528\n"
      "v_mfma_f32_16x16x32_bf16 v[200:203], v[92:95], a[72:75], v[200:203]\n"
      "buffer_load_dword v23, v22, s[60:63], 0 offen\n"
      "v_mfma_f32_16x16x32_bf16 v[200:203], v[96:99], a[76:79], v[200:203]\n"
      "ds_read_b64_tr_b16 a[70:71], v15 offset:30720\n"
      "v_add_u32 v22, 0x400, v22\n"
      "v_mfma_f32_16x16x32_bf16 v[204:207], v[100:103], a[72:75], v[204:207]\n"
      "v_mfma_f32_16x16x32_bf16 v[204:207], v[104:107], a[76:79], v[204:207]\n"
      "s_waitcnt lgkmcnt(12)\n"
      "v_mfma_f32_16x16x32_bf16 v[208:211], v[84:87], a[80:83], v[208:211]\n"
      "v_mfma_f32_16x16x32_bf16 v[208:211], v[88:91], a[84:87], v[208:211]\n"
      "s_getreg_b32 s29, hwreg(HW_REG_IB_STS, 0, 4)\n"
      "v_mfma_f32_16x16x32_bf16 v[212:215], v[92:95], a[80:83], v[212:215]\n"
      "s_cmp_ge_u32 s29, 10\n"
      "v_mfma_f32_16x16x32_bf16 v[212:215], v[96:99], a[84:87], v[212:215]\n"
      "s_cbranch_scc1 L_midN0_%=\n"
      "v_mfma_f32_16x16x32_bf16 v[216:219], v[100:103], a[80:83], v[216:219]\n"
      "v_mfma_f32_16x16x32_bf16 v[216:219], v[104:107], a[84:87], v[216:219]\n"
      "s_waitcnt lgkmcnt(8)\n"
      "v_mfma_f32_16x16x32_bf16 v[220:223], v[84:87], a[88:91], v[220:223]\n"
      "s_waitcnt vmcnt(17) lgkmcnt(0)\n"
      "v_mfma_f32_16x16x32_bf16 v[220:223], v[88:91], a[92:95], v[220:223]\n"
      "s_barrier\n"
      "ds_read_b128 a[0:3], v3 offset:32768\n"
      "v_mfma_f32_16x16x32_bf16 v[224:227], v[92:95], a[88:91], v[224:227]\n"
      "ds_read_b128 a[4:7], v4 offset:32768\n"
      "v_mfma_f32_16x16x32_bf16 v[224:227], v[96:99], a[92:95], v[224:227]\n"
      "ds_read_b128 a[8:11], v3 offset:34816\n"
      "v_mfma_f32_16x16x32_bf16 v[228:231], v[100:103], a[88:91], v[228:231]\n"
      "ds_read_b128 a[12:15], v4 offset:34816\n"
      "v_mfma_f32_16x16x32_bf16 v[228:231], v[104:107], a[92:95], v[228:231]\n"
      "s_add_u32 m0, s23, 65536\n"
      "ds_read_b128 a[16:19], v3 offset:36864\n"
      "v_mfma_f32_16x16x32_bf16 v[232:235], v[84:87], a[96:99], v[232:235]\n"
      "buffer_load_dwordx4 v2, s[40:43], 0 offen offset:0 lds\n"
      "v_mfma_f32_16x16x32_bf16 v[232:235], v[88:91], a[100:103], v[232:235]\n"
      "ds_read_b128 a[20:23], v4 offset:36864\n"
      "v_mfma_f32_16x16x32_bf16 v[236:239], v[92:95], a[96:99], v[236:239]\n"
      "ds_read_b128 a[24:27], v3 offset:38912\n"
      "v_mfma_f32_16x16x32_bf16 v[236:239], v[96:99], a[100:103], v[236:239]\n"
      "ds_read_b128 a[28:31], v4 offset:38912\n"
      "v_mfma_f32_16x16x32_bf16 v[240:243], v[100:103], a[96:99], v[240:243]\n"
      "ds_read_b128 a[32:35], v3 offset:40960\n"
      "v_mfma_f32_16x16x32_bf16 v[240:243], v[104:107], a[100:103], v[240:243]\n"
      "ds_read_b128 a[36:39], v4 offset:40960\n"
      "v_mfma_f32_16x16x32_bf16 v[244:247], v[84:87], a[64:67], v[244:247]\n"
      "ds_read_b128 a[40:43], v3 offset:43008\n"
      "v_mfma_f32_16x16x32_bf16 v[244:247], v[88:91], a[68:71], v[244:247]\n"
      "s_add_u32 m0, s23, 66560\n"
      "ds_read_b128 a[44:47], v4 offset:43008\n"
      "v_mfma_f32_16x16x32_bf16 v[248:251], v[92:95], a[64:67], v[248:251]\n"
      "buffer_load_dwordx4 v2, s[40:43], s52 offen offset:0 lds\n"
      "v_mfma_f32_16x16x32_bf16 v[248:251], v[96:99], a[68:71], v[248:251]\n"
      "ds_read_b128 a[48:51], v3 offset:45056\n"
      "v_mfma_f32_16x16x32_bf16 v[252:255], v[100:103], a[64:67], v[252:255]\n"
      "ds_read_b128 a[52:55], v4 offset:45056\n"
      "v_mfma_f32_16x16x32_bf16 v[252:255], v[104:107], a[68:71], v[252:255]\n"
      "s_waitcnt lgkmcnt(6)\n"
      "ds_read_b128 a[56:59], v3 offset:47104\n"
      "v_mfma_f32_16x16x32_bf16 v[36:39], a[0:3], a[104:107], v[24:27]\n"
      "ds_read_b128 a[60:63], v4 offset:47104\n"
      "v_mfma_f32_16x16x32_bf16 v[36:39], a[4:7], a[108:111], v[36:39]\n"
      "v_mfma_f32_16x16x32_bf16 v[36:39], a[8:11], a[112:115], v[36:39]\n"
      "v_mfma_f32_16x16x32_bf16 v[36:39], a[12:15], a[116:119], v[36:39]\n"
      "v_mfma_f32_16x16x32_bf16 v[36:39], a[16:19], a[120:123], v[36:39]\n"
      "v_mfma_f32_16x16x32_bf16 v[36:39], a[20:23], a[124:127], v[36:39]\n"
      "v_mfma_f32_16x16x32_bf16 v[36:39], a[24:27], a[128:131], v[36:39]\n"
      "v_mfma_f32_16x16x32_bf16 v[36:39], a[28:31], a[132:135], v[36:39]\n"
      "s_add_u32 m0, s23, 67456\n"
      "v_mfma_f32_16x16x32_bf16 v[40:43], a[0:3], a[136:139], v[28:31]\n"
      "buffer_load_dwordx4 v2, s[40:43], 0 offen offset:128 lds\n"
      "v_mfma_f32_16x16x32_bf16 v[40:43], a[4:7], a[140:143], v[40:43]\n"
      "v_mfma_f32_16x16x32_bf16 v[40:43], a[8:11], a[144:147], v[40:43]\n"
      "v_mfma_f32_16x16x32_bf16 v[40:43], a[12:15], a[148:151], v[40:43]\n"
      "v_exp_f32 v36, v36\n"
      "v_mfma_f32_16x16x32_bf16 v[40:43], a[16:19], a[152:155], v[40:43]\n"
      "v_add_f32 v6, v6, v36\n"
      "v_mfma_f32_16x16x32_bf16 v[40:43], a[20:23], a[156:159], v[40:43]\n"
      "v_exp_f32 v37, v37\n"
      "v_mfma_f32_16x16x32_bf16 v[40:43], a[24:27], a[160:163], v[40:43]\n"
      "v_cvt_pk_bf16_f32 v84, v36, v37\n"
      "v_add_f32 v6, v6, v37\n"
      "v_mfma_f32_16x16x32_bf16 v[40:43], a[28:31], a[164:167], v[40:43]\n"
      "s_add_u32 m0, s23, 68480\n"
      "v_exp_f32 v38, v38\n"
      "v_mfma_f32_16x16x32_bf16 v[44:47], a[0:3], a[168:171], v[32:35]\n"
      "buffer_load_dwordx4 v2, s[40:43], s52 offen offset:128 lds\n"
      "v_mfma_f32_16x16x32_bf16 v[44:47], a[4:7], a[172:175], v[44:47]\n"
      "v_exp_f32 v39, v39\n"
      "v_mfma_f32_16x16x32_bf16 v[44:47], a[8:11], a[176:179], v[44:47]\n"
      "ds_read_b128 a[0:3], v3 offset:49152\n"
      "v_cvt_pk_bf16_f32 v85, v38, v39\n"
      "v_mfma_f32_16x16x32_bf16 v[44:47], a[12:15], a[180:183], v[44:47]\n"
      "v_exp_f32 v40, v40\n"
      "v_mfma_f32_16x16x32_bf16 v[44:47], a[16:19], a[184:187], v[44:47]\n"
      "ds_read_b128 a[4:7], v4 offset:49152\n"
      "v_add_f32 v6, v6, v38\n"
      "v_mfma_f32_16x16x32_bf16 v[44:47], a[20:23], a[188:191], v[44:47]\n"
      "v_exp_f32 v41, v41\n"
      "v_mfma_f32_16x16x32_bf16 v[44:47], a[24:27], a[192:195], v[44:47]\n"
      "ds_read_b128 a[8:11], v3 offset:51200\n"
      "v_cvt_pk_bf16_f32 v92, v40, v41\n"
      "v_mfma_f32_16x16x32_bf16 v[44:47], a[28:31], a[196:199], v[44:47]\n"
      "s_waitcnt lgkmcnt(3)\n"
      "v_exp_f32 v42, v42\n"
      "v_mfma_f32_16x16x32_bf16 v[48:51], a[32:35], a[104:107], v[24:27]\n"
      "ds_read_b128 a[12:15], v4 offset:51200\n"
      "v_add_f32 v6, v6, v39\n"
      "v_mfma_f32_16x16x32_bf16 v[48:51], a[36:39], a[108:111], v[48:51]\n"
      "v_exp_f32 v43, v43\n"
      "v_mfma_f32_16x16x32_bf16 v[48:51], a[40:43], a[112:115], v[48:51]\n"
      "ds_read_b128 a[16:19], v3 offset:53248\n"
      "v_cvt_pk_bf16_f32 v93, v42, v43\n"
      "v_mfma_f32_16x16x32_bf16 v[48:51], a[44:47], a[116:119], v[48:51]\n"
      "s_add_u32 m0, s23, 69376\n"
      "v_exp_f32 v44, v44\n"
      "v_mfma_f32_16x16x32_bf16 v[48:51], a[48:51], a[120:123], v[48:51]\n"
      "buffer_load_dwordx4 v2, s[40:43], 0 offen offset:256 lds\n"
      "v_mfma_f32_16x16x32_bf16 v[48:51], a[52:55], a[124:127], v[48:51]\n"
      "v_exp_f32 v45, v45\n"
      "v_mfma_f32_16x16x32_bf16 v[48:51], a[56:59], a[128:131], v[48:51]\n"
      "ds_read_b128 a[20:23], v4 offset:53248\n"
      "v_cvt_pk_bf16_f32 v100, v44, v45\n"
      "v_mfma_f32_16x16x32_bf16 v[48:51], a[60:63], a[132:135], v[48:51]\n"
      "v_exp_f32 v46, v46\n"
      "v_mfma_f32_16x16x32_bf16 v[52:55], a[32:35], a[136:139], v[28:31]\n"
      "ds_read_b128 a[24:27], v3 offset:55296\n"
      "v_add_f32 v8, v8, v40\n"
      "v_mfma_f32_16x16x32_bf16 v[52:55], a[36:39], a[140:143], v[52:55]\n"
      "v_exp_f32 v47, v47\n"
      "v_mfma_f32_16x16x32_bf16 v[52:55], a[40:43], a[144:147], v[52:55]\n"
      "ds_read_b128 a[28:31], v4 offset:55296\n"
      "v_cvt_pk_bf16_f32 v101, v46, v47\n"
      "v_mfma_f32_16x16x32_bf16 v[52:55], a[44:47], a[148:151], v[52:55]\n"
      "s_add_u32 m0, s23, 70400\n"
      "v_exp_f32 v48, v48\n"
      "v_mfma_f32_16x16x32_bf16 v[52:55], a[48:51], a[152:155], v[52:55]\n"
      "buffer_load_dwordx4 v2, s[40:43], s52 offen offset:256 lds\n"
      "v_mfma_f32_16x16x32_bf16 v[52:55], a[52:55], a[156:159], v[52:55]\n"
      "v_exp_f32 v49, v49\n"
      "v_mfma_f32_16x16x32_bf16 v[52:55], a[56:59], a[160:163], v[52:55]\n"
      "v_cvt_pk_bf16_f32 v86, v48, v49\n"
      "v_add_f32 v8, v8, v41\n"
      "v_mfma_f32_16x16x32_bf16 v[52:55], a[60:63], a[164:167], v[52:55]\n"
      "v_exp_f32 v50, v50\n"
      "v_mfma_f32_16x16x32_bf16 v[56:59], a[32:35], a[168:171], v[32:35]\n"
      "ds_read_b128 a[32:35], v3 offset:57344\n"
      "v_add_f32 v8, v8, v42\n"
      "v_mfma_f32_16x16x32_bf16 v[56:59], a[36:39], a[172:175], v[56:59]\n"
      "v_exp_f32 v51, v51\n"
      "v_mfma_f32_16x16x32_bf16 v[56:59], a[40:43], a[176:179], v[56:59]\n"
      "ds_read_b128 a[36:39], v4 offset:57344\n"
      "v_cvt_pk_bf16_f32 v87, v50, v51\n"
      "v_mfma_f32_16x16x32_bf16 v[56:59], a[44:47], a[180:183], v[56:59]\n"
      "v_exp_f32 v52, v52\n"
      "v_mfma_f32_16x16x32_bf16 v[56:59], a[48:51], a[184:187], v[56:59]\n"
      "ds_read_b128 a[40:43], v3 offset:59392\n"
      "v_add_f32 v8, v8, v43\n"
      "v_mfma_f32_16x16x32_bf16 v[56:59], a[52:55], a[188:191], v[56:59]\n"
      "v_exp_f32 v53, v53\n"
      "v_mfma_f32_16x16x32_bf16 v[56:59], a[56:59], a[192:195], v[56:59]\n"
      "s_add_u32 m0, s23, 71296\n"
      "ds_read_b128 a[44:47], v4 offset:59392\n"
      "v_cvt_pk_bf16_f32 v94, v52, v53\n"
      "v_mfma_f32_16x16x32_bf16 v[56:59], a[60:63], a[196:199], v[56:59]\n"
      "s_waitcnt lgkmcnt(4)\n"
      "v_exp_f32 v54, v54\n"
      "v_mfma_f32_16x16x32_bf16 v[60:63], a[0:3], a[104:107], v[24:27]\n"
      "buffer_load_dwordx4 v2, s[40:43], 0 offen offset:384 lds\n"
      "v_mfma_f32_16x16x32_bf16 v[60:63], a[4:7], a[108:111], v[60:63]\n"
      "v_exp_f32 v55, v55\n"
      "v_mfma_f32_16x16x32_bf16 v[60:63], a[8:11], a[112:115], v[60:63]\n"
      "ds_read_b128 a[48:51], v3 offset:61440\n"
      "v_cvt_pk_bf16_f32 v95, v54, v55\n"
      "v_mfma_f32_16x16x32_bf16 v[60:63], a[12:15], a[116:119], v[60:63]\n"
      "v_exp_f32 v56, v56\n"
      "v_mfma_f32_16x16x32_bf16 v[60:63], a[16:19], a[120:123], v[60:63]\n"
      "ds_read_b128 a[52:55], v4 offset:61440\n"
      "v_add_f32 v10, v10, v44\n"
      "v_mfma_f32_16x16x32_bf16 v[60:63], a[20:23], a[124:127], v[60:63]\n"
      "v_exp_f32 v57, v57\n"
      "v_mfma_f32_16x16x32_bf16 v[60:63], a[24:27], a[128:131], v[60:63]\n"
      "ds_read_b128 a[56:59], v3 offset:63488\n"
      "v_cvt_pk_bf16_f32 v102, v56, v57\n"
      "v_mfma_f32_16x16x32_bf16 v[60:63], a[28:31], a[132:135], v[60:63]\n"
      "s_add_u32 m0, s23, 72320\n"
      "v_exp_f32 v58, v58\n"
      "v_mfma_f32_16x16x32_bf16 v[64:67], a[0:3], a[136:139], v[28:31]\n"
      "buffer_load_dwordx4 v2, s[40:43], s52 offen offset:384 lds\n"
      "v_mfma_f32_16x16x32_bf16 v[64:67], a[4:7], a[140:143], v[64:67]\n"
      "v_exp_f32 v59, v59\n"
      "v_mfma_f32_16x16x32_bf16 v[64:67], a[8:11], a[144:147], v[64:67]\n"
      "ds_read_b128 a[60:63], v4 offset:63488\n"
      "v_cvt_pk_bf16_f32 v103, v58, v59\n"
      "v_mfma_f32_16x16x32_bf16 v[64:67], a[12:15], a[148:151], v[64:67]\n"
      "v_exp_f32 v60, v60\n"
      "v_mfma_f32_16x16x32_bf16 v[64:67], a[16:19], a[152:155], v[64:67]\n"
      "v_add_u32 v2, 0x8000, v2\n"
      "v_add_f32 v10, v10, v45\n"
      "v_mfma_f32_16x16x32_bf16 v[64:67], a[20:23], a[156:159], v[64:67]\n"
      "v_exp_f32 v61, v61\n"
      "v_mfma_f32_16x16x32_bf16 v[64:67], a[24:27], a[160:163], v[64:67]\n"
      "v_cvt_pk_bf16_f32 v88, v60, v61\n"
      "v_add_f32 v10, v10, v46\n"
      "v_mfma_f32_16x16x32_bf16 v[64:67], a[28:31], a[164:167], v[64:67]\n"
      "v_exp_f32 v62, v62\n"
      "v_mfma_f32_16x16x32_bf16 v[68:71], a[0:3], a[168:171], v[32:35]\n"
      "v_add_f32 v10, v10, v47\n"
      "v_add_f32 v6, v6, v48\n"
      "v_mfma_f32_16x16x32_bf16 v[68:71], a[4:7], a[172:175], v[68:71]\n"
      "v_exp_f32 v63, v63\n"
      "v_mfma_f32_16x16x32_bf16 v[68:71], a[8:11], a[176:179], v[68:71]\n"
      "s_waitcnt vmcnt(17) lgkmcnt(0)\n"
      "v_cvt_pk_bf16_f32 v89, v62, v63\n"
      "v_add_f32 v6, v6, v49\n"
      "v_mfma_f32_16x16x32_bf16 v[68:71], a[12:15], a[180:183], v[68:71]\n"
      "s_barrier\n"
      "v_exp_f32 v64, v64\n"
      "v_mfma_f32_16x16x32_bf16 v[68:71], a[16:19], a[184:187], v[68:71]\n"
      "ds_read_b64_tr_b16 a[64:65], v12 offset:32768\n"
      "v_add_f32 v6, v6, v50\n"
      "v_mfma_f32_16x16x32_bf16 v[68:71], a[20:23], a[188:191], v[68:71]\n"
      "v_exp_f32 v65, v65\n"
      "v_mfma_f32_16x16x32_bf16 v[68:71], a[24:27], a[192:195], v[68:71]\n"
      "ds_read_b64_tr_b16 a[66:67], v12 offset:40960\n"
      "v_cvt_pk_bf16_f32 v96, v64, v65\n"
      "v_mfma_f32_16x16x32_bf16 v[68:71], a[28:31], a[196:199], v[68:71]\n"
      "v_exp_f32 v66, v66\n"
      "v_mfma_f32_16x16x32_bf16 v[72:75], a[32:35], a[104:107], v[24:27]\n"
      "ds_read_b64_tr_b16 a[68:69], v12 offset:49152\n"
      "v_add_f32 v6, v6, v51\n"
      "v_mfma_f32_16x16x32_bf16 v[72:75], a[36:39], a[108:111], v[72:75]\n"
      "v_exp_f32 v67, v67\n"
      "v_mfma_f32_16x16x32_bf16 v[72:75], a[40:43], a[112:115], v[72:75]\n"
      "ds_read_b64_tr_b16 a[70:71], v12 offset:57344\n"
      "v_cvt_pk_bf16_f32 v97, v66, v67\n"
      "v_mfma_f32_16x16x32_bf16 v[72:75], a[44:47], a[116:119], v[72:75]\n"
      "v_exp_f32 v68, v68\n"
      "v_mfma_f32_16x16x32_bf16 v[72:75], a[48:51], a[120:123], v[72:75]\n"
      "ds_read_b64_tr_b16 a[72:73], v13 offset:32768\n"
      "v_add_f32 v8, v8, v52\n"
      "v_mfma_f32_16x16x32_bf16 v[72:75], a[52:55], a[124:127], v[72:75]\n"
      "v_exp_f32 v69, v69\n"
      "v_mfma_f32_16x16x32_bf16 v[72:75], a[56:59], a[128:131], v[72:75]\n"
      "ds_read_b64_tr_b16 a[74:75], v13 offset:40960\n"
      "v_cvt_pk_bf16_f32 v104, v68, v69\n"
      "v_mfma_f32_16x16x32_bf16 v[72:75], a[60:63], a[132:135], v[72:75]\n"
      "v_exp_f32 v70, v70\n"
      "v_mfma_f32_16x16x32_bf16 v[76:79], a[32:35], a[136:139], v[28:31]\n"
      "ds_read_b64_tr_b16 a[76:77], v13 offset:49152\n"
      "v_add_f32 v8, v8, v53\n"
      "v_mfma_f32_16x16x32_bf16 v[76:79], a[36:39], a[140:143], v[76:79]\n"
      "v_exp_f32 v71, v71\n"
      "v_mfma_f32_16x16x32_bf16 v[76:79], a[40:43], a[144:147], v[76:79]\n"
      "ds_read_b64_tr_b16 a[78:79], v13 offset:57344\n"
      "v_cvt_pk_bf16_f32 v105, v70, v71\n"
      "v_mfma_f32_16x16x32_bf16 v[76:79], a[44:47], a[148:151], v[76:79]\n"
      "v_exp_f32 v72, v72\n"
      "v_mfma_f32_16x16x32_bf16 v[76:79], a[48:51], a[152:155], v[76:79]\n"
      "ds_read_b64_tr_b16 a[80:81], v14 offset:32768\n"
      "v_add_f32 v8, v8, v54\n"
      "v_mfma_f32_16x16x32_bf16 v[76:79], a[52:55], a[156:159], v[76:79]\n"
      "v_exp_f32 v73, v73\n"
      "v_mfma_f32_16x16x32_bf16 v[76:79], a[56:59], a[160:163], v[76:79]\n"
      "ds_read_b64_tr_b16 a[82:83], v14 offset:40960\n"
      "v_cvt_pk_bf16_f32 v90, v72, v73\n"
      "v_mfma_f32_16x16x32_bf16 v[76:79], a[60:63], a[164:167], v[76:79]\n"
      "v_exp_f32 v74, v74\n"
      "v_mfma_f32_16x16x32_bf16 v[80:83], a[32:35], a[168:171], v[32:35]\n"
      "ds_read_b64_tr_b16 a[84:85], v14 offset:49152\n"
      "v_add_f32 v8, v8, v55\n"
      "v_mfma_f32_16x16x32_bf16 v[80:83], a[36:39], a[172:175], v[80:83]\n"
      "v_exp_f32 v75, v75\n"
      "v_mfma_f32_16x16x32_bf16 v[80:83], a[40:43], a[176:179], v[80:83]\n"
      "ds_read_b64_tr_b16 a[86:87], v14 offset:57344\n"
      "v_cvt_pk_bf16_f32 v91, v74, v75\n"
      "v_mfma_f32_16x16x32_bf16 v[80:83], a[44:47], a[180:183], v[80:83]\n"
      "v_exp_f32 v76, v76\n"
      "v_mfma_f32_16x16x32_bf16 v[80:83], a[48:51], a[184:187], v[80:83]\n"
      "v_mfma_f32_16x16x32_bf16 v[80:83], a[52:55], a[188:191], v[80:83]\n"
      "v_exp_f32 v77, v77\n"
      "v_mfma_f32_16x16x32_bf16 v[80:83], a[56:59], a[192:195], v[80:83]\n"
      "s_sub_u32 s20, s20, 1\n"
      "v_cvt_pk_bf16_f32 v98, v76, v77\n"
      "v_mfma_f32_16x16x32_bf16 v[80:83], a[60:63], a[196:199], v[80:83]\n"
      "s_waitcnt lgkmcnt(8)\n"
      "v_exp_f32 v78, v78\n"
      "s_cbranch_scc1 L_tail_%=\n"
      "v_mfma_f32_16x16x32_bf16 a[200:203], v[84:87], a[64:67], a[200:203]\n"
      "ds_read_b64_tr_b16 a[88:89], v15 offset:32768\n"
      "v_add_f32 v10, v10, v56\n"
      "v_mfma_f32_16x16x32_bf16 a[200:203], v[88:91], a[68:71], a[200:203]\n"
      "s_waitcnt lgkmcnt(5)\n"
      "v_exp_f32 v79, v79\n"
      "v_mfma_f32_16x16x32_bf16 a[212:215], v[84:87], a[72:75], a[212:215]\n"
      "s_add_u32 m0, s23, 32768\n"
      "ds_read_b64_tr_b16 a[90:91], v15 offset:40960\n"
      "v_cvt_pk_bf16_f32 v99, v78, v79\n"
      "v_mfma_f32_16x16x32_bf16 a[212:215], v[88:91], a[76:79], a[212:215]\n"
      "s_waitcnt lgkmcnt(2)\n"
      "v_exp_f32 v80, v80\n"
      "v_mfma_f32_16x16x32_bf16 a[224:227], v[84:87], a[80:83], a[224:227]\n"
      "buffer_load_dwordx4 v1, s[36:39], 0 offen offset:0 lds\n"
      "v_mfma_f32_16x16x32_bf16 a[224:227], v[88:91], a[84:87], a[224:227]\n"
      "v_exp_f32 v81, v81\n"
      "v_mfma_f32_16x16x32_bf16 a[204:207], v[92:95], a[64:67], a[204:207]\n"
      "ds_read_b64_tr_b16 a[92:93], v15 offset:49152\n"
      "v_cvt_pk_bf16_f32 v106, v80, v81\n"
      "v_mfma_f32_16x16x32_bf16 a[204:207], v[96:99], a[68:71], a[204:207]\n"
      "v_exp_f32 v82, v82\n"
      "v_mfma_f32_16x16x32_bf16 a[216:219], v[92:95], a[72:75], a[216:219]\n"
      "ds_read_b64_tr_b16 a[94:95], v15 offset:57344\n"
      "v_add_f32 v10, v10, v57\n"
      "v_mfma_f32_16x16x32_bf16 a[216:219], v[96:99], a[76:79], a[216:219]\n"
      "v_exp_f32 v83, v83\n"
      "v_mfma_f32_16x16x32_bf16 a[228:231], v[92:95], a[80:83], a[228:231]\n"
      "ds_read_b64_tr_b16 a[96:97], v12 offset:34816\n"
      "v_cvt_pk_bf16_f32 v107, v82, v83\n"
      "v_mfma_f32_16x16x32_bf16 a[228:231], v[96:99], a[84:87], a[228:231]\n"
      "s_add_u32 m0, s23, 33792\n"
      "ds_read_b64_tr_b16 a[98:99], v12 offset:43008\n"
      "v_add_f32 v10, v10, v58\n"
      "v_mfma_f32_16x16x32_bf16 a[208:211], v[100:103], a[64:67], a[208:211]\n"
      "buffer_load_dwordx4 v1, s[36:39], s52 offen offset:0 lds\n"
      "v_mfma_f32_16x16x32_bf16 a[208:211], v[104:107], a[68:71], a[208:211]\n"
      "ds_read_b64_tr_b16 a[100:101], v12 offset:51200\n"
      "v_add_f32 v10, v10, v59\n"
      "v_mfma_f32_16x16x32_bf16 a[220:223], v[100:103], a[72:75], a[220:223]\n"
      "ds_read_b64_tr_b16 a[102:103], v12 offset:59392\n"
      "v_add_f32 v6, v6, v60\n"
      "v_mfma_f32_16x16x32_bf16 a[220:223], v[104:107], a[76:79], a[220:223]\n"
      "ds_read_b64_tr_b16 a[64:65], v13 offset:34816\n"
      "v_add_f32 v6, v6, v61\n"
      "v_mfma_f32_16x16x32_bf16 a[232:235], v[100:103], a[80:83], a[232:235]\n"
      "ds_read_b64_tr_b16 a[66:67], v13 offset:43008\n"
      "v_add_f32 v6, v6, v62\n"
      "v_mfma_f32_16x16x32_bf16 a[232:235], v[104:107], a[84:87], a[232:235]\n"
      "s_waitcnt lgkmcnt(6)\n"
      "ds_read_b64_tr_b16 a[68:69], v13 offset:51200\n"
      "v_add_f32 v6, v6, v63\n"
      "v_mfma_f32_16x16x32_bf16 a[236:239], v[84:87], a[88:91], a[236:239]\n"
      "ds_read_b64_tr_b16 a[70:71], v13 offset:59392\n"
      "v_add_f32 v8, v8, v64\n"
      "v_mfma_f32_16x16x32_bf16 a[236:239], v[88:91], a[92:95], a[236:239]\n"
      "s_add_u32 m0, s23, 34688\n"
      "ds_read_b64_tr_b16 a[72:73], v14 offset:34816\n"
      "v_add_f32 v8, v8, v65\n"
      "v_mfma_f32_16x16x32_bf16 a[240:243], v[92:95], a[88:91], a[240:243]\n"
      "buffer_load_dwordx4 v1, s[36:39], 0 offen offset:128 lds\n"
      "v_mfma_f32_16x16x32_bf16 a[240:243], v[96:99], a[92:95], a[240:243]\n"
      "ds_read_b64_tr_b16 a[74:75], v14 offset:43008\n"
      "v_add_f32 v8, v8, v66\n"
      "v_mfma_f32_16x16x32_bf16 v[108:111], v[100:103], a[88:91], v[108:111]\n"
      "ds_read_b64_tr_b16 a[76:77], v14 offset:51200\n"
      "v_add_f32 v8, v8, v67\n"
      "v_mfma_f32_16x16x32_bf16 v[108:111], v[104:107], a[92:95], v[108:111]\n"
      "s_waitcnt lgkmcnt(7)\n"
      "ds_read_b64_tr_b16 a[78:79], v14 offset:59392\n"
      "v_add_f32 v10, v10, v68\n"
      "v_mfma_f32_16x16x32_bf16 v[112:115], v[84:87], a[96:99], v[112:115]\n"
      "ds_read_b64_tr_b16 a[80:81], v15 offset:34816\n"
      "v_add_f32 v10, v10, v69\n"
      "v_mfma_f32_16x16x32_bf16 v[112:115], v[88:91], a[100:103], v[112:115]\n"
      "ds_read_b64_tr_b16 a[82:83], v15 offset:43008\n"
      "v_add_f32 v10, v10, v70\n"
      "v_mfma_f32_16x16x32_bf16 v[116:119], v[92:95], a[96:99], v[116:119]\n"
      "ds_read_b64_tr_b16 a[84:85], v15 offset:51200\n"
      "v_add_f32 v10, v10, v71\n"
      "v_mfma_f32_16x16x32_bf16 v[116:119], v[96:99], a[100:103], v[116:119]\n"
      "s_add_u32 m0, s23, 35712\n"
      "ds_read_b64_tr_b16 a[86:87], v15 offset:59392\n"
      "v_add_f32 v6, v6, v72\n"
      "v_mfma_f32_16x16x32_bf16 v[120:123], v[100:103], a[96:99], v[120:123]\n"
      "buffer_load_dwordx4 v1, s[36:39], s52 offen offset:128 lds\n"
      "v_mfma_f32_16x16x32_bf16 v[120:123], v[104:107], a[100:103], v[120:123]\n"
      "s_waitcnt lgkmcnt(8)\n"
      "ds_read_b64_tr_b16 a[88:89], v12 offset:36864\n"
      "v_add_f32 v6, v6, v73\n"
      "v_mfma_f32_16x16x32_bf16 v[124:127], v[84:87], a[64:67], v[124:127]\n"
      "ds_read_b64_tr_b16 a[90:91], v12 offset:45056\n"
      "v_add_f32 v6, v6, v74\n"
      "v_mfma_f32_16x16x32_bf16 v[124:127], v[88:91], a[68:71], v[124:127]\n"
      "ds_read_b64_tr_b16 a[92:93], v12 offset:53248\n"
      "v_add_f32 v6, v6, v75\n"
      "v_mfma_f32_16x16x32_bf16 v[128:131], v[92:95], a[64:67], v[128:131]\n"
      "ds_read_b64_tr_b16 a[94:95], v12 offset:61440\n"
      "v_add_f32 v8, v8, v76\n"
      "v_mfma_f32_16x16x32_bf16 v[128:131], v[96:99], a[68:71], v[128:131]\n"
      "ds_read_b64_tr_b16 a[96:97], v13 offset:36864\n"
      "v_add_f32 v8, v8, v77\n"
      "v_mfma_f32_16x16x32_bf16 v[132:135], v[100:103], a[64:67], v[132:135]\n"
      "s_add_u32 m0, s23, 36608\n"
      "ds_read_b64_tr_b16 a[98:99], v13 offset:45056\n"
      "v_add_f32 v8, v8, v78\n"
      "v_mfma_f32_16x16x32_bf16 v[132:135], v[104:107], a[68:71], v[132:135]\n"
      "s_waitcnt lgkmcnt(10)\n"
      "ds_read_b64_tr_b16 a[100:101], v13 offset:53248\n"
      "v_add_f32 v8, v8, v79\n"
      "v_mfma_f32_16x16x32_bf16 v[136:139], v[84:87], a[72:75], v[136:139]\n"
      "buffer_load_dwordx4 v1, s[36:39], 0 offen offset:256 lds\n"
      "v_mfma_f32_16x16x32_bf16 v[136:139], v[88:91], a[76:79], v[136:139]\n"
      "ds_read_b64_tr_b16 a[102:103], v13 offset:61440\n"
      "v_add_f32 v10, v10, v80\n"
      "v_mfma_f32_16x16x32_bf16 v[140:143], v[92:95], a[72:75], v[140:143]\n"
      "ds_read_b64_tr_b16 a[64:65], v14 offset:36864\n"
      "v_add_f32 v10, v10, v81\n"
      "v_mfma_f32_16x16x32_bf16 v[140:143], v[96:99], a[76:79], v[140:143]\n"
      "ds_read_b64_tr_b16 a[66:67], v14 offset:45056\n"
      "v_add_f32 v10, v10, v82\n"
      "v_mfma_f32_16x16x32_bf16 v[144:147], v[100:103], a[72:75], v[144:147]\n"
      "ds_read_b64_tr_b16 a[68:69], v14 offset:53248\n"
      "v_add_f32 v10, v10, v83\n"
      "v_mfma_f32_16x16x32_bf16 v[144:147], v[104:107], a[76:79], v[144:147]\n"
      "s_waitcnt lgkmcnt(11)\n"
      "ds_read_b64_tr_b16 a[70:71], v14 offset:61440\n"
      "v_mfma_f32_16x16x32_bf16 v[148:151], v[84:87], a[80:83], v[148:151]\n"
      "ds_read_b64_tr_b16 a[72:73], v15 offset:36864\n"
      "v_mfma_f32_16x16x32_bf16 v[148:151], v[88:91], a[84:87], v[148:151]\n"
      "ds_read_b64_tr_b16 a[74:75], v15 offset:45056\n"
      "v_mfma_f32_16x16x32_bf16 v[152:155], v[92:95], a[80:83], v[152:155]\n"
      "ds_read_b64_tr_b16 a[76:77], v15 offset:53248\n"
      "v_mfma_f32_16x16x32_bf16 v[152:155], v[96:99], a[84:87], v[152:155]\n"
      "ds_read_b64_tr_b16 a[78:79], v15 offset:61440\n"
      "v_mfma_f32_16x16x32_bf16 v[156:159], v[100:103], a[80:83], v[156:159]\n"
      "s_add_u32 m0, s23, 37632\n"
      "v_mfma_f32_16x16x32_bf16 v[156:159], v[104:107], a[84:87], v[156:159]\n"
      "s_waitcnt lgkmcnt(12)\n"
      "ds_read_b64_tr_b16 a[80:81], v12 offset:38912\n"
      "v_mfma_f32_16x16x32_bf16 v[160:163], v[84:87], a[88:91], v[160:163]\n"
      "buffer_load_dwordx4 v1, s[36:39], s52 offen offset:256 lds\n"
      "v_mfma_f32_16x16x32_bf16 v[160:163], v[88:91], a[92:95], v[160:163]\n"
      "ds_read_b64_tr_b16 a[82:83], v12 offset:47104\n"
      "v_mfma_f32_16x16x32_bf16 v[164:167], v[92:95], a[88:91], v[164:167]\n"
      "ds_read_b64_tr_b16 a[84:85], v12 offset:55296\n"
      "v_mfma_f32_16x16x32_bf16 v[164:167], v[96:99], a[92:95], v[164:167]\n"
      "ds_read_b64_tr_b16 a[86:87], v12 offset:63488\n"
      "v_mfma_f32_16x16x32_bf16 v[168:171], v[100:103], a[88:91], v[168:171]\n"
      "v_mfma_f32_16x16x32_bf16 v[168:171], v[104:107], a[92:95], v[168:171]\n"
      "s_waitcnt lgkmcnt(12)\n"
      "ds_read_b64_tr_b16 a[88:89], v13 offset:38912\n"
      "v_mfma_f32_16x16x32_bf16 v[172:175], v[84:87], a[96:99], v[172:175]\n"
      "ds_read_b64_tr_b16 a[90:91], v13 offset:47104\n"
      "v_mfma_f32_16x16x32_bf16 v[172:175], v[88:91], a[100:103], v[172:175]\n"
      "s_add_u32 m0, s23, 38528\n"
      "ds_read_b64_tr_b16 a[92:93], v13 offset:55296\n"
      "v_mfma_f32_16x16x32_bf16 v[176:179], v[92:95], a[96:99], v[176:179]\n"
      "buffer_load_dwordx4 v1, s[36:39], 0 offen offset:384 lds\n"
      "v_mfma_f32_16x16x32_bf16 v[176:179], v[96:99], a[100:103], v[176:179]\n"
      "ds_read_b64_tr_b16 a[94:95], v13 offset:63488\n"
      "v_mfma_f32_16x16x32_bf16 v[180:183], v[100:103], a[96:99], v[180:183]\n"
      "v_mfma_f32_16x16x32_bf16 v[180:183], v[104:107], a[100:103], v[180:183]\n"
      "s_waitcnt lgkmcnt(12)\n"
      "ds_read_b64_tr_b16 a[96:97], v14 offset:38912\n"
      "v_mfma_f32_16x16x32_bf16 v[184:187], v[84:87], a[64:67], v[184:187]\n"
      "ds_read_b64_tr_b16 a[98:99], v14 offset:47104\n"
      "v_mfma_f32_16x16x32_bf16 v[184:187], v[88:91], a[68:71], v[184:187]\n"
      "ds_read_b64_tr_b16 a[100:101], v14 offset:55296\n"
      "v_mfma_f32_16x16x32_bf16 v[188:191], v[92:95], a[64:67], v[188:191]\n"
      "ds_read_b64_tr_b16 a[102:103], v14 offset:63488\n"
      "v_mfma_f32_16x16x32_bf16 v[188:191], v[96:99], a[68:71], v[188:191]\n"
      "s_add_u32 m0, s23, 39552\n"
      "v_mfma_f32_16x16x32_bf16 v[192:195], v[100:103], a[64:67], v[192:195]\n"
      "buffer_load_dwordx4 v1, s[36:39], s52 offen offset:384 lds\n"
      "v_mfma_f32_16x16x32_bf16 v[192:195], v[104:107], a[68:71], v[192:195]\n"
      "s_waitcnt lgkmcnt(12)\n"
      "ds_read_b64_tr_b16 a[64:65], v15 offset:38912\n"
      "v_add_u32 v1, 0x8000, v1\n"
      "v_mfma_f32_16x16x32_bf16 v[196:199], v[84:87], a[72:75], v[196:199]\n"
      "ds_read_b64_tr_b16 a[66:67], v15 offset:47104\n"
      "v_mfma_f32_16x16x32_bf16 v[196:199], v[88:91], a[76:79], v[196:199]\n"
      "ds_read_b64_tr_b16 a[68:69], v15 offset:55296\n"
      "v_mfma_f32_16x16x32_bf16 v[200:203], v[92:95], a[72:75], v[200:203]\n"
      "buffer_load_dword v23, v22, s[60:63], 0 offen\n"
      "v_mfma_f32_16x16x32_bf16 v[200:203], v[96:99], a[76:79], v[200:203]\n"
      "ds_read_b64_tr_b16 a[70:71], v15 offset:63488\n"
      "v_add_u32 v22, 0x400, v22\n"
      "v_mfma_f32_16x16x32_bf16 v[204:207], v[100:103], a[72:75], v[204:207]\n"
      "v_mfma_f32_16x16x32_bf16 v[204:207], v[104:107], a[76:79], v[204:207]\n"
      "s_waitcnt lgkmcnt(12)\n"
      "v_mfma_f32_16x16x32_bf16 v[208:211], v[84:87], a[80:83], v[208:211]\n"
      "v_mfma_f32_16x16x32_bf16 v[208:211], v[88:91], a[84:87], v[208:211]\n"
      "s_getreg_b32 s29, hwreg(HW_REG_IB_STS, 0, 4)\n"
      "v_mfma_f32_16x16x32_bf16 v[212:215], v[92:95], a[80:83], v[212:215]\n"
      "s_cmp_ge_u32 s29, 10\n"
      "v_mfma_f32_16x16x32_bf16 v[212:215], v[96:99], a[84:87], v[212:215]\n"
      "s_cbranch_scc1 L_topN0_%=\n"
      "s_branch L_topD0_%=\n"
      ".p2align 6\n"
      "L_topN0_%=:\n"
      "v_mfma_f32_16x16x32_bf16 v[216:219], v[100:103], a[80:83], v[216:219]\n"
      "v_mfma_f32_16x16x32_bf16 v[216:219], v[104:107], a[84:87], v[216:219]\n"
      "s_waitcnt lgkmcnt(8)\n"
      "v_mfma_f32_16x16x32_bf16 v[220:223], v[84:87], a[88:91], v[220:223]\n"
      "s_waitcnt vmcnt(17) lgkmcnt(0)\n"
      "v_mfma_f32_16x16x32_bf16 v[220:223], v[88:91], a[92:95], v[220:223]\n"
      "s_barrier\n"
      "ds_read_b128 a[0:3], v3 offset:0\n"
      "v_mfma_f32_16x16x32_bf16 v[224:227], v[92:95], a[88:91], v[224:227]\n"
      "ds_read_b128 a[4:7], v4 offset:0\n"
      "v_mfma_f32_16x16x32_bf16 v[224:227], v[96:99], a[92:95], v[224:227]\n"
      "ds_read_b128 a[8:11], v3 offset:2048\n"
      "v_mfma_f32_16x16x32_bf16 v[228:231], v[100:103], a[88:91], v[228:231]\n"
      "ds_read_b128 a[12:15], v4 offset:2048\n"
      "v_mfma_f32_16x16x32_bf16 v[228:231], v[104:107], a[92:95], v[228:231]\n"
      "s_add_u32 m0, s23, 98304\n"
      "ds_read_b128 a[16:19], v3 offset:4096\n"
      "v_mfma_f32_16x16x32_bf16 v[232:235], v[84:87], a[96:99], v[232:235]\n"
      "buffer_load_dwordx4 v2, s[40:43], 0 offen offset:0 nt lds\n"
      "v_mfma_f32_16x16x32_bf16 v[232:235], v[88:91], a[100:103], v[232:235]\n"
      "ds_read_b128 a[20:23], v4 offset:4096\n"
      "v_mfma_f32_16x16x32_bf16 v[236:239], v[92:95], a[96:99], v[236:239]\n"
      "ds_read_b128 a[24:27], v3 offset:6144\n"
      "v_mfma_f32_16x16x32_bf16 v[236:239], v[96:99], a[100:103], v[236:239]\n"
      "ds_read_b128 a[28:31], v4 offset:6144\n"
      "v_mfma_f32_16x16x32_bf16 v[240:243], v[100:103], a[96:99], v[240:243]\n"
      "ds_read_b128 a[32:35], v3 offset:8192\n"
      "v_mfma_f32_16x16x32_bf16 v[240:243], v[104:107], a[100:103], v[240:243]\n"
      "ds_read_b128 a[36:39], v4 offset:8192\n"
      "v_mfma_f32_16x16x32_bf16 v[244:247], v[84:87], a[64:67], v[244:247]\n"
      "ds_read_b128 a[40:43], v3 offset:10240\n"
      "v_mfma_f32_16x16x32_bf16 v[244:247], v[88:91], a[68:71], v[244:247]\n"
      "s_add_u32 m0, s23, 99328\n"
      "ds_read_b128 a[44:47], v4 offset:10240\n"
      "v_mfma_f32_16x16x32_bf16 v[248:251], v[92:95], a[64:67], v[248:251]\n"
      "buffer_load_dwordx4 v2, s[40:43], s52 offen offset:0 nt lds\n"
      "v_mfma_f32_16x16x32_bf16 v[248:251], v[96:99], a[68:71], v[248:251]\n"
      "ds_read_b128 a[48:51], v3 offset:12288\n"
      "v_mfma_f32_16x16x32_bf16 v[252:255], v[100:103], a[64:67], v[252:255]\n"
      "ds_read_b128 a[52:55], v4 offset:12288\n"
      "v_mfma_f32_16x16x32_bf16 v[252:255], v[104:107], a[68:71], v[252:255]\n"
      "s_waitcnt lgkmcnt(6)\n"
      "ds_read_b128 a[56:59], v3 offset:14336\n"
      "v_mfma_f32_16x16x32_bf16 v[36:39], a[0:3], a[104:107], v[24:27]\n"
      "ds_read_b128 a[60:63], v4 offset:14336\n"
      "v_mfma_f32_16x16x32_bf16 v[36:39], a[4:7], a[108:111], v[36:39]\n"
      "v_mfma_f32_16x16x32_bf16 v[36:39], a[8:11], a[112:115], v[36:39]\n"
      "v_mfma_f32_16x16x32_bf16 v[36:39], a[12:15], a[116:119], v[36:39]\n"
      "v_mfma_f32_16x16x32_bf16 v[36:39], a[16:19], a[120:123], v[36:39]\n"
      "v_mfma_f32_16x16x32_bf16 v[36:39], a[20:23], a[124:127], v[36:39]\n"
      "v_mfma_f32_16x16x32_bf16 v[36:39], a[24:27], a[128:131], v[36:39]\n"
      "v_mfma_f32_16x16x32_bf16 v[36:39], a[28:31], a[132:135], v[36:39]\n"
      "s_add_u32 m0, s23, 100224\n"
      "v_mfma_f32_16x16x32_bf16 v[40:43], a[0:3], a[136:139], v[28:31]\n"
      "buffer_load_dwordx4 v2, s[40:43], 0 offen offset:128 nt lds\n"
      "v_mfma_f32_16x16x32_bf16 v[40:43], a[4:7], a[140:143], v[40:43]\n"
      "v_mfma_f32_16x16x32_bf16 v[40:43], a[8:11], a[144:147], v[40:43]\n"
      "v_mfma_f32_16x16x32_bf16 v[40:43], a[12:15], a[148:151], v[40:43]\n"
      "v_exp_f32 v36, v36\n"
      "v_mfma_f32_16x16x32_bf16 v[40:43], a[16:19], a[152:155], v[40:43]\n"
      "v_add_f32 v6, v6, v36\n"
      "v_mfma_f32_16x16x32_bf16 v[40:43], a[20:23], a[156:159], v[40:43]\n"
      "v_exp_f32 v37, v37\n"
      "v_mfma_f32_16x16x32_bf16 v[40:43], a[24:27], a[160:163], v[40:43]\n"
      "v_cvt_pk_bf16_f32 v84, v36, v37\n"
      "v_add_f32 v6, v6, v37\n"
      "v_mfma_f32_16x16x32_bf16 v[40:43], a[28:31], a[164:167], v[40:43]\n"
      "s_add_u32 m0, s23, 101248\n"
      "v_exp_f32 v38, v38\n"
      "v_mfma_f32_16x16x32_bf16 v[44:47], a[0:3], a[168:171], v[32:35]\n"
      "buffer_load_dwordx4 v2, s[40:43], s52 offen offset:128 nt lds\n"
      "v_mfma_f32_16x16x32_bf16 v[44:47], a[4:7], a[172:175], v[44:47]\n"
      "v_exp_f32 v39, v39\n"
      "v_mfma_f32_16x16x32_bf16 v[44:47], a[8:11], a[176:179], v[44:47]\n"
      "ds_read_b128 a[0:3], v3 offset:16384\n"
      "v_cvt_pk_bf16_f32 v85, v38, v39\n"
      "v_mfma_f32_16x16x32_bf16 v[44:47], a[12:15], a[180:183], v[44:47]\n"
      "v_exp_f32 v40, v40\n"
      "v_mfma_f32_16x16x32_bf16 v[44:47], a[16:19], a[184:187], v[44:47]\n"
      "ds_read_b128 a[4:7], v4 offset:16384\n"
      "v_add_f32 v6, v6, v38\n"
      "v_mfma_f32_16x16x32_bf16 v[44:47], a[20:23], a[188:191], v[44:47]\n"
      "v_exp_f32 v41, v41\n"
      "v_mfma_f32_16x16x32_bf16 v[44:47], a[24:27], a[192:195], v[44:47]\n"
      "ds_read_b128 a[8:11], v3 offset:18432\n"
      "v_cvt_pk_bf16_f32 v92, v40, v41\n"
      "v_mfma_f32_16x16x32_bf16 v[44:47], a[28:31], a[196:199], v[44:47]\n"
      "s_waitcnt lgkmcnt(3)\n"
      "v_exp_f32 v42, v42\n"
      "v_mfma_f32_16x16x32_bf16 v[48:51], a[32:35], a[104:107], v[24:27]\n"
      "ds_read_b128 a[12:15], v4 offset:18432\n"
      "v_add_f32 v6, v6, v39\n"
      "v_mfma_f32_16x16x32_bf16 v[48:51], a[36:39], a[108:111], v[48:51]\n"
      "v_exp_f32 v43, v43\n"
      "v_mfma_f32_16x16x32_bf16 v[48:51], a[40:43], a[112:115], v[48:51]\n"
      "ds_read_b128 a[16:19], v3 offset:20480\n"
      "v_cvt_pk_bf16_f32 v93, v42, v43\n"
      "v_mfma_f32_16x16x32_bf16 v[48:51], a[44:47], a[116:119], v[48:51]\n"
      "s_add_u32 m0, s23, 102144\n"
      "v_exp_f32 v44, v44\n"
      "v_mfma_f32_16x16x32_bf16 v[48:51], a[48:51], a[120:123], v[48:51]\n"
      "buffer_load_dwordx4 v2, s[40:43], 0 offen offset:256 nt lds\n"
      "v_mfma_f32_16x16x32_bf16 v[48:51], a[52:55], a[124:127], v[48:51]\n"
      "v_exp_f32 v45, v45\n"
      "v_mfma_f32_16x16x32_bf16 v[48:51], a[56:59], a[128:131], v[48:51]\n"
      "ds_read_b128 a[20:23], v4 offset:20480\n"
      "v_cvt_pk_bf16_f32 v100, v44, v45\n"
      "v_mfma_f32_16x16x32_bf16 v[48:51], a[60:63], a[132:135], v[48:51]\n"
      "v_exp_f32 v46, v46\n"
      "v_mfma_f32_16x16x32_bf16 v[52:55], a[32:35], a[136:139], v[28:31]\n"
      "ds_read_b128 a[24:27], v3 offset:22528\n"
      "v_add_f32 v8, v8, v40\n"
      "v_mfma_f32_16x16x32_bf16 v[52:55], a[36:39], a[140:143], v[52:55]\n"
      "v_exp_f32 v47, v47\n"
      "v_mfma_f32_16x16x32_bf16 v[52:55], a[40:43], a[144:147], v[52:55]\n"
      "ds_read_b128 a[28:31], v4 offset:22528\n"
      "v_cvt_pk_bf16_f32 v101, v46, v47\n"
      "v_mfma_f32_16x16x32_bf16 v[52:55], a[44:47], a[148:151], v[52:55]\n"
      "s_add_u32 m0, s23, 103168\n"
      "v_exp_f32 v48, v48\n"
      "v_mfma_f32_16x16x32_bf16 v[52:55], a[48:51], a[152:155], v[52:55]\n"
      "buffer_load_dwordx4 v2, s[40:43], s52 offen offset:256 nt lds\n"
      "v_mfma_f32_16x16x32_bf16 v[52:55], a[52:55], a[156:159], v[52:55]\n"
      "v_exp_f32 v49, v49\n"
      "v_mfma_f32_16x16x32_bf16 v[52:55], a[56:59], a[160:163], v[52:55]\n"
      "v_cvt_pk_bf16_f32 v86, v48, v49\n"
      "v_add_f32 v8, v8, v41\n"
      "v_mfma_f32_16x16x32_bf16 v[52:55], a[60:63], a[164:167], v[52:55]\n"
      "v_exp_f32 v50, v50\n"
      "v_mfma_f32_16x16x32_bf16 v[56:59], a[32:35], a[168:171], v[32:35]\n"
      "ds_read_b128 a[32:35], v3 offset:24576\n"
      "v_add_f32 v8, v8, v42\n"
      "v_mfma_f32_16x16x32_bf16 v[56:59], a[36:39], a[172:175], v[56:59]\n"
      "v_exp_f32 v51, v51\n"
      "v_mfma_f32_16x16x32_bf16 v[56:59], a[40:43], a[176:179], v[56:59]\n"
      "ds_read_b128 a[36:39], v4 offset:24576\n"
      "v_cvt_pk_bf16_f32 v87, v50, v51\n"
      "v_mfma_f32_16x16x32_bf16 v[56:59], a[44:47], a[180:183], v[56:59]\n"
      "v_exp_f32 v52, v52\n"
      "v_mfma_f32_16x16x32_bf16 v[56:59], a[48:51], a[184:187], v[56:59]\n"
      "ds_read_b128 a[40:43], v3 offset:26624\n"
      "v_add_f32 v8, v8, v43\n"
      "v_mfma_f32_16x16x32_bf16 v[56:59], a[52:55], a[188:191], v[56:59]\n"
      "v_exp_f32 v53, v53\n"
      "v_mfma_f32_16x16x32_bf16 v[56:59], a[56:59], a[192:195], v[56:59]\n"
      "s_add_u32 m0, s23, 104064\n"
      "ds_read_b128 a[44:47], v4 offset:26624\n"
      "v_cvt_pk_bf16_f32 v94, v52, v53\n"
      "v_mfma_f32_16x16x32_bf16 v[56:59], a[60:63], a[196:199], v[56:59]\n"
      "s_waitcnt lgkmcnt(4)\n"
      "v_exp_f32 v54, v54\n"
      "v_mfma_f32_16x16x32_bf16 v[60:63], a[0:3], a[104:107], v[24:27]\n"
      "buffer_load_dwordx4 v2, s[40:43], 0 offen offset:384 nt lds\n"
      "v_mfma_f32_16x16x32_bf16 v[60:63], a[4:7], a[108:111], v[60:63]\n"
      "v_exp_f32 v55, v55\n"
      "v_mfma_f32_16x16x32_bf16 v[60:63], a[8:11], a[112:115], v[60:63]\n"
      "ds_read_b128 a[48:51], v3 offset:28672\n"
      "v_cvt_pk_bf16_f32 v95, v54, v55\n"
      "v_mfma_f32_16x16x32_bf16 v[60:63], a[12:15], a[116:119], v[60:63]\n"
      "v_exp_f32 v56, v56\n"
      "v_mfma_f32_16x16x32_bf16 v[60:63], a[16:19], a[120:123], v[60:63]\n"
      "ds_read_b128 a[52:55], v4 offset:28672\n"
      "v_add_f32 v10, v10, v44\n"
      "v_mfma_f32_16x16x32_bf16 v[60:63], a[20:23], a[124:127], v[60:63]\n"
      "v_exp_f32 v57, v57\n"
      "v_mfma_f32_16x16x32_bf16 v[60:63], a[24:27], a[128:131], v[60:63]\n"
      "ds_read_b128 a[56:59], v3 offset:30720\n"
      "v_cvt_pk_bf16_f32 v102, v56, v57\n"
      "v_mfma_f32_16x16x32_bf16 v[60:63], a[28:31], a[132:135], v[60:63]\n"
      "s_add_u32 m0, s23, 105088\n"
      "v_exp_f32 v58, v58\n"
      "v_mfma_f32_16x16x32_bf16 v[64:67], a[0:3], a[136:139], v[28:31]\n"
      "buffer_load_dwordx4 v2, s[40:43], s52 offen offset:384 nt lds\n"
      "v_mfma_f32_16x16x32_bf16 v[64:67], a[4:7], a[140:143], v[64:67]\n"
      "v_exp_f32 v59, v59\n"
      "v_mfma_f32_16x16x32_bf16 v[64:67], a[8:11], a[144:147], v[64:67]\n"
      "ds_read_b128 a[60:63], v4 offset:30720\n"
      "v_cvt_pk_bf16_f32 v103, v58, v59\n"
      "v_mfma_f32_16x16x32_bf16 v[64:67], a[12:15], a[148:151], v[64:67]\n"
      "v_exp_f32 v60, v60\n"
      "v_mfma_f32_16x16x32_bf16 v[64:67], a[16:19], a[152:155], v[64:67]\n"
      "v_add_u32 v2, 0x8000, v2\n"
      "v_add_f32 v10, v10, v45\n"
      "v_mfma_f32_16x16x32_bf16 v[64:67], a[20:23], a[156:159], v[64:67]\n"
      "v_exp_f32 v61, v61\n"
      "v_mfma_f32_16x16x32_bf16 v[64:67], a[24:27], a[160:163], v[64:67]\n"
      "v_cvt_pk_bf16_f32 v88, v60, v61\n"
      "v_add_f32 v10, v10, v46\n"
      "v_mfma_f32_16x16x32_bf16 v[64:67], a[28:31], a[164:167], v[64:67]\n"
      "v_exp_f32 v62, v62\n"
      "v_mfma_f32_16x16x32_bf16 v[68:71], a[0:3], a[168:171], v[32:35]\n"
      "v_add_f32 v10, v10, v47\n"
      "v_add_f32 v6, v6, v48\n"
      "v_mfma_f32_16x16x32_bf16 v[68:71], a[4:7], a[172:175], v[68:71]\n"
      "v_exp_f32 v63, v63\n"
      "v_mfma_f32_16x16x32_bf16 v[68:71], a[8:11], a[176:179], v[68:71]\n"
      "s_waitcnt vmcnt(17) lgkmcnt(0)\n"
      "v_cvt_pk_bf16_f32 v89, v62, v63\n"
      "v_add_f32 v6, v6, v49\n"
      "v_mfma_f32_16x16x32_bf16 v[68:71], a[12:15], a[180:183], v[68:71]\n"
      "s_barrier\n"
      "v_exp_f32 v64, v64\n"
      "v_mfma_f32_16x16x32_bf16 v[68:71], a[16:19], a[184:187], v[68:71]\n"
      "ds_read_b64_tr_b16 a[64:65], v12 offset:0\n"
      "v_add_f32 v6, v6, v50\n"
      "v_mfma_f32_16x16x32_bf16 v[68:71], a[20:23], a[188:191], v[68:71]\n"
      "v_exp_f32 v65, v65\n"
      "v_mfma_f32_16x16x32_bf16 v[68:71], a[24:27], a[192:195], v[68:71]\n"
      "ds_read_b64_tr_b16 a[66:67], v12 offset:8192\n"
      "v_cvt_pk_bf16_f32 v96, v64, v65\n"
      "v_mfma_f32_16x16x32_bf16 v[68:71], a[28:31], a[196:199], v[68:71]\n"
      "v_exp_f32 v66, v66\n"
      "v_mfma_f32_16x16x32_bf16 v[72:75], a[32:35], a[104:107], v[24:27]\n"
      "ds_read_b64_tr_b16 a[68:69], v12 offset:16384\n"
      "v_add_f32 v6, v6, v51\n"
      "v_mfma_f32_16x16x32_bf16 v[72:75], a[36:39], a[108:111], v[72:75]\n"
      "v_exp_f32 v67, v67\n"
      "v_mfma_f32_16x16x32_bf16 v[72:75], a[40:43], a[112:115], v[72:75]\n"
      "ds_read_b64_tr_b16 a[70:71], v12 offset:24576\n"
      "v_cvt_pk_bf16_f32 v97, v66, v67\n"
      "v_mfma_f32_16x16x32_bf16 v[72:75], a[44:47], a[116:119], v[72:75]\n"
      "v_exp_f32 v68, v68\n"
      "v_mfma_f32_16x16x32_bf16 v[72:75], a[48:51], a[120:123], v[72:75]\n"
      "ds_read_b64_tr_b16 a[72:73], v13 offset:0\n"
      "v_add_f32 v8, v8, v52\n"
      "v_mfma_f32_16x16x32_bf16 v[72:75], a[52:55], a[124:127], v[72:75]\n"
      "v_exp_f32 v69, v69\n"
      "v_mfma_f32_16x16x32_bf16 v[72:75], a[56:59], a[128:131], v[72:75]\n"
      "ds_read_b64_tr_b16 a[74:75], v13 offset:8192\n"
      "v_cvt_pk_bf16_f32 v104, v68, v69\n"
      "v_mfma_f32_16x16x32_bf16 v[72:75], a[60:63], a[132:135], v[72:75]\n"
      "v_exp_f32 v70, v70\n"
      "v_mfma_f32_16x16x32_bf16 v[76:79], a[32:35], a[136:139], v[28:31]\n"
      "ds_read_b64_tr_b16 a[76:77], v13 offset:16384\n"
      "v_add_f32 v8, v8, v53\n"
      "v_mfma_f32_16x16x32_bf16 v[76:79], a[36:39], a[140:143], v[76:79]\n"
      "v_exp_f32 v71, v71\n"
      "v_mfma_f32_16x16x32_bf16 v[76:79], a[40:43], a[144:147], v[76:79]\n"
      "ds_read_b64_tr_b16 a[78:79], v13 offset:24576\n"
      "v_cvt_pk_bf16_f32 v105, v70, v71\n"
      "v_mfma_f32_16x16x32_bf16 v[76:79], a[44:47], a[148:151], v[76:79]\n"
      "v_exp_f32 v72, v72\n"
      "v_mfma_f32_16x16x32_bf16 v[76:79], a[48:51], a[152:155], v[76:79]\n"
      "ds_read_b64_tr_b16 a[80:81], v14 offset:0\n"
      "v_add_f32 v8, v8, v54\n"
      "v_mfma_f32_16x16x32_bf16 v[76:79], a[52:55], a[156:159], v[76:79]\n"
      "v_exp_f32 v73, v73\n"
      "v_mfma_f32_16x16x32_bf16 v[76:79], a[56:59], a[160:163], v[76:79]\n"
      "ds_read_b64_tr_b16 a[82:83], v14 offset:8192\n"
      "v_cvt_pk_bf16_f32 v90, v72, v73\n"
      "v_mfma_f32_16x16x32_bf16 v[76:79], a[60:63], a[164:167], v[76:79]\n"
      "v_exp_f32 v74, v74\n"
      "v_mfma_f32_16x16x32_bf16 v[80:83], a[32:35], a[168:171], v[32:35]\n"
      "ds_read_b64_tr_b16 a[84:85], v14 offset:16384\n"
      "v_add_f32 v8, v8, v55\n"
      "v_mfma_f32_16x16x32_bf16 v[80:83], a[36:39], a[172:175], v[80:83]\n"
      "v_exp_f32 v75, v75\n"
      "v_mfma_f32_16x16x32_bf16 v[80:83], a[40:43], a[176:179], v[80:83]\n"
      "ds_read_b64_tr_b16 a[86:87], v14 offset:24576\n"
      "v_cvt_pk_bf16_f32 v91, v74, v75\n"
      "v_mfma_f32_16x16x32_bf16 v[80:83], a[44:47], a[180:183], v[80:83]\n"
      "v_exp_f32 v76, v76\n"
      "v_mfma_f32_16x16x32_bf16 v[80:83], a[48:51], a[184:187], v[80:83]\n"
      "v_mfma_f32_16x16x32_bf16 v[80:83], a[52:55], a[188:191], v[80:83]\n"
      "v_exp_f32 v77, v77\n"
      "v_mfma_f32_16x16x32_bf16 v[80:83], a[56:59], a[192:195], v[80:83]\n"
      "v_cvt_pk_bf16_f32 v98, v76, v77\n"
      "v_mfma_f32_16x16x32_bf16 v[80:83], a[60:63], a[196:199], v[80:83]\n"
      "s_waitcnt lgkmcnt(8)\n"
      "v_exp_f32 v78, v78\n"
      "L_loopN0_%=:\n"
      "v_mfma_f32_16x16x32_bf16 a[200:203], v[84:87], a[64:67], a[200:203]\n"
      "ds_read_b64_tr_b16 a[88:89], v15 offset:0\n"
      "v_add_f32 v10, v10, v56\n"
      "v_mfma_f32_16x16x32_bf16 a[200:203], v[88:91], a[68:71], a[200:203]\n"
      "s_waitcnt lgkmcnt(5)\n"
      "v_exp_f32 v79, v79\n"
      "v_mfma_f32_16x16x32_bf16 a[212:215], v[84:87], a[72:75], a[212:215]\n"
      "s_add_u32 m0, s23, 0\n"
      "ds_read_b64_tr_b16 a[90:91], v15 offset:8192\n"
      "v_cvt_pk_bf16_f32 v99, v78, v79\n"
      "v_mfma_f32_16x16x32_bf16 a[212:215], v[88:91], a[76:79], a[212:215]\n"
      "s_waitcnt lgkmcnt(2)\n"
      "v_exp_f32 v80, v80\n"
      "v_mfma_f32_16x16x32_bf16 a[224:227], v[84:87], a[80:83], a[224:227]\n"
      "buffer_load_dwordx4 v1, s[36:39], 0 offen offset:0 nt lds\n"
      "v_mfma_f32_16x16x32_bf16 a[224:227], v[88:91], a[84:87], a[224:227]\n"
      "v_exp_f32 v81, v81\n"
      "v_mfma_f32_16x16x32_bf16 a[204:207], v[92:95], a[64:67], a[204:207]\n"
      "ds_read_b64_tr_b16 a[92:93], v15 offset:16384\n"
      "v_cvt_pk_bf16_f32 v106, v80, v81\n"
      "v_mfma_f32_16x16x32_bf16 a[204:207], v[96:99], a[68:71], a[204:207]\n"
      "v_exp_f32 v82, v82\n"
      "v_mfma_f32_16x16x32_bf16 a[216:219], v[92:95], a[72:75], a[216:219]\n"
      "ds_read_b64_tr_b16 a[94:95], v15 offset:24576\n"
      "v_add_f32 v10, v10, v57\n"
      "v_mfma_f32_16x16x32_bf16 a[216:219], v[96:99], a[76:79], a[216:219]\n"
      "v_exp_f32 v83, v83\n"
      "v_mfma_f32_16x16x32_bf16 a[228:231], v[92:95], a[80:83], a[228:231]\n"
      "ds_read_b64_tr_b16 a[96:97], v12 offset:2048\n"
      "v_cvt_pk_bf16_f32 v107, v82, v83\n"
      "v_mfma_f32_16x16x32_bf16 a[228:231], v[96:99], a[84:87], a[228:231]\n"
      "s_add_u32 m0, s23, 1024\n"
      "ds_read_b64_tr_b16 a[98:99], v12 offset:10240\n"
      "v_add_f32 v10, v10, v58\n"
      "v_mfma_f32_16x16x32_bf16 a[208:211], v[100:103], a[64:67], a[208:211]\n"
      "buffer_load_dwordx4 v1, s[36:39], s52 offen offset:0 nt lds\n"
      "v_mfma_f32_16x16x32_bf16 a[208:211], v[104:107], a[68:71], a[208:211]\n"
      "ds_read_b64_tr_b16 a[100:101], v12 offset:18432\n"
      "v_add_f32 v10, v10, v59\n"
      "v_mfma_f32_16x16x32_bf16 a[220:223], v[100:103], a[72:75], a[220:223]\n"
      "ds_read_b64_tr_b16 a[102:103], v12 offset:26624\n"
      "v_add_f32 v6, v6, v60\n"
      "v_mfma_f32_16x16x32_bf16 a[220:223], v[104:107], a[76:79], a[220:223]\n"
      "ds_read_b64_tr_b16 a[64:65], v13 offset:2048\n"
      "v_add_f32 v6, v6, v61\n"
      "v_mfma_f32_16x16x32_bf16 a[232:235], v[100:103], a[80:83], a[232:235]\n"
      "ds_read_b64_tr_b16 a[66:67], v13 offset:10240\n"
      "v_add_f32 v6, v6, v62\n"
      "v_mfma_f32_16x16x32_bf16 a[232:235], v[104:107], a[84:87], a[232:235]\n"
      "s_waitcnt lgkmcnt(6)\n"
      "ds_read_b64_tr_b16 a[68:69], v13 offset:18432\n"
      "v_add_f32 v6, v6, v63\n"
      "v_mfma_f32_16x16x32_bf16 a[236:239], v[84:87], a[88:91], a[236:239]\n"
      "ds_read_b64_tr_b16 a[70:71], v13 offset:26624\n"
      "v_add_f32 v8, v8, v64\n"
      "v_mfma_f32_16x16x32_bf16 a[236:239], v[88:91], a[92:95], a[236:239]\n"
      "s_add_u32 m0, s23, 1920\n"
      "ds_read_b64_tr_b16 a[72:73], v14 offset:2048\n"
      "v_add_f32 v8, v8, v65\n"
      "v_mfma_f32_16x16x32_bf16 a[240:243], v[92:95], a[88:91], a[240:243]\n"
      "buffer_load_dwordx4 v1, s[36:39], 0 offen offset:128 nt lds\n"
      "v_mfma_f32_16x16x32_bf16 a[240:243], v[96:99], a[92:95], a[240:243]\n"
      "ds_read_b64_tr_b16 a[74:75], v14 offset:10240\n"
      "v_add_f32 v8, v8, v66\n"
      "v_mfma_f32_16x16x32_bf16 v[108:111], v[100:103], a[88:91], v[108:111]\n"
      "ds_read_b64_tr_b16 a[76:77], v14 offset:18432\n"
      "v_add_f32 v8, v8, v67\n"
      "v_mfma_f32_16x16x32_bf16 v[108:111], v[104:107], a[92:95], v[108:111]\n"
      "s_waitcnt lgkmcnt(7)\n"
      "ds_read_b64_tr_b16 a[78:79], v14 offset:26624\n"
      "v_add_f32 v10, v10, v68\n"
      "v_mfma_f32_16x16x32_bf16 v[112:115], v[84:87], a[96:99], v[112:115]\n"
      "ds_read_b64_tr_b16 a[80:81], v15 offset:2048\n"
      "v_add_f32 v10, v10, v69\n"
      "v_mfma_f32_16x16x32_bf16 v[112:115], v[88:91], a[100:103], v[112:115]\n"
      "ds_read_b64_tr_b16 a[82:83], v15 offset:10240\n"
      "v_add_f32 v10, v10, v70\n"
      "v_mfma_f32_16x16x32_bf16 v[116:119], v[92:95], a[96:99], v[116:119]\n"
      "ds_read_b64_tr_b16 a[84:85], v15 offset:18432\n"
      "v_add_f32 v10, v10, v71\n"
      "v_mfma_f32_16x16x32_bf16 v[116:119], v[96:99], a[100:103], v[116:119]\n"
      "s_add_u32 m0, s23, 2944\n"
      "ds_read_b64_tr_b16 a[86:87], v15 offset:26624\n"
      "v_add_f32 v6, v6, v72\n"
      "v_mfma_f32_16x16x32_bf16 v[120:123], v[100:103], a[96:99], v[120:123]\n"
      "buffer_load_dwordx4 v1, s[36:39], s52 offen offset:128 nt lds\n"
      "v_mfma_f32_16x16x32_bf16 v[120:123], v[104:107], a[100:103], v[120:123]\n"
      "s_waitcnt lgkmcnt(8)\n"
      "ds_read_b64_tr_b16 a[88:89], v12 offset:4096\n"
      "v_add_f32 v6, v6, v73\n"
      "v_mfma_f32_16x16x32_bf16 v[124:127], v[84:87], a[64:67], v[124:127]\n"
      "ds_read_b64_tr_b16 a[90:91], v12 offset:12288\n"
      "v_add_f32 v6, v6, v74\n"
      "v_mfma_f32_16x16x32_bf16 v[124:127], v[88:91], a[68:71], v[124:127]\n"
      "ds_read_b64_tr_b16 a[92:93], v12 offset:20480\n"
      "v_add_f32 v6, v6, v75\n"
      "v_mfma_f32_16x16x32_bf16 v[128:131], v[92:95], a[64:67], v[128:131]\n"
      "ds_read_b64_tr_b16 a[94:95], v12 offset:28672\n"
      "v_add_f32 v8, v8, v76\n"
      "v_mfma_f32_16x16x32_bf16 v[128:131], v[96:99], a[68:71], v[128:131]\n"
      "ds_read_b64_tr_b16 a[96:97], v13 offset:4096\n"
      "v_add_f32 v8, v8, v77\n"
      "v_mfma_f32_16x16x32_bf16 v[132:135], v[100:103], a[64:67], v[132:135]\n"
      "s_add_u32 m0, s23, 3840\n"
      "ds_read_b64_tr_b16 a[98:99], v13 offset:12288\n"
      "v_add_f32 v8, v8, v78\n"
      "v_mfma_f32_16x16x32_bf16 v[132:135], v[104:107], a[68:71], v[132:135]\n"
      "s_waitcnt lgkmcnt(10)\n"
      "ds_read_b64_tr_b16 a[100:101], v13 offset:20480\n"
      "v_add_f32 v8, v8, v79\n"
      "v_mfma_f32_16x16x32_bf16 v[136:139], v[84:87], a[72:75], v[136:139]\n"
      "buffer_load_dwordx4 v1, s[36:39], 0 offen offset:256 nt lds\n"
      "v_mfma_f32_16x16x32_bf16 v[136:139], v[88:91], a[76:79], v[136:139]\n"
      "ds_read_b64_tr_b16 a[102:103], v13 offset:28672\n"
      "v_add_f32 v10, v10, v80\n"
      "v_mfma_f32_16x16x32_bf16 v[140:143], v[92:95], a[72:75], v[140:143]\n"
      "ds_read_b64_tr_b16 a[64:65], v14 offset:4096\n"
      "v_add_f32 v10, v10, v81\n"
      "v_mfma_f32_16x16x32_bf16 v[140:143], v[96:99], a[76:79], v[140:143]\n"
      "ds_read_b64_tr_b16 a[66:67], v14 offset:12288\n"
      "v_add_f32 v10, v10, v82\n"
      "v_mfma_f32_16x16x32_bf16 v[144:147], v[100:103], a[72:75], v[144:147]\n"
      "ds_read_b64_tr_b16 a[68:69], v14 offset:20480\n"
      "v_add_f32 v10, v10, v83\n"
      "v_mfma_f32_16x16x32_bf16 v[144:147], v[104:107], a[76:79], v[144:147]\n"
      "s_waitcnt lgkmcnt(11)\n"
      "ds_read_b64_tr_b16 a[70:71], v14 offset:28672\n"
      "v_mfma_f32_16x16x32_bf16 v[148:151], v[84:87], a[80:83], v[148:151]\n"
      "ds_read_b64_tr_b16 a[72:73], v15 offset:4096\n"
      "v_mfma_f32_16x16x32_bf16 v[148:151], v[88:91], a[84:87], v[148:151]\n"
      "ds_read_b64_tr_b16 a[74:75], v15 offset:12288\n"
      "v_mfma_f32_16x16x32_bf16 v[152:155], v[92:95], a[80:83], v[152:155]\n"
      "ds_read_b64_tr_b16 a[76:77], v15 offset:20480\n"
      "v_mfma_f32_16x16x32_bf16 v[152:155], v[96:99], a[84:87], v[152:155]\n"
      "ds_read_b64_tr_b16 a[78:79], v15 offset:28672\n"
      "v_mfma_f32_16x16x32_bf16 v[156:159], v[100:103], a[80:83], v[156:159]\n"
      "s_add_u32 m0, s23, 4864\n"
      "v_mfma_f32_16x16x32_bf16 v[156:159], v[104:107], a[84:87], v[156:159]\n"
      "s_waitcnt lgkmcnt(12)\n"
      "ds_read_b64_tr_b16 a[80:81], v12 offset:6144\n"
      "v_mfma_f32_16x16x32_bf16 v[160:163], v[84:87], a[88:91], v[160:163]\n"
      "buffer_load_dwordx4 v1, s[36:39], s52 offen offset:256 nt lds\n"
      "v_mfma_f32_16x16x32_bf16 v[160:163], v[88:91], a[92:95], v[160:163]\n"
      "ds_read_b64_tr_b16 a[82:83], v12 offset:14336\n"
      "v_mfma_f32_16x16x32_bf16 v[164:167], v[92:95], a[88:91], v[164:167]\n"
      "ds_read_b64_tr_b16 a[84:85], v12 offset:22528\n"
      "v_mfma_f32_16x16x32_bf16 v[164:167], v[96:99], a[92:95], v[164:167]\n"
      "ds_read_b64_tr_b16 a[86:87], v12 offset:30720\n"
      "v_mfma_f32_16x16x32_bf16 v[168:171], v[100:103], a[88:91], v[168:171]\n"
      "v_mfma_f32_16x16x32_bf16 v[168:171], v[104:107], a[92:95], v[168:171]\n"
      "s_waitcnt lgkmcnt(12)\n"
      "ds_read_b64_tr_b16 a[88:89], v13 offset:6144\n"
      "v_mfma_f32_16x16x32_bf16 v[172:175], v[84:87], a[96:99], v[172:175]\n"
      "ds_read_b64_tr_b16 a[90:91], v13 offset:14336\n"
      "v_mfma_f32_16x16x32_bf16 v[172:175], v[88:91], a[100:103], v[172:175]\n"
      "s_add_u32 m0, s23, 5760\n"
      "ds_read_b64_tr_b16 a[92:93], v13 offset:22528\n"
      "v_mfma_f32_16x16x32_bf16 v[176:179], v[92:95], a[96:99], v[176:179]\n"
      "buffer_load_dwordx4 v1, s[36:39], 0 offen offset:384 nt lds\n"
      "v_mfma_f32_16x16x32_bf16 v[176:179], v[96:99], a[100:103], v[176:179]\n"
      "ds_read_b64_tr_b16 a[94:95], v13 offset:30720\n"
      "v_mfma_f32_16x16x32_bf16 v[180:183], v[100:103], a[96:99], v[180:183]\n"
      "v_mfma_f32_16x16x32_bf16 v[180:183], v[104:107], a[100:103], v[180:183]\n"
      "s_waitcnt lgkmcnt(12)\n"
      "ds_read_b64_tr_b16 a[96:97], v14 offset:6144\n"
      "v_mfma_f32_16x16x32_bf16 v[184:187], v[84:87], a[64:67], v[184:187]\n"
      "ds_read_b64_tr_b16 a[98:99], v14 offset:14336\n"
      "v_mfma_f32_16x16x32_bf16 v[184:187], v[88:91], a[68:71], v[184:187]\n"
      "ds_read_b64_tr_b16 a[100:101], v14 offset:22528\n"
      "v_mfma_f32_16x16x32_bf16 v[188:191], v[92:95], a[64:67], v[188:191]\n"
      "ds_read_b64_tr_b16 a[102:103], v14 offset:30720\n"
      "v_mfma_f32_16x16x32_bf16 v[188:191], v[96:99], a[68:71], v[188:191]\n"
      "s_add_u32 m0, s23, 6784\n"
      "v_mfma_f32_16x16x32_bf16 v[192:195], v[100:103], a[64:67], v[192:195]\n"
      "buffer_load_dwordx4 v1, s[36:39], s52 offen offset:384 nt lds\n"
      "v_mfma_f32_16x16x32_bf16 v[192:195], v[104:107], a[68:71], v[192:195]\n"
      "s_waitcnt lgkmcnt(12)\n"
      "ds_read_b64_tr_b16 a[64:65], v15 offset:6144\n"
      "v_add_u32 v1, 0x8000, v1\n"
      "v_mfma_f32_16x16x32_bf16 v[196:199], v[84:87], a[72:75], v[196:199]\n"
      "ds_read_b64_tr_b16 a[66:67], v15 offset:14336\n"
      "v_mfma_f32_16x16x32_bf16 v[196:199], v[88:91], a[76:79], v[196:199]\n"
      "ds_read_b64_tr_b16 a[68:69], v15 offset:22528\n"
      "v_mfma_f32_16x16x32_bf16 v[200:203], v[92:95], a[72:75], v[200:203]\n"
      "buffer_load_dword v23, v22, s[60:63], 0 offen\n"
      "v_mfma_f32_16x16x32_bf16 v[200:203], v[96:99], a[76:79], v[200:203]\n"
      "ds_read_b64_tr_b16 a[70:71], v15 offset:30720\n"
      "v_add_u32 v22, 0x400, v22\n"
      "v_mfma_f32_16x16x32_bf16 v[204:207], v[100:103], a[72:75], v[204:207]\n"
      "v_mfma_f32_16x16x32_bf16 v[204:207], v[104:107], a[76:79], v[204:207]\n"
      "s_waitcnt lgkmcnt(12)\n"
      "v_mfma_f32_16x16x32_bf16 v[208:211], v[84:87], a[80:83], v[208:211]\n"
      "v_mfma_f32_16x16x32_bf16 v[208:211], v[88:91], a[84:87], v[208:211]\n"
      "v_mfma_f32_16x16x32_bf16 v[212:215], v[92:95], a[80:83], v[212:215]\n"
      "v_mfma_f32_16x16x32_bf16 v[212:215], v[96:99], a[84:87], v[212:215]\n"
      "L_midN0_%=:\n"
      "v_mfma_f32_16x16x32_bf16 v[216:219], v[100:103], a[80:83], v[216:219]\n"
      "v_mfma_f32_16x16x32_bf16 v[216:219], v[104:107], a[84:87], v[216:219]\n"
      "s_waitcnt lgkmcnt(8)\n"
      "v_mfma_f32_16x16x32_bf16 v[220:223], v[84:87], a[88:91], v[220:223]\n"
      "s_waitcnt vmcnt(17) lgkmcnt(0)\n"
      "v_mfma_f32_16x16x32_bf16 v[220:223], v[88:91], a[92:95], v[220:223]\n"
      "s_barrier\n"
      "ds_read_b128 a[0:3], v3 offset:32768\n"
      "v_mfma_f32_16x16x32_bf16 v[224:227], v[92:95], a[88:91], v[224:227]\n"
      "ds_read_b128 a[4:7], v4 offset:32768\n"
      "v_mfma_f32_16x16x32_bf16 v[224:227], v[96:99], a[92:95], v[224:227]\n"
      "ds_read_b128 a[8:11], v3 offset:34816\n"
      "v_mfma_f32_16x16x32_bf16 v[228:231], v[100:103], a[88:91], v[228:231]\n"
      "ds_read_b128 a[12:15], v4 offset:34816\n"
      "v_mfma_f32_16x16x32_bf16 v[228:231], v[104:107], a[92:95], v[228:231]\n"
      "s_add_u32 m0, s23, 65536\n"
      "ds_read_b128 a[16:19], v3 offset:36864\n"
      "v_mfma_f32_16x16x32_bf16 v[232:235], v[84:87], a[96:99], v[232:235]\n"
      "buffer_load_dwordx4 v2, s[40:43], 0 offen offset:0 nt lds\n"
      "v_mfma_f32_16x16x32_bf16 v[232:235], v[88:91], a[100:103], v[232:235]\n"
      "ds_read_b128 a[20:23], v4 offset:36864\n"
      "v_mfma_f32_16x16x32_bf16 v[236:239], v[92:95], a[96:99], v[236:239]\n"
      "ds_read_b128 a[24:27], v3 offset:38912\n"
      "v_mfma_f32_16x16x32_bf16 v[236:239], v[96:99], a[100:103], v[236:239]\n"
      "ds_read_b128 a[28:31], v4 offset:38912\n"
      "v_mfma_f32_16x16x32_bf16 v[240:243], v[100:103], a[96:99], v[240:243]\n"
      "ds_read_b128 a[32:35], v3 offset:40960\n"
      "v_mfma_f32_16x16x32_bf16 v[240:243], v[104:107], a[100:103], v[240:243]\n"
      "ds_read_b128 a[36:39], v4 offset:40960\n"
      "v_mfma_f32_16x16x32_bf16 v[244:247], v[84:87], a[64:67], v[244:247]\n"
      "ds_read_b128 a[40:43], v3 offset:43008\n"
      "v_mfma_f32_16x16x32_bf16 v[244:247], v[88:91], a[68:71], v[244:247]\n"
      "s_add_u32 m0, s23, 66560\n"
      "ds_read_b128 a[44:47], v4 offset:43008\n"
      "v_mfma_f32_16x16x32_bf16 v[248:251], v[92:95], a[64:67], v[248:251]\n"
      "buffer_load_dwordx4 v2, s[40:43], s52 offen offset:0 nt lds\n"
      "v_mfma_f32_16x16x32_bf16 v[248:251], v[96:99], a[68:71], v[248:251]\n"
      "ds_read_b128 a[48:51], v3 offset:45056\n"
      "v_mfma_f32_16x16x32_bf16 v[252:255], v[100:103], a[64:67], v[252:255]\n"
      "ds_read_b128 a[52:55], v4 offset:45056\n"
      "v_mfma_f32_16x16x32_bf16 v[252:255], v[104:107], a[68:71], v[252:255]\n"
      "s_waitcnt lgkmcnt(6)\n"
      "ds_read_b128 a[56:59], v3 offset:47104\n"
      "v_mfma_f32_16x16x32_bf16 v[36:39], a[0:3], a[104:107], v[24:27]\n"
      "ds_read_b128 a[60:63], v4 offset:47104\n"
      "v_mfma_f32_16x16x32_bf16 v[36:39], a[4:7], a[108:111], v[36:39]\n"
      "v_mfma_f32_16x16x32_bf16 v[36:39], a[8:11], a[112:115], v[36:39]\n"
      "v_mfma_f32_16x16x32_bf16 v[36:39], a[12:15], a[116:119], v[36:39]\n"
      "v_mfma_f32_16x16x32_bf16 v[36:39], a[16:19], a[120:123], v[36:39]\n"
      "v_mfma_f32_16x16x32_bf16 v[36:39], a[20:23], a[124:127], v[36:39]\n"
      "v_mfma_f32_16x16x32_bf16 v[36:39], a[24:27], a[128:131], v[36:39]\n"
      "v_mfma_f32_16x16x32_bf16 v[36:39], a[28:31], a[132:135], v[36:39]\n"
      "s_add_u32 m0, s23, 67456\n"
      "v_mfma_f32_16x16x32_bf16 v[40:43], a[0:3], a[136:139], v[28:31]\n"
      "buffer_load_dwordx4 v2, s[40:43], 0 offen offset:128 nt lds\n"
      "v_mfma_f32_16x16x32_bf16 v[40:43], a[4:7], a[140:143], v[40:43]\n"
      "v_mfma_f32_16x16x32_bf16 v[40:43], a[8:11], a[144:147], v[40:43]\n"
      "v_mfma_f32_16x16x32_bf16 v[40:43], a[12:15], a[148:151], v[40:43]\n"
      "v_exp_f32 v36, v36\n"
      "v_mfma_f32_16x16x32_bf16 v[40:43], a[16:19], a[152:155], v[40:43]\n"
      "v_add_f32 v6, v6, v36\n"
      "v_mfma_f32_16x16x32_bf16 v[40:43], a[20:23], a[156:159], v[40:43]\n"
      "v_exp_f32 v37, v37\n"
      "v_mfma_f32_16x16x32_bf16 v[40:43], a[24:27], a[160:163], v[40:43]\n"
      "v_cvt_pk_bf16_f32 v84, v36, v37\n"
      "v_add_f32 v6, v6, v37\n"
      "v_mfma_f32_16x16x32_bf16 v[40:43], a[28:31], a[164:167], v[40:43]\n"
      "s_add_u32 m0, s23, 68480\n"
      "v_exp_f32 v38, v38\n"
      "v_mfma_f32_16x16x32_bf16 v[44:47], a[0:3], a[168:171], v[32:35]\n"
      "buffer_load_dwordx4 v2, s[40:43], s52 offen offset:128 nt lds\n"
      "v_mfma_f32_16x16x32_bf16 v[44:47], a[4:7], a[172:175], v[44:47]\n"
      "v_exp_f32 v39, v39\n"
      "v_mfma_f32_16x16x32_bf16 v[44:47], a[8:11], a[176:179], v[44:47]\n"
      "ds_read_b128 a[0:3], v3 offset:49152\n"
      "v_cvt_pk_bf16_f32 v85, v38, v39\n"
      "v_mfma_f32_16x16x32_bf16 v[44:47], a[12:15], a[180:183], v[44:47]\n"
      "v_exp_f32 v40, v40\n"
      "v_mfma_f32_16x16x32_bf16 v[44:47], a[16:19], a[184:187], v[44:47]\n"
      "ds_read_b128 a[4:7], v4 offset:49152\n"
      "v_add_f32 v6, v6, v38\n"
      "v_mfma_f32_16x16x32_bf16 v[44:47], a[20:23], a[188:191], v[44:47]\n"
      "v_exp_f32 v41, v41\n"
      "v_mfma_f32_16x16x32_bf16 v[44:47], a[24:27], a[192:195], v[44:47]\n"
      "ds_read_b128 a[8:11], v3 offset:51200\n"
      "v_cvt_pk_bf16_f32 v92, v40, v41\n"
      "v_mfma_f32_16x16x32_bf16 v[44:47], a[28:31], a[196:199], v[44:47]\n"
      "s_waitcnt lgkmcnt(3)\n"
      "v_exp_f32 v42, v42\n"
      "v_mfma_f32_16x16x32_bf16 v[48:51], a[32:35], a[104:107], v[24:27]\n"
      "ds_read_b128 a[12:15], v4 offset:51200\n"
      "v_add_f32 v6, v6, v39\n"
      "v_mfma_f32_16x16x32_bf16 v[48:51], a[36:39], a[108:111], v[48:51]\n"
      "v_exp_f32 v43, v43\n"
      "v_mfma_f32_16x16x32_bf16 v[48:51], a[40:43], a[112:115], v[48:51]\n"
      "ds_read_b128 a[16:19], v3 offset:53248\n"
      "v_cvt_pk_bf16_f32 v93, v42, v43\n"
      "v_mfma_f32_16x16x32_bf16 v[48:51], a[44:47], a[116:119], v[48:51]\n"
      "s_add_u32 m0, s23, 69376\n"
      "v_exp_f32 v44, v44\n"
      "v_mfma_f32_16x16x32_bf16 v[48:51], a[48:51], a[120:123], v[48:51]\n"
      "buffer_load_dwordx4 v2, s[40:43], 0 offen offset:256 nt lds\n"
      "v_mfma_f32_16x16x32_bf16 v[48:51], a[52:55], a[124:127], v[48:51]\n"
      "v_exp_f32 v45, v45\n"
      "v_mfma_f32_16x16x32_bf16 v[48:51], a[56:59], a[128:131], v[48:51]\n"
      "ds_read_b128 a[20:23], v4 offset:53248\n"
      "v_cvt_pk_bf16_f32 v100, v44, v45\n"
      "v_mfma_f32_16x16x32_bf16 v[48:51], a[60:63], a[132:135], v[48:51]\n"
      "v_exp_f32 v46, v46\n"
      "v_mfma_f32_16x16x32_bf16 v[52:55], a[32:35], a[136:139], v[28:31]\n"
      "ds_read_b128 a[24:27], v3 offset:55296\n"
      "v_add_f32 v8, v8, v40\n"
      "v_mfma_f32_16x16x32_bf16 v[52:55], a[36:39], a[140:143], v[52:55]\n"
      "v_exp_f32 v47, v47\n"
      "v_mfma_f32_16x16x32_bf16 v[52:55], a[40:43], a[144:147], v[52:55]\n"
      "ds_read_b128 a[28:31], v4 offset:55296\n"
      "v_cvt_pk_bf16_f32 v101, v46, v47\n"
      "v_mfma_f32_16x16x32_bf16 v[52:55], a[44:47], a[148:151], v[52:55]\n"
      "s_add_u32 m0, s23, 70400\n"
      "v_exp_f32 v48, v48\n"
      "v_mfma_f32_16x16x32_bf16 v[52:55], a[48:51], a[152:155], v[52:55]\n"
      "buffer_load_dwordx4 v2, s[40:43], s52 offen offset:256 nt lds\n"
      "v_mfma_f32_16x16x32_bf16 v[52:55], a[52:55], a[156:159], v[52:55]\n"
      "v_exp_f32 v49, v49\n"
      "v_mfma_f32_16x16x32_bf16 v[52:55], a[56:59], a[160:163], v[52:55]\n"
      "v_cvt_pk_bf16_f32 v86, v48, v49\n"
      "v_add_f32 v8, v8, v41\n"
      "v_mfma_f32_16x16x32_bf16 v[52:55], a[60:63], a[164:167], v[52:55]\n"
      "v_exp_f32 v50, v50\n"
      "v_mfma_f32_16x16x32_bf16 v[56:59], a[32:35], a[168:171], v[32:35]\n"
      "ds_read_b128 a[32:35], v3 offset:57344\n"
      "v_add_f32 v8, v8, v42\n"
      "v_mfma_f32_16x16x32_bf16 v[56:59], a[36:39], a[172:175], v[56:59]\n"
      "v_exp_f32 v51, v51\n"
      "v_mfma_f32_16x16x32_bf16 v[56:59], a[40:43], a[176:179], v[56:59]\n"
      "ds_read_b128 a[36:39], v4 offset:57344\n"
      "v_cvt_pk_bf16_f32 v87, v50, v51\n"
      "v_mfma_f32_16x16x32_bf16 v[56:59], a[44:47], a[180:183], v[56:59]\n"
      "v_exp_f32 v52, v52\n"
      "v_mfma_f32_16x16x32_bf16 v[56:59], a[48:51], a[184:187], v[56:59]\n"
      "ds_read_b128 a[40:43], v3 offset:59392\n"
      "v_add_f32 v8, v8, v43\n"
      "v_mfma_f32_16x16x32_bf16 v[56:59], a[52:55], a[188:191], v[56:59]\n"
      "v_exp_f32 v53, v53\n"
      "v_mfma_f32_16x16x32_bf16 v[56:59], a[56:59], a[192:195], v[56:59]\n"
      "s_add_u32 m0, s23, 71296\n"
      "ds_read_b128 a[44:47], v4 offset:59392\n"
      "v_cvt_pk_bf16_f32 v94, v52, v53\n"
      "v_mfma_f32_16x16x32_bf16 v[56:59], a[60:63], a[196:199], v[56:59]\n"
      "s_waitcnt lgkmcnt(4)\n"
      "v_exp_f32 v54, v54\n"
      "v_mfma_f32_16x16x32_bf16 v[60:63], a[0:3], a[104:107], v[24:27]\n"
      "buffer_load_dwordx4 v2, s[40:43], 0 offen offset:384 nt lds\n"
      "v_mfma_f32_16x16x32_bf16 v[60:63], a[4:7], a[108:111], v[60:63]\n"
      "v_exp_f32 v55, v55\n"
      "v_mfma_f32_16x16x32_bf16 v[60:63], a[8:11], a[112:115], v[60:63]\n"
      "ds_read_b128 a[48:51], v3 offset:61440\n"
      "v_cvt_pk_bf16_f32 v95, v54, v55\n"
      "v_mfma_f32_16x16x32_bf16 v[60:63], a[12:15], a[116:119], v[60:63]\n"
      "v_exp_f32 v56, v56\n"
      "v_mfma_f32_16x16x32_bf16 v[60:63], a[16:19], a[120:123], v[60:63]\n"
      "ds_read_b128 a[52:55], v4 offset:61440\n"
      "v_add_f32 v10, v10, v44\n"
      "v_mfma_f32_16x16x32_bf16 v[60:63], a[20:23], a[124:127], v[60:63]\n"
      "v_exp_f32 v57, v57\n"
      "v_mfma_f32_16x16x32_bf16 v[60:63], a[24:27], a[128:131], v[60:63]\n"
      "ds_read_b128 a[56:59], v3 offset:63488\n"
      "v_cvt_pk_bf16_f32 v102, v56, v57\n"
      "v_mfma_f32_16x16x32_bf16 v[60:63], a[28:31], a[132:135], v[60:63]\n"
      "s_add_u32 m0, s23, 72320\n"
      "v_exp_f32 v58, v58\n"
      "v_mfma_f32_16x16x32_bf16 v[64:67], a[0:3], a[136:139], v[28:31]\n"
      "buffer_load_dwordx4 v2, s[40:43], s52 offen offset:384 nt lds\n"
      "v_mfma_f32_16x16x32_bf16 v[64:67], a[4:7], a[140:143], v[64:67]\n"
      "v_exp_f32 v59, v59\n"
      "v_mfma_f32_16x16x32_bf16 v[64:67], a[8:11], a[144:147], v[64:67]\n"
      "ds_read_b128 a[60:63], v4 offset:63488\n"
      "v_cvt_pk_bf16_f32 v103, v58, v59\n"
      "v_mfma_f32_16x16x32_bf16 v[64:67], a[12:15], a[148:151], v[64:67]\n"
      "v_exp_f32 v60, v60\n"
      "v_mfma_f32_16x16x32_bf16 v[64:67], a[16:19], a[152:155], v[64:67]\n"
      "v_add_u32 v2, 0x8000, v2\n"
      "v_add_f32 v10, v10, v45\n"
      "v_mfma_f32_16x16x32_bf16 v[64:67], a[20:23], a[156:159], v[64:67]\n"
      "v_exp_f32 v61, v61\n"
      "v_mfma_f32_16x16x32_bf16 v[64:67], a[24:27], a[160:163], v[64:67]\n"
      "v_cvt_pk_bf16_f32 v88, v60, v61\n"
      "v_add_f32 v10, v10, v46\n"
      "v_mfma_f32_16x16x32_bf16 v[64:67], a[28:31], a[164:167], v[64:67]\n"
      "v_exp_f32 v62, v62\n"
      "v_mfma_f32_16x16x32_bf16 v[68:71], a[0:3], a[168:171], v[32:35]\n"
      "v_add_f32 v10, v10, v47\n"
      "v_add_f32 v6, v6, v48\n"
      "v_mfma_f32_16x16x32_bf16 v[68:71], a[4:7], a[172:175], v[68:71]\n"
      "v_exp_f32 v63, v63\n"
      "v_mfma_f32_16x16x32_bf16 v[68:71], a[8:11], a[176:179], v[68:71]\n"
      "s_waitcnt vmcnt(17) lgkmcnt(0)\n"
      "v_cvt_pk_bf16_f32 v89, v62, v63\n"
      "v_add_f32 v6, v6, v49\n"
      "v_mfma_f32_16x16x32_bf16 v[68:71], a[12:15], a[180:183], v[68:71]\n"
      "s_barrier\n"
      "v_exp_f32 v64, v64\n"
      "v_mfma_f32_16x16x32_bf16 v[68:71], a[16:19], a[184:187], v[68:71]\n"
      "ds_read_b64_tr_b16 a[64:65], v12 offset:32768\n"
      "v_add_f32 v6, v6, v50\n"
      "v_mfma_f32_16x16x32_bf16 v[68:71], a[20:23], a[188:191], v[68:71]\n"
      "v_exp_f32 v65, v65\n"
      "v_mfma_f32_16x16x32_bf16 v[68:71], a[24:27], a[192:195], v[68:71]\n"
      "ds_read_b64_tr_b16 a[66:67], v12 offset:40960\n"
      "v_cvt_pk_bf16_f32 v96, v64, v65\n"
      "v_mfma_f32_16x16x32_bf16 v[68:71], a[28:31], a[196:199], v[68:71]\n"
      "v_exp_f32 v66, v66\n"
      "v_mfma_f32_16x16x32_bf16 v[72:75], a[32:35], a[104:107], v[24:27]\n"
      "ds_read_b64_tr_b16 a[68:69], v12 offset:49152\n"
      "v_add_f32 v6, v6, v51\n"
      "v_mfma_f32_16x16x32_bf16 v[72:75], a[36:39], a[108:111], v[72:75]\n"
      "v_exp_f32 v67, v67\n"
      "v_mfma_f32_16x16x32_bf16 v[72:75], a[40:43], a[112:115], v[72:75]\n"
      "ds_read_b64_tr_b16 a[70:71], v12 offset:57344\n"
      "v_cvt_pk_bf16_f32 v97, v66, v67\n"
      "v_mfma_f32_16x16x32_bf16 v[72:75], a[44:47], a[116:119], v[72:75]\n"
      "v_exp_f32 v68, v68\n"
      "v_mfma_f32_16x16x32_bf16 v[72:75], a[48:51], a[120:123], v[72:75]\n"
      "ds_read_b64_tr_b16 a[72:73], v13 offset:32768\n"
      "v_add_f32 v8, v8, v52\n"
      "v_mfma_f32_16x16x32_bf16 v[72:75], a[52:55], a[124:127], v[72:75]\n"
      "v_exp_f32 v69, v69\n"
      "v_mfma_f32_16x16x32_bf16 v[72:75], a[56:59], a[128:131], v[72:75]\n"
      "ds_read_b64_tr_b16 a[74:75], v13 offset:40960\n"
      "v_cvt_pk_bf16_f32 v104, v68, v69\n"
      "v_mfma_f32_16x16x32_bf16 v[72:75], a[60:63], a[132:135], v[72:75]\n"
      "v_exp_f32 v70, v70\n"
      "v_mfma_f32_16x16x32_bf16 v[76:79], a[32:35], a[136:139], v[28:31]\n"
      "ds_read_b64_tr_b16 a[76:77], v13 offset:49152\n"
      "v_add_f32 v8, v8, v53\n"
      "v_mfma_f32_16x16x32_bf16 v[76:79], a[36:39], a[140:143], v[76:79]\n"
      "v_exp_f32 v71, v71\n"
      "v_mfma_f32_16x16x32_bf16 v[76:79], a[40:43], a[144:147], v[76:79]\n"
      "ds_read_b64_tr_b16 a[78:79], v13 offset:57344\n"
      "v_cvt_pk_bf16_f32 v105, v70, v71\n"
      "v_mfma_f32_16x16x32_bf16 v[76:79], a[44:47], a[148:151], v[76:79]\n"
      "v_exp_f32 v72, v72\n"
      "v_mfma_f32_16x16x32_bf16 v[76:79], a[48:51], a[152:155], v[76:79]\n"
      "ds_read_b64_tr_b16 a[80:81], v14 offset:32768\n"
      "v_add_f32 v8, v8, v54\n"
      "v_mfma_f32_16x16x32_bf16 v[76:79], a[52:55], a[156:159], v[76:79]\n"
      "v_exp_f32 v73, v73\n"
      "v_mfma_f32_16x16x32_bf16 v[76:79], a[56:59], a[160:163], v[76:79]\n"
      "ds_read_b64_tr_b16 a[82:83], v14 offset:40960\n"
      "v_cvt_pk_bf16_f32 v90, v72, v73\n"
      "v_mfma_f32_16x16x32_bf16 v[76:79], a[60:63], a[164:167], v[76:79]\n"
      "v_exp_f32 v74, v74\n"
      "v_mfma_f32_16x16x32_bf16 v[80:83], a[32:35], a[168:171], v[32:35]\n"
      "ds_read_b64_tr_b16 a[84:85], v14 offset:49152\n"
      "v_add_f32 v8, v8, v55\n"
      "v_mfma_f32_16x16x32_bf16 v[80:83], a[36:39], a[172:175], v[80:83]\n"
      "v_exp_f32 v75, v75\n"
      "v_mfma_f32_16x16x32_bf16 v[80:83], a[40:43], a[176:179], v[80:83]\n"
      "ds_read_b64_tr_b16 a[86:87], v14 offset:57344\n"
      "v_cvt_pk_bf16_f32 v91, v74, v75\n"
      "v_mfma_f32_16x16x32_bf16 v[80:83], a[44:47], a[180:183], v[80:83]\n"
      "v_exp_f32 v76, v76\n"
      "v_mfma_f32_16x16x32_bf16 v[80:83], a[48:51], a[184:187], v[80:83]\n"
      "v_mfma_f32_16x16x32_bf16 v[80:83], a[52:55], a[188:191], v[80:83]\n"
      "v_exp_f32 v77, v77\n"
      "v_mfma_f32_16x16x32_bf16 v[80:83], a[56:59], a[192:195], v[80:83]\n"
      "s_sub_u32 s20, s20, 1\n"
      "v_cvt_pk_bf16_f32 v98, v76, v77\n"
      "v_mfma_f32_16x16x32_bf16 v[80:83], a[60:63], a[196:199], v[80:83]\n"
      "s_waitcnt lgkmcnt(8)\n"
      "v_exp_f32 v78, v78\n"
      "s_cbranch_scc1 L_tail_%=\n"
      "v_mfma_f32_16x16x32_bf16 a[200:203], v[84:87], a[64:67], a[200:203]\n"
      "ds_read_b64_tr_b16 a[88:89], v15 offset:32768\n"
      "v_add_f32 v10, v10, v56\n"
      "v_mfma_f32_16x16x32_bf16 a[200:203], v[88:91], a[68:71], a[200:203]\n"
      "s_waitcnt lgkmcnt(5)\n"
      "v_exp_f32 v79, v79\n"
      "v_mfma_f32_16x16x32_bf16 a[212:215], v[84:87], a[72:75], a[212:215]\n"
      "s_add_u32 m0, s23, 32768\n"
      "ds_read_b64_tr_b16 a[90:91], v15 offset:40960\n"
      "v_cvt_pk_bf16_f32 v99, v78, v79\n"
      "v_mfma_f32_16x16x32_bf16 a[212:215], v[88:91], a[76:79], a[212:215]\n"
      "s_waitcnt lgkmcnt(2)\n"
      "v_exp_f32 v80, v80\n"
      "v_mfma_f32_16x16x32_bf16 a[224:227], v[84:87], a[80:83], a[224:227]\n"
      "buffer_load_dwordx4 v1, s[36:39], 0 offen offset:0 nt lds\n"
      "v_mfma_f32_16x16x32_bf16 a[224:227], v[88:91], a[84:87], a[224:227]\n"
      "v_exp_f32 v81, v81\n"
      "v_mfma_f32_16x16x32_bf16 a[204:207], v[92:95], a[64:67], a[204:207]\n"
      "ds_read_b64_tr_b16 a[92:93], v15 offset:49152\n"
      "v_cvt_pk_bf16_f32 v106, v80, v81\n"
      "v_mfma_f32_16x16x32_bf16 a[204:207], v[96:99], a[68:71], a[204:207]\n"
      "v_exp_f32 v82, v82\n"
      "v_mfma_f32_16x16x32_bf16 a[216:219], v[92:95], a[72:75], a[216:219]\n"
      "ds_read_b64_tr_b16 a[94:95], v15 offset:57344\n"
      "v_add_f32 v10, v10, v57\n"
      "v_mfma_f32_16x16x32_bf16 a[216:219], v[96:99], a[76:79], a[216:219]\n"
      "v_exp_f32 v83, v83\n"
      "v_mfma_f32_16x16x32_bf16 a[228:231], v[92:95], a[80:83], a[228:231]\n"
      "ds_read_b64_tr_b16 a[96:97], v12 offset:34816\n"
      "v_cvt_pk_bf16_f32 v107, v82, v83\n"
      "v_mfma_f32_16x16x32_bf16 a[228:231], v[96:99], a[84:87], a[228:231]\n"
      "s_add_u32 m0, s23, 33792\n"
      "ds_read_b64_tr_b16 a[98:99], v12 offset:43008\n"
      "v_add_f32 v10, v10, v58\n"
      "v_mfma_f32_16x16x32_bf16 a[208:211], v[100:103], a[64:67], a[208:211]\n"
      "buffer_load_dwordx4 v1, s[36:39], s52 offen offset:0 nt lds\n"
      "v_mfma_f32_16x16x32_bf16 a[208:211], v[104:107], a[68:71], a[208:211]\n"
      "ds_read_b64_tr_b16 a[100:101], v12 offset:51200\n"
      "v_add_f32 v10, v10, v59\n"
      "v_mfma_f32_16x16x32_bf16 a[220:223], v[100:103], a[72:75], a[220:223]\n"
      "ds_read_b64_tr_b16 a[102:103], v12 offset:59392\n"
      "v_add_f32 v6, v6, v60\n"
      "v_mfma_f32_16x16x32_bf16 a[220:223], v[104:107], a[76:79], a[220:223]\n"
      "ds_read_b64_tr_b16 a[64:65], v13 offset:34816\n"
      "v_add_f32 v6, v6, v61\n"
      "v_mfma_f32_16x16x32_bf16 a[232:235], v[100:103], a[80:83], a[232:235]\n"
      "ds_read_b64_tr_b16 a[66:67], v13 offset:43008\n"
      "v_add_f32 v6, v6, v62\n"
      "v_mfma_f32_16x16x32_bf16 a[232:235], v[104:107], a[84:87], a[232:235]\n"
      "s_waitcnt lgkmcnt(6)\n"
      "ds_read_b64_tr_b16 a[68:69], v13 offset:51200\n"
      "v_add_f32 v6, v6, v63\n"
      "v_mfma_f32_16x16x32_bf16 a[236:239], v[84:87], a[88:91], a[236:239]\n"
      "ds_read_b64_tr_b16 a[70:71], v13 offset:59392\n"
      "v_add_f32 v8, v8, v64\n"
      "v_mfma_f32_16x16x32_bf16 a[236:239], v[88:91], a[92:95], a[236:239]\n"
      "s_add_u32 m0, s23, 34688\n"
      "ds_read_b64_tr_b16 a[72:73], v14 offset:34816\n"
      "v_add_f32 v8, v8, v65\n"
      "v_mfma_f32_16x16x32_bf16 a[240:243], v[92:95], a[88:91], a[240:243]\n"
      "buffer_load_dwordx4 v1, s[36:39], 0 offen offset:128 nt lds\n"
      "v_mfma_f32_16x16x32_bf16 a[240:243], v[96:99], a[92:95], a[240:243]\n"
      "ds_read_b64_tr_b16 a[74:75], v14 offset:43008\n"
      "v_add_f32 v8, v8, v66\n"
      "v_mfma_f32_16x16x32_bf16 v[108:111], v[100:103], a[88:91], v[108:111]\n"
      "ds_read_b64_tr_b16 a[76:77], v14 offset:51200\n"
      "v_add_f32 v8, v8, v67\n"
      "v_mfma_f32_16x16x32_bf16 v[108:111], v[104:107], a[92:95], v[108:111]\n"
      "s_waitcnt lgkmcnt(7)\n"
      "ds_read_b64_tr_b16 a[78:79], v14 offset:59392\n"
      "v_add_f32 v10, v10, v68\n"
      "v_mfma_f32_16x16x32_bf16 v[112:115], v[84:87], a[96:99], v[112:115]\n"
      "ds_read_b64_tr_b16 a[80:81], v15 offset:34816\n"
      "v_add_f32 v10, v10, v69\n"
      "v_mfma_f32_16x16x32_bf16 v[112:115], v[88:91], a[100:103], v[112:115]\n"
      "ds_read_b64_tr_b16 a[82:83], v15 offset:43008\n"
      "v_add_f32 v10, v10, v70\n"
      "v_mfma_f32_16x16x32_bf16 v[116:119], v[92:95], a[96:99], v[116:119]\n"
      "ds_read_b64_tr_b16 a[84:85], v15 offset:51200\n"
      "v_add_f32 v10, v10, v71\n"
      "v_mfma_f32_16x16x32_bf16 v[116:119], v[96:99], a[100:103], v[116:119]\n"
      "s_add_u32 m0, s23, 35712\n"
      "ds_read_b64_tr_b16 a[86:87], v15 offset:59392\n"
      "v_add_f32 v6, v6, v72\n"
      "v_mfma_f32_16x16x32_bf16 v[120:123], v[100:103], a[96:99], v[120:123]\n"
      "buffer_load_dwordx4 v1, s[36:39], s52 offen offset:128 nt lds\n"
      "v_mfma_f32_16x16x32_bf16 v[120:123], v[104:107], a[100:103], v[120:123]\n"
      "s_waitcnt lgkmcnt(8)\n"
      "ds_read_b64_tr_b16 a[88:89], v12 offset:36864\n"
      "v_add_f32 v6, v6, v73\n"
      "v_mfma_f32_16x16x32_bf16 v[124:127], v[84:87], a[64:67], v[124:127]\n"
      "ds_read_b64_tr_b16 a[90:91], v12 offset:45056\n"
      "v_add_f32 v6, v6, v74\n"
      "v_mfma_f32_16x16x32_bf16 v[124:127], v[88:91], a[68:71], v[124:127]\n"
      "ds_read_b64_tr_b16 a[92:93], v12 offset:53248\n"
      "v_add_f32 v6, v6, v75\n"
      "v_mfma_f32_16x16x32_bf16 v[128:131], v[92:95], a[64:67], v[128:131]\n"
      "ds_read_b64_tr_b16 a[94:95], v12 offset:61440\n"
      "v_add_f32 v8, v8, v76\n"
      "v_mfma_f32_16x16x32_bf16 v[128:131], v[96:99], a[68:71], v[128:131]\n"
      "ds_read_b64_tr_b16 a[96:97], v13 offset:36864\n"
      "v_add_f32 v8, v8, v77\n"
      "v_mfma_f32_16x16x32_bf16 v[132:135], v[100:103], a[64:67], v[132:135]\n"
      "s_add_u32 m0, s23, 36608\n"
      "ds_read_b64_tr_b16 a[98:99], v13 offset:45056\n"
      "v_add_f32 v8, v8, v78\n"
      "v_mfma_f32_16x16x32_bf16 v[132:135], v[104:107], a[68:71], v[132:135]\n"
      "s_waitcnt lgkmcnt(10)\n"
      "ds_read_b64_tr_b16 a[100:101], v13 offset:53248\n"
      "v_add_f32 v8, v8, v79\n"
      "v_mfma_f32_16x16x32_bf16 v[136:139], v[84:87], a[72:75], v[136:139]\n"
      "buffer_load_dwordx4 v1, s[36:39], 0 offen offset:256 nt lds\n"
      "v_mfma_f32_16x16x32_bf16 v[136:139], v[88:91], a[76:79], v[136:139]\n"
      "ds_read_b64_tr_b16 a[102:103], v13 offset:61440\n"
      "v_add_f32 v10, v10, v80\n"
      "v_mfma_f32_16x16x32_bf16 v[140:143], v[92:95], a[72:75], v[140:143]\n"
      "ds_read_b64_tr_b16 a[64:65], v14 offset:36864\n"
      "v_add_f32 v10, v10, v81\n"
      "v_mfma_f32_16x16x32_bf16 v[140:143], v[96:99], a[76:79], v[140:143]\n"
      "ds_read_b64_tr_b16 a[66:67], v14 offset:45056\n"
      "v_add_f32 v10, v10, v82\n"
      "v_mfma_f32_16x16x32_bf16 v[144:147], v[100:103], a[72:75], v[144:147]\n"
      "ds_read_b64_tr_b16 a[68:69], v14 offset:53248\n"
      "v_add_f32 v10, v10, v83\n"
      "v_mfma_f32_16x16x32_bf16 v[144:147], v[104:107], a[76:79], v[144:147]\n"
      "s_waitcnt lgkmcnt(11)\n"
      "ds_read_b64_tr_b16 a[70:71], v14 offset:61440\n"
      "v_mfma_f32_16x16x32_bf16 v[148:151], v[84:87], a[80:83], v[148:151]\n"
      "ds_read_b64_tr_b16 a[72:73], v15 offset:36864\n"
      "v_mfma_f32_16x16x32_bf16 v[148:151], v[88:91], a[84:87], v[148:151]\n"
      "ds_read_b64_tr_b16 a[74:75], v15 offset:45056\n"
      "v_mfma_f32_16x16x32_bf16 v[152:155], v[92:95], a[80:83], v[152:155]\n"
      "ds_read_b64_tr_b16 a[76:77], v15 offset:53248\n"
      "v_mfma_f32_16x16x32_bf16 v[152:155], v[96:99], a[84:87], v[152:155]\n"
      "ds_read_b64_tr_b16 a[78:79], v15 offset:61440\n"
      "v_mfma_f32_16x16x32_bf16 v[156:159], v[100:103], a[80:83], v[156:159]\n"
      "s_add_u32 m0, s23, 37632\n"
      "v_mfma_f32_16x16x32_bf16 v[156:159], v[104:107], a[84:87], v[156:159]\n"
      "s_waitcnt lgkmcnt(12)\n"
      "ds_read_b64_tr_b16 a[80:81], v12 offset:38912\n"
      "v_mfma_f32_16x16x32_bf16 v[160:163], v[84:87], a[88:91], v[160:163]\n"
      "buffer_load_dwordx4 v1, s[36:39], s52 offen offset:256 nt lds\n"
      "v_mfma_f32_16x16x32_bf16 v[160:163], v[88:91], a[92:95], v[160:163]\n"
      "ds_read_b64_tr_b16 a[82:83], v12 offset:47104\n"
      "v_mfma_f32_16x16x32_bf16 v[164:167], v[92:95], a[88:91], v[164:167]\n"
      "ds_read_b64_tr_b16 a[84:85], v12 offset:55296\n"
      "v_mfma_f32_16x16x32_bf16 v[164:167], v[96:99], a[92:95], v[164:167]\n"
      "ds_read_b64_tr_b16 a[86:87], v12 offset:63488\n"
      "v_mfma_f32_16x16x32_bf16 v[168:171], v[100:103], a[88:91], v[168:171]\n"
      "v_mfma_f32_16x16x32_bf16 v[168:171], v[104:107], a[92:95], v[168:171]\n"
      "s_waitcnt lgkmcnt(12)\n"
      "ds_read_b64_tr_b16 a[88:89], v13 offset:38912\n"
      "v_mfma_f32_16x16x32_bf16 v[172:175], v[84:87], a[96:99], v[172:175]\n"
      "ds_read_b64_tr_b16 a[90:91], v13 offset:47104\n"
      "v_mfma_f32_16x16x32_bf16 v[172:175], v[88:91], a[100:103], v[172:175]\n"
      "s_add_u32 m0, s23, 38528\n"
      "ds_read_b64_tr_b16 a[92:93], v13 offset:55296\n"
      "v_mfma_f32_16x16x32_bf16 v[176:179], v[92:95], a[96:99], v[176:179]\n"
      "buffer_load_dwordx4 v1, s[36:39], 0 offen offset:384 nt lds\n"
      "v_mfma_f32_16x16x32_bf16 v[176:179], v[96:99], a[100:103], v[176:179]\n"
      "ds_read_b64_tr_b16 a[94:95], v13 offset:63488\n"
      "v_mfma_f32_16x16x32_bf16 v[180:183], v[100:103], a[96:99], v[180:183]\n"
      "v_mfma_f32_16x16x32_bf16 v[180:183], v[104:107], a[100:103], v[180:183]\n"
      "s_waitcnt lgkmcnt(12)\n"
      "ds_read_b64_tr_b16 a[96:97], v14 offset:38912\n"
      "v_mfma_f32_16x16x32_bf16 v[184:187], v[84:87], a[64:67], v[184:187]\n"
      "ds_read_b64_tr_b16 a[98:99], v14 offset:47104\n"
      "v_mfma_f32_16x16x32_bf16 v[184:187], v[88:91], a[68:71], v[184:187]\n"
      "ds_read_b64_tr_b16 a[100:101], v14 offset:55296\n"
      "v_mfma_f32_16x16x32_bf16 v[188:191], v[92:95], a[64:67], v[188:191]\n"
      "ds_read_b64_tr_b16 a[102:103], v14 offset:63488\n"
      "v_mfma_f32_16x16x32_bf16 v[188:191], v[96:99], a[68:71], v[188:191]\n"
      "s_add_u32 m0, s23, 39552\n"
      "v_mfma_f32_16x16x32_bf16 v[192:195], v[100:103], a[64:67], v[192:195]\n"
      "buffer_load_dwordx4 v1, s[36:39], s52 offen offset:384 nt lds\n"
      "v_mfma_f32_16x16x32_bf16 v[192:195], v[104:107], a[68:71], v[192:195]\n"
      "s_waitcnt lgkmcnt(12)\n"
      "ds_read_b64_tr_b16 a[64:65], v15 offset:38912\n"
      "v_add_u32 v1, 0x8000, v1\n"
      "v_mfma_f32_16x16x32_bf16 v[196:199], v[84:87], a[72:75], v[196:199]\n"
      "ds_read_b64_tr_b16 a[66:67], v15 offset:47104\n"
      "v_mfma_f32_16x16x32_bf16 v[196:199], v[88:91], a[76:79], v[196:199]\n"
      "ds_read_b64_tr_b16 a[68:69], v15 offset:55296\n"
      "v_mfma_f32_16x16x32_bf16 v[200:203], v[92:95], a[72:75], v[200:203]\n"
      "buffer_load_dword v23, v22, s[60:63], 0 offen\n"
      "v_mfma_f32_16x16x32_bf16 v[200:203], v[96:99], a[76:79], v[200:203]\n"
      "ds_read_b64_tr_b16 a[70:71], v15 offset:63488\n"
      "v_add_u32 v22, 0x400, v22\n"
      "v_mfma_f32_16x16x32_bf16 v[204:207], v[100:103], a[72:75], v[204:207]\n"
      "v_mfma_f32_16x16x32_bf16 v[204:207], v[104:107], a[76:79], v[204:207]\n"
      "s_waitcnt lgkmcnt(12)\n"
      "v_mfma_f32_16x16x32_bf16 v[208:211], v[84:87], a[80:83], v[208:211]\n"
      "v_mfma_f32_16x16x32_bf16 v[208:211], v[88:91], a[84:87], v[208:211]\n"
      "v_mfma_f32_16x16x32_bf16 v[212:215], v[92:95], a[80:83], v[212:215]\n"
      "s_cmp_eq_u32 s20, 14\n"
      "v_mfma_f32_16x16x32_bf16 v[212:215], v[96:99], a[84:87], v[212:215]\n"
      "s_cbranch_scc1 L_topD0_%=\n"
      "s_branch L_topN0_%=\n"
      ".p2align 6\n"
      "L_topD1_%=:\n"
      "v_mfma_f32_16x16x32_bf16 v[216:219], v[100:103], a[80:83], v[216:219]\n"
      "v_mfma_f32_16x16x32_bf16 v[216:219], v[104:107], a[84:87], v[216:219]\n"
      "s_waitcnt lgkmcnt(8)\n"
      "v_mfma_f32_16x16x32_bf16 v[220:223], v[84:87], a[88:91], v[220:223]\n"
      "s_waitcnt vmcnt(17) lgkmcnt(0)\n"
      "v_mfma_f32_16x16x32_bf16 v[220:223], v[88:91], a[92:95], v[220:223]\n"
      "s_barrier\n"
      "ds_read_b128 a[0:3], v3 offset:0\n"
      "v_mfma_f32_16x16x32_bf16 v[224:227], v[92:95], a[88:91], v[224:227]\n"
      "ds_read_b128 a[4:7], v4 offset:0\n"
      "v_mfma_f32_16x16x32_bf16 v[224:227], v[96:99], a[92:95], v[224:227]\n"
      "ds_read_b128 a[8:11], v3 offset:2048\n"
      "v_mfma_f32_16x16x32_bf16 v[228:231], v[100:103], a[88:91], v[228:231]\n"
      "ds_read_b128 a[12:15], v4 offset:2048\n"
      "v_mfma_f32_16x16x32_bf16 v[228:231], v[104:107], a[92:95], v[228:231]\n"
      "ds_read_b128 a[16:19], v3 offset:4096\n"
      "v_mfma_f32_16x16x32_bf16 v[232:235], v[84:87], a[96:99], v[232:235]\n"
      "s_add_u32 m0, s23, 98304\n"
      "ds_read_b128 a[20:23], v4 offset:4096\n"
      "v_mfma_f32_16x16x32_bf16 v[232:235], v[88:91], a[100:103], v[232:235]\n"
      "buffer_load_dwordx4 v2, s[40:43], 0 offen offset:0 lds\n"
      "v_mfma_f32_16x16x32_bf16 v[236:239], v[92:95], a[96:99], v[236:239]\n"
      "ds_read_b128 a[24:27], v3 offset:6144\n"
      "v_mfma_f32_16x16x32_bf16 v[236:239], v[96:99], a[100:103], v[236:239]\n"
      "ds_read_b128 a[28:31], v4 offset:6144\n"
      "v_mfma_f32_16x16x32_bf16 v[240:243], v[100:103], a[96:99], v[240:243]\n"
      "ds_read_b128 a[32:35], v3 offset:8192\n"
      "v_mfma_f32_16x16x32_bf16 v[240:243], v[104:107], a[100:103], v[240:243]\n"
      "ds_read_b128 a[36:39], v4 offset:8192\n"
      "v_mfma_f32_16x16x32_bf16 v[244:247], v[84:87], a[64:67], v[244:247]\n"
      "ds_read_b128 a[40:43], v3 offset:10240\n"
      "v_mfma_f32_16x16x32_bf16 v[244:247], v[88:91], a[68:71], v[244:247]\n"
      "ds_read_b128 a[44:47], v4 offset:10240\n"
      "v_mfma_f32_16x16x32_bf16 v[248:251], v[92:95], a[64:67], v[248:251]\n"
      "s_add_u32 m0, s23, 99328\n"
      "ds_read_b128 a[48:51], v3 offset:12288\n"
      "v_mfma_f32_16x16x32_bf16 v[248:251], v[96:99], a[68:71], v[248:251]\n"
      "buffer_load_dwordx4 v2, s[40:43], s52 offen offset:0 lds\n"
      "v_mfma_f32_16x16x32_bf16 v[252:255], v[100:103], a[64:67], v[252:255]\n"
      "ds_read_b128 a[52:55], v4 offset:12288\n"
      "v_mfma_f32_16x16x32_bf16 v[252:255], v[104:107], a[68:71], v[252:255]\n"
      "s_waitcnt lgkmcnt(6)\n"
      "ds_read_b128 a[56:59], v3 offset:14336\n"
      "v_mfma_f32_16x16x32_bf16 v[36:39], a[0:3], a[104:107], v[24:27]\n"
      "ds_read_b128 a[60:63], v4 offset:14336\n"
      "v_mfma_f32_16x16x32_bf16 v[36:39], a[4:7], a[108:111], v[36:39]\n"
      "v_mfma_f32_16x16x32_bf16 v[36:39], a[8:11], a[112:115], v[36:39]\n"
      "v_mfma_f32_16x16x32_bf16 v[36:39], a[12:15], a[116:119], v[36:39]\n"
      "v_mfma_f32_16x16x32_bf16 v[36:39], a[16:19], a[120:123], v[36:39]\n"
      "v_mfma_f32_16x16x32_bf16 v[36:39], a[20:23], a[124:127], v[36:39]\n"
      "v_mfma_f32_16x16x32_bf16 v[36:39], a[24:27], a[128:131], v[36:39]\n"
      "v_mfma_f32_16x16x32_bf16 v[36:39], a[28:31], a[132:135], v[36:39]\n"
      "v_mfma_f32_16x16x32_bf16 v[40:43], a[0:3], a[136:139], v[28:31]\n"
      "s_add_u32 m0, s23, 100224\n"
      "v_mfma_f32_16x16x32_bf16 v[40:43], a[4:7], a[140:143], v[40:43]\n"
      "buffer_load_dwordx4 v2, s[40:43], 0 offen offset:128 lds\n"
      "v_mfma_f32_16x16x32_bf16 v[40:43], a[8:11], a[144:147], v[40:43]\n"
      "v_exp_f32 v36, v36\n"
      "v_mfma_f32_16x16x32_bf16 v[40:43], a[12:15], a[148:151], v[40:43]\n"
      "v_add_f32 v6, v6, v36\n"
      "v_mfma_f32_16x16x32_bf16 v[40:43], a[16:19], a[152:155], v[40:43]\n"
      "v_exp_f32 v37, v37\n"
      "v_mfma_f32_16x16x32_bf16 v[40:43], a[20:23], a[156:159], v[40:43]\n"
      "v_cvt_pk_bf16_f32 v84, v36, v37\n"
      "v_add_f32 v6, v6, v37\n"
      "v_mfma_f32_16x16x32_bf16 v[40:43], a[24:27], a[160:163], v[40:43]\n"
      "v_exp_f32 v38, v38\n"
      "v_mfma_f32_16x16x32_bf16 v[40:43], a[28:31], a[164:167], v[40:43]\n"
      "v_add_f32 v6, v6, v38\n"
      "v_mfma_f32_16x16x32_bf16 v[44:47], a[0:3], a[168:171], v[32:35]\n"
      "s_add_u32 m0, s23, 101248\n"
      "v_exp_f32 v39, v39\n"
      "v_mfma_f32_16x16x32_bf16 v[44:47], a[4:7], a[172:175], v[44:47]\n"
      "buffer_load_dwordx4 v2, s[40:43], s52 offen offset:128 lds\n"
      "v_mfma_f32_16x16x32_bf16 v[44:47], a[8:11], a[176:179], v[44:47]\n"
      "v_exp_f32 v40, v40\n"
      "v_mfma_f32_16x16x32_bf16 v[44:47], a[12:15], a[180:183], v[44:47]\n"
      "ds_read_b128 a[0:3], v3 offset:16384\n"
      "v_cvt_pk_bf16_f32 v85, v38, v39\n"
      "v_mfma_f32_16x16x32_bf16 v[44:47], a[16:19], a[184:187], v[44:47]\n"
      "v_exp_f32 v41, v41\n"
      "v_mfma_f32_16x16x32_bf16 v[44:47], a[20:23], a[188:191], v[44:47]\n"
      "ds_read_b128 a[4:7], v4 offset:16384\n"
      "v_cvt_pk_bf16_f32 v92, v40, v41\n"
      "v_mfma_f32_16x16x32_bf16 v[44:47], a[24:27], a[192:195], v[44:47]\n"
      "v_exp_f32 v42, v42\n"
      "v_mfma_f32_16x16x32_bf16 v[44:47], a[28:31], a[196:199], v[44:47]\n"
      "s_waitcnt lgkmcnt(2)\n"
      "ds_read_b128 a[8:11], v3 offset:18432\n"
      "v_add_f32 v6, v6, v39\n"
      "v_mfma_f32_16x16x32_bf16 v[48:51], a[32:35], a[104:107], v[24:27]\n"
      "v_exp_f32 v43, v43\n"
      "v_mfma_f32_16x16x32_bf16 v[48:51], a[36:39], a[108:111], v[48:51]\n"
      "ds_read_b128 a[12:15], v4 offset:18432\n"
      "v_cvt_pk_bf16_f32 v93, v42, v43\n"
      "v_mfma_f32_16x16x32_bf16 v[48:51], a[40:43], a[112:115], v[48:51]\n"
      "v_exp_f32 v44, v44\n"
      "v_mfma_f32_16x16x32_bf16 v[48:51], a[44:47], a[116:119], v[48:51]\n"
      "ds_read_b128 a[16:19], v3 offset:20480\n"
      "v_add_f32 v8, v8, v40\n"
      "v_mfma_f32_16x16x32_bf16 v[48:51], a[48:51], a[120:123], v[48:51]\n"
      "s_add_u32 m0, s23, 102144\n"
      "v_exp_f32 v45, v45\n"
      "v_mfma_f32_16x16x32_bf16 v[48:51], a[52:55], a[124:127], v[48:51]\n"
      "buffer_load_dwordx4 v2, s[40:43], 0 offen offset:256 lds\n"
      "v_mfma_f32_16x16x32_bf16 v[48:51], a[56:59], a[128:131], v[48:51]\n"
      "v_exp_f32 v46, v46\n"
      "v_mfma_f32_16x16x32_bf16 v[48:51], a[60:63], a[132:135], v[48:51]\n"
      "ds_read_b128 a[20:23], v4 offset:20480\n"
      "v_cvt_pk_bf16_f32 v100, v44, v45\n"
      "v_mfma_f32_16x16x32_bf16 v[52:55], a[32:35], a[136:139], v[28:31]\n"
      "v_exp_f32 v47, v47\n"
      "v_mfma_f32_16x16x32_bf16 v[52:55], a[36:39], a[140:143], v[52:55]\n"
      "ds_read_b128 a[24:27], v3 offset:22528\n"
      "v_cvt_pk_bf16_f32 v101, v46, v47\n"
      "v_mfma_f32_16x16x32_bf16 v[52:55], a[40:43], a[144:147], v[52:55]\n"
      "v_exp_f32 v48, v48\n"
      "v_mfma_f32_16x16x32_bf16 v[52:55], a[44:47], a[148:151], v[52:55]\n"
      "ds_read_b128 a[28:31], v4 offset:22528\n"
      "v_add_f32 v8, v8, v41\n"
      "v_mfma_f32_16x16x32_bf16 v[52:55], a[48:51], a[152:155], v[52:55]\n"
      "s_add_u32 m0, s23, 103168\n"
      "v_exp_f32 v49, v49\n"
      "v_mfma_f32_16x16x32_bf16 v[52:55], a[52:55], a[156:159], v[52:55]\n"
      "buffer_load_dwordx4 v2, s[40:43], s52 offen offset:256 lds\n"
      "v_mfma_f32_16x16x32_bf16 v[52:55], a[56:59], a[160:163], v[52:55]\n"
      "v_exp_f32 v50, v50\n"
      "v_mfma_f32_16x16x32_bf16 v[52:55], a[60:63], a[164:167], v[52:55]\n"
      "v_cvt_pk_bf16_f32 v86, v48, v49\n"
      "v_add_f32 v8, v8, v42\n"
      "v_mfma_f32_16x16x32_bf16 v[56:59], a[32:35], a[168:171], v[32:35]\n"
      "v_exp_f32 v51, v51\n"
      "v_mfma_f32_16x16x32_bf16 v[56:59], a[36:39], a[172:175], v[56:59]\n"
      "ds_read_b128 a[32:35], v3 offset:24576\n"
      "v_cvt_pk_bf16_f32 v87, v50, v51\n"
      "v_mfma_f32_16x16x32_bf16 v[56:59], a[40:43], a[176:179], v[56:59]\n"
      "v_exp_f32 v52, v52\n"
      "v_mfma_f32_16x16x32_bf16 v[56:59], a[44:47], a[180:183], v[56:59]\n"
      "ds_read_b128 a[36:39], v4 offset:24576\n"
      "v_add_f32 v8, v8, v43\n"
      "v_mfma_f32_16x16x32_bf16 v[56:59], a[48:51], a[184:187], v[56:59]\n"
      "v_exp_f32 v53, v53\n"
      "v_mfma_f32_16x16x32_bf16 v[56:59], a[52:55], a[188:191], v[56:59]\n"
      "ds_read_b128 a[40:43], v3 offset:26624\n"
      "v_cvt_pk_bf16_f32 v94, v52, v53\n"
      "v_mfma_f32_16x16x32_bf16 v[56:59], a[56:59], a[192:195], v[56:59]\n"
      "v_exp_f32 v54, v54\n"
      "v_mfma_f32_16x16x32_bf16 v[56:59], a[60:63], a[196:199], v[56:59]\n"
      "s_waitcnt lgkmcnt(3)\n"
      "ds_read_b128 a[44:47], v4 offset:26624\n"
      "v_add_f32 v10, v10, v44\n"
      "v_mfma_f32_16x16x32_bf16 v[60:63], a[0:3], a[104:107], v[24:27]\n"
      "s_add_u32 m0, s23, 104064\n"
      "v_exp_f32 v55, v55\n"
      "v_mfma_f32_16x16x32_bf16 v[60:63], a[4:7], a[108:111], v[60:63]\n"
      "buffer_load_dwordx4 v2, s[40:43], 0 offen offset:384 lds\n"
      "v_mfma_f32_16x16x32_bf16 v[60:63], a[8:11], a[112:115], v[60:63]\n"
      "v_exp_f32 v56, v56\n"
      "v_mfma_f32_16x16x32_bf16 v[60:63], a[12:15], a[116:119], v[60:63]\n"
      "ds_read_b128 a[48:51], v3 offset:28672\n"
      "v_cvt_pk_bf16_f32 v95, v54, v55\n"
      "v_mfma_f32_16x16x32_bf16 v[60:63], a[16:19], a[120:123], v[60:63]\n"
      "v_exp_f32 v57, v57\n"
      "v_mfma_f32_16x16x32_bf16 v[60:63], a[20:23], a[124:127], v[60:63]\n"
      "ds_read_b128 a[52:55], v4 offset:28672\n"
      "v_cvt_pk_bf16_f32 v102, v56, v57\n"
      "v_mfma_f32_16x16x32_bf16 v[60:63], a[24:27], a[128:131], v[60:63]\n"
      "v_exp_f32 v58, v58\n"
      "v_mfma_f32_16x16x32_bf16 v[60:63], a[28:31], a[132:135], v[60:63]\n"
      "ds_read_b128 a[56:59], v3 offset:30720\n"
      "v_add_f32 v10, v10, v45\n"
      "v_mfma_f32_16x16x32_bf16 v[64:67], a[0:3], a[136:139], v[28:31]\n"
      "s_add_u32 m0, s23, 105088\n"
      "v_exp_f32 v59, v59\n"
      "v_mfma_f32_16x16x32_bf16 v[64:67], a[4:7], a[140:143], v[64:67]\n"
      "buffer_load_dwordx4 v2, s[40:43], s52 offen offset:384 lds\n"
      "v_mfma_f32_16x16x32_bf16 v[64:67], a[8:11], a[144:147], v[64:67]\n"
      "v_exp_f32 v60, v60\n"
      "v_mfma_f32_16x16x32_bf16 v[64:67], a[12:15], a[148:151], v[64:67]\n"
      "ds_read_b128 a[60:63], v4 offset:30720\n"
      "v_cvt_pk_bf16_f32 v103, v58, v59\n"
      "v_mfma_f32_16x16x32_bf16 v[64:67], a[16:19], a[152:155], v[64:67]\n"
      "v_exp_f32 v61, v61\n"
      "v_mfma_f32_16x16x32_bf16 v[64:67], a[20:23], a[156:159], v[64:67]\n"
      "v_cvt_pk_bf16_f32 v88, v60, v61\n"
      "v_add_u32 v2, 0x8000, v2\n"
      "v_mfma_f32_16x16x32_bf16 v[64:67], a[24:27], a[160:163], v[64:67]\n"
      "v_exp_f32 v62, v62\n"
      "v_mfma_f32_16x16x32_bf16 v[64:67], a[28:31], a[164:167], v[64:67]\n"
      "v_add_f32 v10, v10, v46\n"
      "v_add_f32 v10, v10, v47\n"
      "v_mfma_f32_16x16x32_bf16 v[68:71], a[0:3], a[168:171], v[32:35]\n"
      "v_exp_f32 v63, v63\n"
      "v_mfma_f32_16x16x32_bf16 v[68:71], a[4:7], a[172:175], v[68:71]\n"
      "v_cvt_pk_bf16_f32 v89, v62, v63\n"
      "v_add_f32 v6, v6, v48\n"
      "v_mfma_f32_16x16x32_bf16 v[68:71], a[8:11], a[176:179], v[68:71]\n"
      "s_waitcnt vmcnt(17) lgkmcnt(0)\n"
      "v_exp_f32 v64, v64\n"
      "v_mfma_f32_16x16x32_bf16 v[68:71], a[12:15], a[180:183], v[68:71]\n"
      "s_barrier\n"
      "ds_read_b64_tr_b16 a[64:65], v12 offset:0\n"
      "v_add_f32 v6, v6, v49\n"
      "v_mfma_f32_16x16x32_bf16 v[68:71], a[16:19], a[184:187], v[68:71]\n"
      "v_exp_f32 v65, v65\n"
      "v_mfma_f32_16x16x32_bf16 v[68:71], a[20:23], a[188:191], v[68:71]\n"
      "ds_read_b64_tr_b16 a[66:67], v12 offset:8192\n"
      "v_cvt_pk_bf16_f32 v96, v64, v65\n"
      "v_mfma_f32_16x16x32_bf16 v[68:71], a[24:27], a[192:195], v[68:71]\n"
      "v_exp_f32 v66, v66\n"
      "v_mfma_f32_16x16x32_bf16 v[68:71], a[28:31], a[196:199], v[68:71]\n"
      "ds_read_b64_tr_b16 a[68:69], v12 offset:16384\n"
      "v_add_f32 v6, v6, v50\n"
      "v_mfma_f32_16x16x32_bf16 v[72:75], a[32:35], a[104:107], v[24:27]\n"
      "v_exp_f32 v67, v67\n"
      "v_mfma_f32_16x16x32_bf16 v[72:75], a[36:39], a[108:111], v[72:75]\n"
      "ds_read_b64_tr_b16 a[70:71], v12 offset:24576\n"
      "v_cvt_pk_bf16_f32 v97, v66, v67\n"
      "v_mfma_f32_16x16x32_bf16 v[72:75], a[40:43], a[112:115], v[72:75]\n"
      "v_exp_f32 v68, v68\n"
      "v_mfma_f32_16x16x32_bf16 v[72:75], a[44:47], a[116:119], v[72:75]\n"
      "ds_read_b64_tr_b16 a[72:73], v13 offset:0\n"
      "v_add_f32 v6, v6, v51\n"
      "v_mfma_f32_16x16x32_bf16 v[72:75], a[48:51], a[120:123], v[72:75]\n"
      "v_exp_f32 v69, v69\n"
      "v_mfma_f32_16x16x32_bf16 v[72:75], a[52:55], a[124:127], v[72:75]\n"
      "ds_read_b64_tr_b16 a[74:75], v13 offset:8192\n"
      "v_cvt_pk_bf16_f32 v104, v68, v69\n"
      "v_mfma_f32_16x16x32_bf16 v[72:75], a[56:59], a[128:131], v[72:75]\n"
      "v_exp_f32 v70, v70\n"
      "v_mfma_f32_16x16x32_bf16 v[72:75], a[60:63], a[132:135], v[72:75]\n"
      "ds_read_b64_tr_b16 a[76:77], v13 offset:16384\n"
      "v_add_f32 v8, v8, v52\n"
      "v_mfma_f32_16x16x32_bf16 v[76:79], a[32:35], a[136:139], v[28:31]\n"
      "v_exp_f32 v71, v71\n"
      "v_mfma_f32_16x16x32_bf16 v[76:79], a[36:39], a[140:143], v[76:79]\n"
      "ds_read_b64_tr_b16 a[78:79], v13 offset:24576\n"
      "v_cvt_pk_bf16_f32 v105, v70, v71\n"
      "v_mfma_f32_16x16x32_bf16 v[76:79], a[40:43], a[144:147], v[76:79]\n"
      "v_exp_f32 v72, v72\n"
      "v_mfma_f32_16x16x32_bf16 v[76:79], a[44:47], a[148:151], v[76:79]\n"
      "ds_read_b64_tr_b16 a[80:81], v14 offset:0\n"
      "v_add_f32 v8, v8, v53\n"
      "v_mfma_f32_16x16x32_bf16 v[76:79], a[48:51], a[152:155], v[76:79]\n"
      "v_exp_f32 v73, v73\n"
      "v_mfma_f32_16x16x32_bf16 v[76:79], a[52:55], a[156:159], v[76:79]\n"
      "ds_read_b64_tr_b16 a[82:83], v14 offset:8192\n"
      "v_cvt_pk_bf16_f32 v90, v72, v73\n"
      "v_mfma_f32_16x16x32_bf16 v[76:79], a[56:59], a[160:163], v[76:79]\n"
      "v_exp_f32 v74, v74\n"
      "v_mfma_f32_16x16x32_bf16 v[76:79], a[60:63], a[164:167], v[76:79]\n"
      "ds_read_b64_tr_b16 a[84:85], v14 offset:16384\n"
      "v_add_f32 v8, v8, v54\n"
      "v_mfma_f32_16x16x32_bf16 v[80:83], a[32:35], a[168:171], v[32:35]\n"
      "v_exp_f32 v75, v75\n"
      "v_mfma_f32_16x16x32_bf16 v[80:83], a[36:39], a[172:175], v[80:83]\n"
      "ds_read_b64_tr_b16 a[86:87], v14 offset:24576\n"
      "v_cvt_pk_bf16_f32 v91, v74, v75\n"
      "v_mfma_f32_16x16x32_bf16 v[80:83], a[40:43], a[176:179], v[80:83]\n"
      "v_exp_f32 v76, v76\n"
      "v_mfma_f32_16x16x32_bf16 v[80:83], a[44:47], a[180:183], v[80:83]\n"
      "v_add_f32 v8, v8, v55\n"
      "v_mfma_f32_16x16x32_bf16 v[80:83], a[48:51], a[184:187], v[80:83]\n"
      "v_exp_f32 v77, v77\n"
      "v_mfma_f32_16x16x32_bf16 v[80:83], a[52:55], a[188:191], v[80:83]\n"
      "v_cvt_pk_bf16_f32 v98, v76, v77\n"
      "v_mfma_f32_16x16x32_bf16 v[80:83], a[56:59], a[192:195], v[80:83]\n"
      "v_exp_f32 v78, v78\n"
      "v_mfma_f32_16x16x32_bf16 v[80:83], a[60:63], a[196:199], v[80:83]\n"
      "s_waitcnt lgkmcnt(8)\n"
      "L_loopD1_%=:\n"
      "v_mfma_f32_16x16x32_bf16 a[200:203], v[84:87], a[64:67], a[200:203]\n"
      "s_add_u32 m0, s23, 0\n"
      "v_exp_f32 v79, v79\n"
      "v_mfma_f32_16x16x32_bf16 a[200:203], v[88:91], a[68:71], a[200:203]\n"
      "s_waitcnt lgkmcnt(4)\n"
      "buffer_load_dwordx4 v1, s[36:39], 0 offen offset:0 lds\n"
      "v_mfma_f32_16x16x32_bf16 a[212:215], v[84:87], a[72:75], a[212:215]\n"
      "v_exp_f32 v80, v80\n"
      "v_mfma_f32_16x16x32_bf16 a[212:215], v[88:91], a[76:79], a[212:215]\n"
      "s_waitcnt lgkmcnt(0)\n"
      "ds_read_b64_tr_b16 a[88:89], v15 offset:0\n"
      "v_cvt_pk_bf16_f32 v99, v78, v79\n"
      "v_mfma_f32_16x16x32_bf16 a[224:227], v[84:87], a[80:83], a[224:227]\n"
      "v_exp_f32 v81, v81\n"
      "v_mfma_f32_16x16x32_bf16 a[224:227], v[88:91], a[84:87], a[224:227]\n"
      "ds_read_b64_tr_b16 a[90:91], v15 offset:8192\n"
      "v_cvt_pk_bf16_f32 v106, v80, v81\n"
      "v_mfma_f32_16x16x32_bf16 a[204:207], v[92:95], a[64:67], a[204:207]\n"
      "v_exp_f32 v82, v82\n"
      "v_mfma_f32_16x16x32_bf16 a[204:207], v[96:99], a[68:71], a[204:207]\n"
      "ds_read_b64_tr_b16 a[92:93], v15 offset:16384\n"
      "v_add_f32 v10, v10, v56\n"
      "v_mfma_f32_16x16x32_bf16 a[216:219], v[92:95], a[72:75], a[216:219]\n"
      "s_add_u32 m0, s23, 1024\n"
      "v_exp_f32 v83, v83\n"
      "v_mfma_f32_16x16x32_bf16 a[216:219], v[96:99], a[76:79], a[216:219]\n"
      "buffer_load_dwordx4 v1, s[36:39], s52 offen offset:0 lds\n"
      "v_mfma_f32_16x16x32_bf16 a[228:231], v[92:95], a[80:83], a[228:231]\n"
      "ds_read_b64_tr_b16 a[94:95], v15 offset:24576\n"
      "v_cvt_pk_bf16_f32 v107, v82, v83\n"
      "v_mfma_f32_16x16x32_bf16 a[228:231], v[96:99], a[84:87], a[228:231]\n"
      "ds_read_b64_tr_b16 a[96:97], v12 offset:2048\n"
      "v_add_f32 v10, v10, v57\n"
      "v_mfma_f32_16x16x32_bf16 a[208:211], v[100:103], a[64:67], a[208:211]\n"
      "ds_read_b64_tr_b16 a[98:99], v12 offset:10240\n"
      "v_add_f32 v10, v10, v58\n"
      "v_mfma_f32_16x16x32_bf16 a[208:211], v[104:107], a[68:71], a[208:211]\n"
      "ds_read_b64_tr_b16 a[100:101], v12 offset:18432\n"
      "v_add_f32 v10, v10, v59\n"
      "v_mfma_f32_16x16x32_bf16 a[220:223], v[100:103], a[72:75], a[220:223]\n"
      "ds_read_b64_tr_b16 a[102:103], v12 offset:26624\n"
      "v_add_f32 v6, v6, v60\n"
      "v_mfma_f32_16x16x32_bf16 a[220:223], v[104:107], a[76:79], a[220:223]\n"
      "ds_read_b64_tr_b16 a[64:65], v13 offset:2048\n"
      "v_add_f32 v6, v6, v61\n"
      "v_mfma_f32_16x16x32_bf16 a[232:235], v[100:103], a[80:83], a[232:235]\n"
      "ds_read_b64_tr_b16 a[66:67], v13 offset:10240\n"
      "v_add_f32 v6, v6, v62\n"
      "v_mfma_f32_16x16x32_bf16 a[232:235], v[104:107], a[84:87], a[232:235]\n"
      "s_waitcnt lgkmcnt(6)\n"
      "ds_read_b64_tr_b16 a[68:69], v13 offset:18432\n"
      "v_add_f32 v6, v6, v63\n"
      "v_mfma_f32_16x16x32_bf16 a[236:239], v[84:87], a[88:91], a[236:239]\n"
      "ds_read_b64_tr_b16 a[70:71], v13 offset:26624\n"
      "v_add_f32 v8, v8, v64\n"
      "v_mfma_f32_16x16x32_bf16 a[236:239], v[88:91], a[92:95], a[236:239]\n"
      "ds_read_b64_tr_b16 a[72:73], v14 offset:2048\n"
      "v_add_f32 v8, v8, v65\n"
      "v_mfma_f32_16x16x32_bf16 a[240:243], v[92:95], a[88:91], a[240:243]\n"
      "s_add_u32 m0, s23, 1920\n"
      "ds_read_b64_tr_b16 a[74:75], v14 offset:10240\n"
      "v_add_f32 v8, v8, v66\n"
      "v_mfma_f32_16x16x32_bf16 a[240:243], v[96:99], a[92:95], a[240:243]\n"
      "buffer_load_dwordx4 v1, s[36:39], 0 offen offset:128 lds\n"
      "v_mfma_f32_16x16x32_bf16 v[108:111], v[100:103], a[88:91], v[108:111]\n"
      "ds_read_b64_tr_b16 a[76:77], v14 offset:18432\n"
      "v_add_f32 v8, v8, v67\n"
      "v_mfma_f32_16x16x32_bf16 v[108:111], v[104:107], a[92:95], v[108:111]\n"
      "s_waitcnt lgkmcnt(7)\n"
      "ds_read_b64_tr_b16 a[78:79], v14 offset:26624\n"
      "v_add_f32 v10, v10, v68\n"
      "v_mfma_f32_16x16x32_bf16 v[112:115], v[84:87], a[96:99], v[112:115]\n"
      "ds_read_b64_tr_b16 a[80:81], v15 offset:2048\n"
      "v_add_f32 v10, v10, v69\n"
      "v_mfma_f32_16x16x32_bf16 v[112:115], v[88:91], a[100:103], v[112:115]\n"
      "ds_read_b64_tr_b16 a[82:83], v15 offset:10240\n"
      "v_add_f32 v10, v10, v70\n"
      "v_mfma_f32_16x16x32_bf16 v[116:119], v[92:95], a[96:99], v[116:119]\n"
      "ds_read_b64_tr_b16 a[84:85], v15 offset:18432\n"
      "v_add_f32 v10, v10, v71\n"
      "v_mfma_f32_16x16x32_bf16 v[116:119], v[96:99], a[100:103], v[116:119]\n"
      "ds_read_b64_tr_b16 a[86:87], v15 offset:26624\n"
      "v_add_f32 v6, v6, v72\n"
      "v_mfma_f32_16x16x32_bf16 v[120:123], v[100:103], a[96:99], v[120:123]\n"
      "s_add_u32 m0, s23, 2944\n"
      "ds_read_b64_tr_b16 a[88:89], v12 offset:4096\n"
      "v_add_f32 v6, v6, v73\n"
      "v_mfma_f32_16x16x32_bf16 v[120:123], v[104:107], a[100:103], v[120:123]\n"
      "s_waitcnt lgkmcnt(9)\n"
      "buffer_load_dwordx4 v1, s[36:39], s52 offen offset:128 lds\n"
      "v_mfma_f32_16x16x32_bf16 v[124:127], v[84:87], a[64:67], v[124:127]\n"
      "ds_read_b64_tr_b16 a[90:91], v12 offset:12288\n"
      "v_add_f32 v6, v6, v74\n"
      "v_mfma_f32_16x16x32_bf16 v[124:127], v[88:91], a[68:71], v[124:127]\n"
      "ds_read_b64_tr_b16 a[92:93], v12 offset:20480\n"
      "v_add_f32 v6, v6, v75\n"
      "v_mfma_f32_16x16x32_bf16 v[128:131], v[92:95], a[64:67], v[128:131]\n"
      "ds_read_b64_tr_b16 a[94:95], v12 offset:28672\n"
      "v_add_f32 v8, v8, v76\n"
      "v_mfma_f32_16x16x32_bf16 v[128:131], v[96:99], a[68:71], v[128:131]\n"
      "ds_read_b64_tr_b16 a[96:97], v13 offset:4096\n"
      "v_add_f32 v8, v8, v77\n"
      "v_mfma_f32_16x16x32_bf16 v[132:135], v[100:103], a[64:67], v[132:135]\n"
      "ds_read_b64_tr_b16 a[98:99], v13 offset:12288\n"
      "v_add_f32 v8, v8, v78\n"
      "v_mfma_f32_16x16x32_bf16 v[132:135], v[104:107], a[68:71], v[132:135]\n"
      "s_waitcnt lgkmcnt(10)\n"
      "ds_read_b64_tr_b16 a[100:101], v13 offset:20480\n"
      "v_add_f32 v8, v8, v79\n"
      "v_mfma_f32_16x16x32_bf16 v[136:139], v[84:87], a[72:75], v[136:139]\n"
      "s_add_u32 m0, s23, 3840\n"
      "ds_read_b64_tr_b16 a[102:103], v13 offset:28672\n"
      "v_add_f32 v10, v10, v80\n"
      "v_mfma_f32_16x16x32_bf16 v[136:139], v[88:91], a[76:79], v[136:139]\n"
      "buffer_load_dwordx4 v1, s[36:39], 0 offen offset:256 lds\n"
      "v_mfma_f32_16x16x32_bf16 v[140:143], v[92:95], a[72:75], v[140:143]\n"
      "ds_read_b64_tr_b16 a[64:65], v14 offset:4096\n"
      "v_add_f32 v10, v10, v81\n"
      "v_mfma_f32_16x16x32_bf16 v[140:143], v[96:99], a[76:79], v[140:143]\n"
      "ds_read_b64_tr_b16 a[66:67], v14 offset:12288\n"
      "v_add_f32 v10, v10, v82\n"
      "v_mfma_f32_16x16x32_bf16 v[144:147], v[100:103], a[72:75], v[144:147]\n"
      "ds_read_b64_tr_b16 a[68:69], v14 offset:20480\n"
      "v_add_f32 v10, v10, v83\n"
      "v_mfma_f32_16x16x32_bf16 v[144:147], v[104:107], a[76:79], v[144:147]\n"
      "s_waitcnt lgkmcnt(11)\n"
      "ds_read_b64_tr_b16 a[70:71], v14 offset:28672\n"
      "v_mfma_f32_16x16x32_bf16 v[148:151], v[84:87], a[80:83], v[148:151]\n"
      "ds_read_b64_tr_b16 a[72:73], v15 offset:4096\n"
      "v_mfma_f32_16x16x32_bf16 v[148:151], v[88:91], a[84:87], v[148:151]\n"
      "ds_read_b64_tr_b16 a[74:75], v15 offset:12288\n"
      "v_mfma_f32_16x16x32_bf16 v[152:155], v[92:95], a[80:83], v[152:155]\n"
      "s_add_u32 m0, s23, 4864\n"
      "ds_read_b64_tr_b16 a[76:77], v15 offset:20480\n"
      "v_mfma_f32_16x16x32_bf16 v[152:155], v[96:99], a[84:87], v[152:155]\n"
      "buffer_load_dwordx4 v1, s[36:39], s52 offen offset:256 lds\n"
      "v_mfma_f32_16x16x32_bf16 v[156:159], v[100:103], a[80:83], v[156:159]\n"
      "ds_read_b64_tr_b16 a[78:79], v15 offset:28672\n"
      "v_mfma_f32_16x16x32_bf16 v[156:159], v[104:107], a[84:87], v[156:159]\n"
      "s_waitcnt lgkmcnt(12)\n"
      "ds_read_b64_tr_b16 a[80:81], v12 offset:6144\n"
      "v_mfma_f32_16x16x32_bf16 v[160:163], v[84:87], a[88:91], v[160:163]\n"
      "ds_read_b64_tr_b16 a[82:83], v12 offset:14336\n"
      "v_mfma_f32_16x16x32_bf16 v[160:163], v[88:91], a[92:95], v[160:163]\n"
      "ds_read_b64_tr_b16 a[84:85], v12 offset:22528\n"
      "v_mfma_f32_16x16x32_bf16 v[164:167], v[92:95], a[88:91], v[164:167]\n"
      "ds_read_b64_tr_b16 a[86:87], v12 offset:30720\n"
      "v_mfma_f32_16x16x32_bf16 v[164:167], v[96:99], a[92:95], v[164:167]\n"
      "v_mfma_f32_16x16x32_bf16 v[168:171], v[100:103], a[88:91], v[168:171]\n"
      "v_mfma_f32_16x16x32_bf16 v[168:171], v[104:107], a[92:95], v[168:171]\n"
      "s_waitcnt lgkmcnt(12)\n"
      "ds_read_b64_tr_b16 a[88:89], v13 offset:6144\n"
      "v_mfma_f32_16x16x32_bf16 v[172:175], v[84:87], a[96:99], v[172:175]\n"
      "ds_read_b64_tr_b16 a[90:91], v13 offset:14336\n"
      "v_mfma_f32_16x16x32_bf16 v[172:175], v[88:91], a[100:103], v[172:175]\n"
      "ds_read_b64_tr_b16 a[92:93], v13 offset:22528\n"
      "v_mfma_f32_16x16x32_bf16 v[176:179], v[92:95], a[96:99], v[176:179]\n"
      "s_add_u32 m0, s23, 5760\n"
      "ds_read_b64_tr_b16 a[94:95], v13 offset:30720\n"
      "v_mfma_f32_16x16x32_bf16 v[176:179], v[96:99], a[100:103], v[176:179]\n"
      "buffer_load_dwordx4 v1, s[36:39], 0 offen offset:384 lds\n"
      "v_mfma_f32_16x16x32_bf16 v[180:183], v[100:103], a[96:99], v[180:183]\n"
      "v_mfma_f32_16x16x32_bf16 v[180:183], v[104:107], a[100:103], v[180:183]\n"
      "s_waitcnt lgkmcnt(12)\n"
      "ds_read_b64_tr_b16 a[96:97], v14 offset:6144\n"
      "v_mfma_f32_16x16x32_bf16 v[184:187], v[84:87], a[64:67], v[184:187]\n"
      "ds_read_b64_tr_b16 a[98:99], v14 offset:14336\n"
      "v_mfma_f32_16x16x32_bf16 v[184:187], v[88:91], a[68:71], v[184:187]\n"
      "ds_read_b64_tr_b16 a[100:101], v14 offset:22528\n"
      "v_mfma_f32_16x16x32_bf16 v[188:191], v[92:95], a[64:67], v[188:191]\n"
      "ds_read_b64_tr_b16 a[102:103], v14 offset:30720\n"
      "v_mfma_f32_16x16x32_bf16 v[188:191], v[96:99], a[68:71], v[188:191]\n"
      "v_mfma_f32_16x16x32_bf16 v[192:195], v[100:103], a[64:67], v[192:195]\n"
      "s_add_u32 m0, s23, 6784\n"
      "v_mfma_f32_16x16x32_bf16 v[192:195], v[104:107], a[68:71], v[192:195]\n"
      "s_waitcnt lgkmcnt(12)\n"
      "buffer_load_dwordx4 v1, s[36:39], s52 offen offset:384 lds\n"
      "v_mfma_f32_16x16x32_bf16 v[196:199], v[84:87], a[72:75], v[196:199]\n"
      "ds_read_b64_tr_b16 a[64:65], v15 offset:6144\n"
      "v_add_u32 v1, 0x8000, v1\n"
      "v_mfma_f32_16x16x32_bf16 v[196:199], v[88:91], a[76:79], v[196:199]\n"
      "ds_read_b64_tr_b16 a[66:67], v15 offset:14336\n"
      "v_mfma_f32_16x16x32_bf16 v[200:203], v[92:95], a[72:75], v[200:203]\n"
      "ds_read_b64_tr_b16 a[68:69], v15 offset:22528\n"
      "v_mfma_f32_16x16x32_bf16 v[200:203], v[96:99], a[76:79], v[200:203]\n"
      "buffer_load_dword v23, v22, s[60:63], 0 offen\n"
      "v_mfma_f32_16x16x32_bf16 v[204:207], v[100:103], a[72:75], v[204:207]\n"
      "ds_read_b64_tr_b16 a[70:71], v15 offset:30720\n"
      "v_add_u32 v22, 0x400, v22\n"
      "v_mfma_f32_16x16x32_bf16 v[204:207], v[104:107], a[76:79], v[204:207]\n"
      "s_waitcnt lgkmcnt(12)\n"
      "v_mfma_f32_16x16x32_bf16 v[208:211], v[84:87], a[80:83], v[208:211]\n"
      "v_mfma_f32_16x16x32_bf16 v[208:211], v[88:91], a[84:87], v[208:211]\n"
      "s_getreg_b32 s29, hwreg(HW_REG_IB_STS, 0, 4)\n"
      "v_mfma_f32_16x16x32_bf16 v[212:215], v[92:95], a[80:83], v[212:215]\n"
      "s_cmp_ge_u32 s29, 10\n"
      "v_mfma_f32_16x16x32_bf16 v[212:215], v[96:99], a[84:87], v[212:215]\n"
      "s_cbranch_scc1 L_midN1_%=\n"
      "v_mfma_f32_16x16x32_bf16 v[216:219], v[100:103], a[80:83], v[216:219]\n"
      "v_mfma_f32_16x16x32_bf16 v[216:219], v[104:107], a[84:87], v[216:219]\n"
      "s_waitcnt lgkmcnt(8)\n"
      "v_mfma_f32_16x16x32_bf16 v[220:223], v[84:87], a[88:91], v[220:223]\n"
      "s_waitcnt vmcnt(17) lgkmcnt(0)\n"
      "v_mfma_f32_16x16x32_bf16 v[220:223], v[88:91], a[92:95], v[220:223]\n"
      "s_barrier\n"
      "ds_read_b128 a[0:3], v3 offset:32768\n"
      "v_mfma_f32_16x16x32_bf16 v[224:227], v[92:95], a[88:91], v[224:227]\n"
      "ds_read_b128 a[4:7], v4 offset:32768\n"
      "v_mfma_f32_16x16x32_bf16 v[224:227], v[96:99], a[92:95], v[224:227]\n"
      "ds_read_b128 a[8:11], v3 offset:34816\n"
      "v_mfma_f32_16x16x32_bf16 v[228:231], v[100:103], a[88:91], v[228:231]\n"
      "ds_read_b128 a[12:15], v4 offset:34816\n"
      "v_mfma_f32_16x16x32_bf16 v[228:231], v[104:107], a[92:95], v[228:231]\n"
      "ds_read_b128 a[16:19], v3 offset:36864\n"
      "v_mfma_f32_16x16x32_bf16 v[232:235], v[84:87], a[96:99], v[232:235]\n"
      "s_add_u32 m0, s23, 65536\n"
      "ds_read_b128 a[20:23], v4 offset:36864\n"
      "v_mfma_f32_16x16x32_bf16 v[232:235], v[88:91], a[100:103], v[232:235]\n"
      "buffer_load_dwordx4 v2, s[40:43], 0 offen offset:0 lds\n"
      "v_mfma_f32_16x16x32_bf16 v[236:239], v[92:95], a[96:99], v[236:239]\n"
      "ds_read_b128 a[24:27], v3 offset:38912\n"
      "v_mfma_f32_16x16x32_bf16 v[236:239], v[96:99], a[100:103], v[236:239]\n"
      "ds_read_b128 a[28:31], v4 offset:38912\n"
      "v_mfma_f32_16x16x32_bf16 v[240:243], v[100:103], a[96:99], v[240:243]\n"
      "ds_read_b128 a[32:35], v3 offset:40960\n"
      "v_mfma_f32_16x16x32_bf16 v[240:243], v[104:107], a[100:103], v[240:243]\n"
      "ds_read_b128 a[36:39], v4 offset:40960\n"
      "v_mfma_f32_16x16x32_bf16 v[244:247], v[84:87], a[64:67], v[244:247]\n"
      "ds_read_b128 a[40:43], v3 offset:43008\n"
      "v_mfma_f32_16x16x32_bf16 v[244:247], v[88:91], a[68:71], v[244:247]\n"
      "ds_read_b128 a[44:47], v4 offset:43008\n"
      "v_mfma_f32_16x16x32_bf16 v[248:251], v[92:95], a[64:67], v[248:251]\n"
      "s_add_u32 m0, s23, 66560\n"
      "ds_read_b128 a[48:51], v3 offset:45056\n"
      "v_mfma_f32_16x16x32_bf16 v[248:251], v[96:99], a[68:71], v[248:251]\n"
      "buffer_load_dwordx4 v2, s[40:43], s52 offen offset:0 lds\n"
      "v_mfma_f32_16x16x32_bf16 v[252:255], v[100:103], a[64:67], v[252:255]\n"
      "ds_read_b128 a[52:55], v4 offset:45056\n"
      "v_mfma_f32_16x16x32_bf16 v[252:255], v[104:107], a[68:71], v[252:255]\n"
      "s_waitcnt lgkmcnt(6)\n"
      "ds_read_b128 a[56:59], v3 offset:47104\n"
      "v_mfma_f32_16x16x32_bf16 v[36:39], a[0:3], a[104:107], v[24:27]\n"
      "ds_read_b128 a[60:63], v4 offset:47104\n"
      "v_mfma_f32_16x16x32_bf16 v[36:39], a[4:7], a[108:111], v[36:39]\n"
      "v_mfma_f32_16x16x32_bf16 v[36:39], a[8:11], a[112:115], v[36:39]\n"
      "v_mfma_f32_16x16x32_bf16 v[36:39], a[12:15], a[116:119], v[36:39]\n"
      "v_mfma_f32_16x16x32_bf16 v[36:39], a[16:19], a[120:123], v[36:39]\n"
      "v_mfma_f32_16x16x32_bf16 v[36:39], a[20:23], a[124:127], v[36:39]\n"
      "v_mfma_f32_16x16x32_bf16 v[36:39], a[24:27], a[128:131], v[36:39]\n"
      "v_mfma_f32_16x16x32_bf16 v[36:39], a[28:31], a[132:135], v[36:39]\n"
      "v_mfma_f32_16x16x32_bf16 v[40:43], a[0:3], a[136:139], v[28:31]\n"
      "s_add_u32 m0, s23, 67456\n"
      "v_mfma_f32_16x16x32_bf16 v[40:43], a[4:7], a[140:143], v[40:43]\n"
      "buffer_load_dwordx4 v2, s[40:43], 0 offen offset:128 lds\n"
      "v_mfma_f32_16x16x32_bf16 v[40:43], a[8:11], a[144:147], v[40:43]\n"
      "v_exp_f32 v36, v36\n"
      "v_mfma_f32_16x16x32_bf16 v[40:43], a[12:15], a[148:151], v[40:43]\n"
      "v_add_f32 v6, v6, v36\n"
      "v_mfma_f32_16x16x32_bf16 v[40:43], a[16:19], a[152:155], v[40:43]\n"
      "v_exp_f32 v37, v37\n"
      "v_mfma_f32_16x16x32_bf16 v[40:43], a[20:23], a[156:159], v[40:43]\n"
      "v_cvt_pk_bf16_f32 v84, v36, v37\n"
      "v_add_f32 v6, v6, v37\n"
      "v_mfma_f32_16x16x32_bf16 v[40:43], a[24:27], a[160:163], v[40:43]\n"
      "v_exp_f32 v38, v38\n"
      "v_mfma_f32_16x16x32_bf16 v[40:43], a[28:31], a[164:167], v[40:43]\n"
      "v_add_f32 v6, v6, v38\n"
      "v_mfma_f32_16x16x32_bf16 v[44:47], a[0:3], a[168:171], v[32:35]\n"
      "s_add_u32 m0, s23, 68480\n"
      "v_exp_f32 v39, v39\n"
      "v_mfma_f32_16x16x32_bf16 v[44:47], a[4:7], a[172:175], v[44:47]\n"
      "buffer_load_dwordx4 v2, s[40:43], s52 offen offset:128 lds\n"
      "v_mfma_f32_16x16x32_bf16 v[44:47], a[8:11], a[176:179], v[44:47]\n"
      "v_exp_f32 v40, v40\n"
      "v_mfma_f32_16x16x32_bf16 v[44:47], a[12:15], a[180:183], v[44:47]\n"
      "ds_read_b128 a[0:3], v3 offset:49152\n"
      "v_cvt_pk_bf16_f32 v85, v38, v39\n"
      "v_mfma_f32_16x16x32_bf16 v[44:47], a[16:19], a[184:187], v[44:47]\n"
      "v_exp_f32 v41, v41\n"
      "v_mfma_f32_16x16x32_bf16 v[44:47], a[20:23], a[188:191], v[44:47]\n"
      "ds_read_b128 a[4:7], v4 offset:49152\n"
      "v_cvt_pk_bf16_f32 v92, v40, v41\n"
      "v_mfma_f32_16x16x32_bf16 v[44:47], a[24:27], a[192:195], v[44:47]\n"
      "v_exp_f32 v42, v42\n"
      "v_mfma_f32_16x16x32_bf16 v[44:47], a[28:31], a[196:199], v[44:47]\n"
      "s_waitcnt lgkmcnt(2)\n"
      "ds_read_b128 a[8:11], v3 offset:51200\n"
      "v_add_f32 v6, v6, v39\n"
      "v_mfma_f32_16x16x32_bf16 v[48:51], a[32:35], a[104:107], v[24:27]\n"
      "v_exp_f32 v43, v43\n"
      "v_mfma_f32_16x16x32_bf16 v[48:51], a[36:39], a[108:111], v[48:51]\n"
      "ds_read_b128 a[12:15], v4 offset:51200\n"
      "v_cvt_pk_bf16_f32 v93, v42, v43\n"
      "v_mfma_f32_16x16x32_bf16 v[48:51], a[40:43], a[112:115], v[48:51]\n"
      "v_exp_f32 v44, v44\n"
      "v_mfma_f32_16x16x32_bf16 v[48:51], a[44:47], a[116:119], v[48:51]\n"
      "ds_read_b128 a[16:19], v3 offset:53248\n"
      "v_add_f32 v8, v8, v40\n"
      "v_mfma_f32_16x16x32_bf16 v[48:51], a[48:51], a[120:123], v[48:51]\n"
      "s_add_u32 m0, s23, 69376\n"
      "v_exp_f32 v45, v45\n"
      "v_mfma_f32_16x16x32_bf16 v[48:51], a[52:55], a[124:127], v[48:51]\n"
      "buffer_load_dwordx4 v2, s[40:43], 0 offen offset:256 lds\n"
      "v_mfma_f32_16x16x32_bf16 v[48:51], a[56:59], a[128:131], v[48:51]\n"
      "v_exp_f32 v46, v46\n"
      "v_mfma_f32_16x16x32_bf16 v[48:51], a[60:63], a[132:135], v[48:51]\n"
      "ds_read_b128 a[20:23], v4 offset:53248\n"
      "v_cvt_pk_bf16_f32 v100, v44, v45\n"
      "v_mfma_f32_16x16x32_bf16 v[52:55], a[32:35], a[136:139], v[28:31]\n"
      "v_exp_f32 v47, v47\n"
      "v_mfma_f32_16x16x32_bf16 v[52:55], a[36:39], a[140:143], v[52:55]\n"
      "ds_read_b128 a[24:27], v3 offset:55296\n"
      "v_cvt_pk_bf16_f32 v101, v46, v47\n"
      "v_mfma_f32_16x16x32_bf16 v[52:55], a[40:43], a[144:147], v[52:55]\n"
      "v_exp_f32 v48, v48\n"
      "v_mfma_f32_16x16x32_bf16 v[52:55], a[44:47], a[148:151], v[52:55]\n"
      "ds_read_b128 a[28:31], v4 offset:55296\n"
      "v_add_f32 v8, v8, v41\n"
      "v_mfma_f32_16x16x32_bf16 v[52:55], a[48:51], a[152:155], v[52:55]\n"
      "s_add_u32 m0, s23, 70400\n"
      "v_exp_f32 v49, v49\n"
      "v_mfma_f32_16x16x32_bf16 v[52:55], a[52:55], a[156:159], v[52:55]\n"
      "buffer_load_dwordx4 v2, s[40:43], s52 offen offset:256 lds\n"
      "v_mfma_f32_16x16x32_bf16 v[52:55], a[56:59], a[160:163], v[52:55]\n"
      "v_exp_f32 v50, v50\n"
      "v_mfma_f32_16x16x32_bf16 v[52:55], a[60:63], a[164:167], v[52:55]\n"
      "v_cvt_pk_bf16_f32 v86, v48, v49\n"
      "v_add_f32 v8, v8, v42\n"
      "v_mfma_f32_16x16x32_bf16 v[56:59], a[32:35], a[168:171], v[32:35]\n"
      "v_exp_f32 v51, v51\n"
      "v_mfma_f32_16x16x32_bf16 v[56:59], a[36:39], a[172:175], v[56:59]\n"
      "ds_read_b128 a[32:35], v3 offset:57344\n"
      "v_cvt_pk_bf16_f32 v87, v50, v51\n"
      "v_mfma_f32_16x16x32_bf16 v[56:59], a[40:43], a[176:179], v[56:59]\n"
      "v_exp_f32 v52, v52\n"
      "v_mfma_f32_16x16x32_bf16 v[56:59], a[44:47], a[180:183], v[56:59]\n"
      "ds_read_b128 a[36:39], v4 offset:57344\n"
      "v_add_f32 v8, v8, v43\n"
      "v_mfma_f32_16x16x32_bf16 v[56:59], a[48:51], a[184:187], v[56:59]\n"
      "v_exp_f32 v53, v53\n"
      "v_mfma_f32_16x16x32_bf16 v[56:59], a[52:55], a[188:191], v[56:59]\n"
      "ds_read_b128 a[40:43], v3 offset:59392\n"
      "v_cvt_pk_bf16_f32 v94, v52, v53\n"
      "v_mfma_f32_16x16x32_bf16 v[56:59], a[56:59], a[192:195], v[56:59]\n"
      "v_exp_f32 v54, v54\n"
      "v_mfma_f32_16x16x32_bf16 v[56:59], a[60:63], a[196:199], v[56:59]\n"
      "s_waitcnt lgkmcnt(3)\n"
      "ds_read_b128 a[44:47], v4 offset:59392\n"
      "v_add_f32 v10, v10, v44\n"
      "v_mfma_f32_16x16x32_bf16 v[60:63], a[0:3], a[104:107], v[24:27]\n"
      "s_add_u32 m0, s23, 71296\n"
      "v_exp_f32 v55, v55\n"
      "v_mfma_f32_16x16x32_bf16 v[60:63], a[4:7], a[108:111], v[60:63]\n"
      "buffer_load_dwordx4 v2, s[40:43], 0 offen offset:384 lds\n"
      "v_mfma_f32_16x16x32_bf16 v[60:63], a[8:11], a[112:115], v[60:63]\n"
      "v_exp_f32 v56, v56\n"
      "v_mfma_f32_16x16x32_bf16 v[60:63], a[12:15], a[116:119], v[60:63]\n"
      "ds_read_b128 a[48:51], v3 offset:61440\n"
      "v_cvt_pk_bf16_f32 v95, v54, v55\n"
      "v_mfma_f32_16x16x32_bf16 v[60:63], a[16:19], a[120:123], v[60:63]\n"
      "v_exp_f32 v57, v57\n"
      "v_mfma_f32_16x16x32_bf16 v[60:63], a[20:23], a[124:127], v[60:63]\n"
      "ds_read_b128 a[52:55], v4 offset:61440\n"
      "v_cvt_pk_bf16_f32 v102, v56, v57\n"
      "v_mfma_f32_16x16x32_bf16 v[60:63], a[24:27], a[128:131], v[60:63]\n"
      "v_exp_f32 v58, v58\n"
      "v_mfma_f32_16x16x32_bf16 v[60:63], a[28:31], a[132:135], v[60:63]\n"
      "ds_read_b128 a[56:59], v3 offset:63488\n"
      "v_add_f32 v10, v10, v45\n"
      "v_mfma_f32_16x16x32_bf16 v[64:67], a[0:3], a[136:139], v[28:31]\n"
      "s_add_u32 m0, s23, 72320\n"
      "v_exp_f32 v59, v59\n"
      "v_mfma_f32_16x16x32_bf16 v[64:67], a[4:7], a[140:143], v[64:67]\n"
      "buffer_load_dwordx4 v2, s[40:43], s52 offen offset:384 lds\n"
      "v_mfma_f32_16x16x32_bf16 v[64:67], a[8:11], a[144:147], v[64:67]\n"
      "v_exp_f32 v60, v60\n"
      "v_mfma_f32_16x16x32_bf16 v[64:67], a[12:15], a[148:151], v[64:67]\n"
      "ds_read_b128 a[60:63], v4 offset:63488\n"
      "v_cvt_pk_bf16_f32 v103, v58, v59\n"
      "v_mfma_f32_16x16x32_bf16 v[64:67], a[16:19], a[152:155], v[64:67]\n"
      "v_exp_f32 v61, v61\n"
      "v_mfma_f32_16x16x32_bf16 v[64:67], a[20:23], a[156:159], v[64:67]\n"
      "v_cvt_pk_bf16_f32 v88, v60, v61\n"
      "v_add_u32 v2, 0x8000, v2\n"
      "v_mfma_f32_16x16x32_bf16 v[64:67], a[24:27], a[160:163], v[64:67]\n"
      "v_exp_f32 v62, v62\n"
      "v_mfma_f32_16x16x32_bf16 v[64:67], a[28:31], a[164:167], v[64:67]\n"
      "v_add_f32 v10, v10, v46\n"
      "v_add_f32 v10, v10, v47\n"
      "v_mfma_f32_16x16x32_bf16 v[68:71], a[0:3], a[168:171], v[32:35]\n"
      "v_exp_f32 v63, v63\n"
      "v_mfma_f32_16x16x32_bf16 v[68:71], a[4:7], a[172:175], v[68:71]\n"
      "v_cvt_pk_bf16_f32 v89, v62, v63\n"
      "v_add_f32 v6, v6, v48\n"
      "v_mfma_f32_16x16x32_bf16 v[68:71], a[8:11], a[176:179], v[68:71]\n"
      "s_waitcnt vmcnt(17) lgkmcnt(0)\n"
      "v_exp_f32 v64, v64\n"
      "v_mfma_f32_16x16x32_bf16 v[68:71], a[12:15], a[180:183], v[68:71]\n"
      "s_barrier\n"
      "ds_read_b64_tr_b16 a[64:65], v12 offset:32768\n"
      "v_add_f32 v6, v6, v49\n"
      "v_mfma_f32_16x16x32_bf16 v[68:71], a[16:19], a[184:187], v[68:71]\n"
      "v_exp_f32 v65, v65\n"
      "v_mfma_f32_16x16x32_bf16 v[68:71], a[20:23], a[188:191], v[68:71]\n"
      "ds_read_b64_tr_b16 a[66:67], v12 offset:40960\n"
      "v_cvt_pk_bf16_f32 v96, v64, v65\n"
      "v_mfma_f32_16x16x32_bf16 v[68:71], a[24:27], a[192:195], v[68:71]\n"
      "v_exp_f32 v66, v66\n"
      "v_mfma_f32_16x16x32_bf16 v[68:71], a[28:31], a[196:199], v[68:71]\n"
      "ds_read_b64_tr_b16 a[68:69], v12 offset:49152\n"
      "v_add_f32 v6, v6, v50\n"
      "v_mfma_f32_16x16x32_bf16 v[72:75], a[32:35], a[104:107], v[24:27]\n"
      "v_exp_f32 v67, v67\n"
      "v_mfma_f32_16x16x32_bf16 v[72:75], a[36:39], a[108:111], v[72:75]\n"
      "ds_read_b64_tr_b16 a[70:71], v12 offset:57344\n"
      "v_cvt_pk_bf16_f32 v97, v66, v67\n"
      "v_mfma_f32_16x16x32_bf16 v[72:75], a[40:43], a[112:115], v[72:75]\n"
      "v_exp_f32 v68, v68\n"
      "v_mfma_f32_16x16x32_bf16 v[72:75], a[44:47], a[116:119], v[72:75]\n"
      "ds_read_b64_tr_b16 a[72:73], v13 offset:32768\n"
      "v_add_f32 v6, v6, v51\n"
      "v_mfma_f32_16x16x32_bf16 v[72:75], a[48:51], a[120:123], v[72:75]\n"
      "v_exp_f32 v69, v69\n"
      "v_mfma_f32_16x16x32_bf16 v[72:75], a[52:55], a[124:127], v[72:75]\n"
      "ds_read_b64_tr_b16 a[74:75], v13 offset:40960\n"
      "v_cvt_pk_bf16_f32 v104, v68, v69\n"
      "v_mfma_f32_16x16x32_bf16 v[72:75], a[56:59], a[128:131], v[72:75]\n"
      "v_exp_f32 v70, v70\n"
      "v_mfma_f32_16x16x32_bf16 v[72:75], a[60:63], a[132:135], v[72:75]\n"
      "ds_read_b64_tr_b16 a[76:77], v13 offset:49152\n"
      "v_add_f32 v8, v8, v52\n"
      "v_mfma_f32_16x16x32_bf16 v[76:79], a[32:35], a[136:139], v[28:31]\n"
      "v_exp_f32 v71, v71\n"
      "v_mfma_f32_16x16x32_bf16 v[76:79], a[36:39], a[140:143], v[76:79]\n"
      "ds_read_b64_tr_b16 a[78:79], v13 offset:57344\n"
      "v_cvt_pk_bf16_f32 v105, v70, v71\n"
      "v_mfma_f32_16x16x32_bf16 v[76:79], a[40:43], a[144:147], v[76:79]\n"
      "v_exp_f32 v72, v72\n"
      "v_mfma_f32_16x16x32_bf16 v[76:79], a[44:47], a[148:151], v[76:79]\n"
      "ds_read_b64_tr_b16 a[80:81], v14 offset:32768\n"
      "v_add_f32 v8, v8, v53\n"
      "v_mfma_f32_16x16x32_bf16 v[76:79], a[48:51], a[152:155], v[76:79]\n"
      "v_exp_f32 v73, v73\n"
      "v_mfma_f32_16x16x32_bf16 v[76:79], a[52:55], a[156:159], v[76:79]\n"
      "ds_read_b64_tr_b16 a[82:83], v14 offset:40960\n"
      "v_cvt_pk_bf16_f32 v90, v72, v73\n"
      "v_mfma_f32_16x16x32_bf16 v[76:79], a[56:59], a[160:163], v[76:79]\n"
      "v_exp_f32 v74, v74\n"
      "v_mfma_f32_16x16x32_bf16 v[76:79], a[60:63], a[164:167], v[76:79]\n"
      "ds_read_b64_tr_b16 a[84:85], v14 offset:49152\n"
      "v_add_f32 v8, v8, v54\n"
      "v_mfma_f32_16x16x32_bf16 v[80:83], a[32:35], a[168:171], v[32:35]\n"
      "v_exp_f32 v75, v75\n"
      "v_mfma_f32_16x16x32_bf16 v[80:83], a[36:39], a[172:175], v[80:83]\n"
      "ds_read_b64_tr_b16 a[86:87], v14 offset:57344\n"
      "v_cvt_pk_bf16_f32 v91, v74, v75\n"
      "v_mfma_f32_16x16x32_bf16 v[80:83], a[40:43], a[176:179], v[80:83]\n"
      "v_exp_f32 v76, v76\n"
      "v_mfma_f32_16x16x32_bf16 v[80:83], a[44:47], a[180:183], v[80:83]\n"
      "v_add_f32 v8, v8, v55\n"
      "v_mfma_f32_16x16x32_bf16 v[80:83], a[48:51], a[184:187], v[80:83]\n"
      "v_exp_f32 v77, v77\n"
      "v_mfma_f32_16x16x32_bf16 v[80:83], a[52:55], a[188:191], v[80:83]\n"
      "v_cvt_pk_bf16_f32 v98, v76, v77\n"
      "v_mfma_f32_16x16x32_bf16 v[80:83], a[56:59], a[192:195], v[80:83]\n"
      "s_sub_u32 s20, s20, 1\n"
      "v_exp_f32 v78, v78\n"
      "v_mfma_f32_16x16x32_bf16 v[80:83], a[60:63], a[196:199], v[80:83]\n"
      "s_waitcnt lgkmcnt(8)\n"
      "s_cbranch_scc1 L_tail_%=\n"
      "v_mfma_f32_16x16x32_bf16 a[200:203], v[84:87], a[64:67], a[200:203]\n"
      "s_add_u32 m0, s23, 32768\n"
      "v_exp_f32 v79, v79\n"
      "v_mfma_f32_16x16x32_bf16 a[200:203], v[88:91], a[68:71], a[200:203]\n"
      "s_waitcnt lgkmcnt(4)\n"
      "buffer_load_dwordx4 v1, s[36:39], 0 offen offset:0 lds\n"
      "v_mfma_f32_16x16x32_bf16 a[212:215], v[84:87], a[72:75], a[212:215]\n"
      "v_exp_f32 v80, v80\n"
      "v_mfma_f32_16x16x32_bf16 a[212:215], v[88:91], a[76:79], a[212:215]\n"
      "s_waitcnt lgkmcnt(0)\n"
      "ds_read_b64_tr_b16 a[88:89], v15 offset:32768\n"
      "v_cvt_pk_bf16_f32 v99, v78, v79\n"
      "v_mfma_f32_16x16x32_bf16 a[224:227], v[84:87], a[80:83], a[224:227]\n"
      "v_exp_f32 v81, v81\n"
      "v_mfma_f32_16x16x32_bf16 a[224:227], v[88:91], a[84:87], a[224:227]\n"
      "ds_read_b64_tr_b16 a[90:91], v15 offset:40960\n"
      "v_cvt_pk_bf16_f32 v106, v80, v81\n"
      "v_mfma_f32_16x16x32_bf16 a[204:207], v[92:95], a[64:67], a[204:207]\n"
      "v_exp_f32 v82, v82\n"
      "v_mfma_f32_16x16x32_bf16 a[204:207], v[96:99], a[68:71], a[204:207]\n"
      "ds_read_b64_tr_b16 a[92:93], v15 offset:49152\n"
      "v_add_f32 v10, v10, v56\n"
      "v_mfma_f32_16x16x32_bf16 a[216:219], v[92:95], a[72:75], a[216:219]\n"
      "s_add_u32 m0, s23, 33792\n"
      "v_exp_f32 v83, v83\n"
      "v_mfma_f32_16x16x32_bf16 a[216:219], v[96:99], a[76:79], a[216:219]\n"
      "buffer_load_dwordx4 v1, s[36:39], s52 offen offset:0 lds\n"
      "v_mfma_f32_16x16x32_bf16 a[228:231], v[92:95], a[80:83], a[228:231]\n"
      "ds_read_b64_tr_b16 a[94:95], v15 offset:57344\n"
      "v_cvt_pk_bf16_f32 v107, v82, v83\n"
      "v_mfma_f32_16x16x32_bf16 a[228:231], v[96:99], a[84:87], a[228:231]\n"
      "ds_read_b64_tr_b16 a[96:97], v12 offset:34816\n"
      "v_add_f32 v10, v10, v57\n"
      "v_mfma_f32_16x16x32_bf16 a[208:211], v[100:103], a[64:67], a[208:211]\n"
      "ds_read_b64_tr_b16 a[98:99], v12 offset:43008\n"
      "v_add_f32 v10, v10, v58\n"
      "v_mfma_f32_16x16x32_bf16 a[208:211], v[104:107], a[68:71], a[208:211]\n"
      "ds_read_b64_tr_b16 a[100:101], v12 offset:51200\n"
      "v_add_f32 v10, v10, v59\n"
      "v_mfma_f32_16x16x32_bf16 a[220:223], v[100:103], a[72:75], a[220:223]\n"
      "ds_read_b64_tr_b16 a[102:103], v12 offset:59392\n"
      "v_add_f32 v6, v6, v60\n"
      "v_mfma_f32_16x16x32_bf16 a[220:223], v[104:107], a[76:79], a[220:223]\n"
      "ds_read_b64_tr_b16 a[64:65], v13 offset:34816\n"
      "v_add_f32 v6, v6, v61\n"
      "v_mfma_f32_16x16x32_bf16 a[232:235], v[100:103], a[80:83], a[232:235]\n"
      "ds_read_b64_tr_b16 a[66:67], v13 offset:43008\n"
      "v_add_f32 v6, v6, v62\n"
      "v_mfma_f32_16x16x32_bf16 a[232:235], v[104:107], a[84:87], a[232:235]\n"
      "s_waitcnt lgkmcnt(6)\n"
      "ds_read_b64_tr_b16 a[68:69], v13 offset:51200\n"
      "v_add_f32 v6, v6, v63\n"
      "v_mfma_f32_16x16x32_bf16 a[236:239], v[84:87], a[88:91], a[236:239]\n"
      "ds_read_b64_tr_b16 a[70:71], v13 offset:59392\n"
      "v_add_f32 v8, v8, v64\n"
      "v_mfma_f32_16x16x32_bf16 a[236:239], v[88:91], a[92:95], a[236:239]\n"
      "ds_read_b64_tr_b16 a[72:73], v14 offset:34816\n"
      "v_add_f32 v8, v8, v65\n"
      "v_mfma_f32_16x16x32_bf16 a[240:243], v[92:95], a[88:91], a[240:243]\n"
      "s_add_u32 m0, s23, 34688\n"
      "ds_read_b64_tr_b16 a[74:75], v14 offset:43008\n"
      "v_add_f32 v8, v8, v66\n"
      "v_mfma_f32_16x16x32_bf16 a[240:243], v[96:99], a[92:95], a[240:243]\n"
      "buffer_load_dwordx4 v1, s[36:39], 0 offen offset:128 lds\n"
      "v_mfma_f32_16x16x32_bf16 v[108:111], v[100:103], a[88:91], v[108:111]\n"
      "ds_read_b64_tr_b16 a[76:77], v14 offset:51200\n"
      "v_add_f32 v8, v8, v67\n"
      "v_mfma_f32_16x16x32_bf16 v[108:111], v[104:107], a[92:95], v[108:111]\n"
      "s_waitcnt lgkmcnt(7)\n"
      "ds_read_b64_tr_b16 a[78:79], v14 offset:59392\n"
      "v_add_f32 v10, v10, v68\n"
      "v_mfma_f32_16x16x32_bf16 v[112:115], v[84:87], a[96:99], v[112:115]\n"
      "ds_read_b64_tr_b16 a[80:81], v15 offset:34816\n"
      "v_add_f32 v10, v10, v69\n"
      "v_mfma_f32_16x16x32_bf16 v[112:115], v[88:91], a[100:103], v[112:115]\n"
      "ds_read_b64_tr_b16 a[82:83], v15 offset:43008\n"
      "v_add_f32 v10, v10, v70\n"
      "v_mfma_f32_16x16x32_bf16 v[116:119], v[92:95], a[96:99], v[116:119]\n"
      "ds_read_b64_tr_b16 a[84:85], v15 offset:51200\n"
      "v_add_f32 v10, v10, v71\n"
      "v_mfma_f32_16x16x32_bf16 v[116:119], v[96:99], a[100:103], v[116:119]\n"
      "ds_read_b64_tr_b16 a[86:87], v15 offset:59392\n"
      "v_add_f32 v6, v6, v72\n"
      "v_mfma_f32_16x16x32_bf16 v[120:123], v[100:103], a[96:99], v[120:123]\n"
      "s_add_u32 m0, s23, 35712\n"
      "ds_read_b64_tr_b16 a[88:89], v12 offset:36864\n"
      "v_add_f32 v6, v6, v73\n"
      "v_mfma_f32_16x16x32_bf16 v[120:123], v[104:107], a[100:103], v[120:123]\n"
      "s_waitcnt lgkmcnt(9)\n"
      "buffer_load_dwordx4 v1, s[36:39], s52 offen offset:128 lds\n"
      "v_mfma_f32_16x16x32_bf16 v[124:127], v[84:87], a[64:67], v[124:127]\n"
      "ds_read_b64_tr_b16 a[90:91], v12 offset:45056\n"
      "v_add_f32 v6, v6, v74\n"
      "v_mfma_f32_16x16x32_bf16 v[124:127], v[88:91], a[68:71], v[124:127]\n"
      "ds_read_b64_tr_b16 a[92:93], v12 offset:53248\n"
      "v_add_f32 v6, v6, v75\n"
      "v_mfma_f32_16x16x32_bf16 v[128:131], v[92:95], a[64:67], v[128:131]\n"
      "ds_read_b64_tr_b16 a[94:95], v12 offset:61440\n"
      "v_add_f32 v8, v8, v76\n"
      "v_mfma_f32_16x16x32_bf16 v[128:131], v[96:99], a[68:71], v[128:131]\n"
      "ds_read_b64_tr_b16 a[96:97], v13 offset:36864\n"
      "v_add_f32 v8, v8, v77\n"
      "v_mfma_f32_16x16x32_bf16 v[132:135], v[100:103], a[64:67], v[132:135]\n"
      "ds_read_b64_tr_b16 a[98:99], v13 offset:45056\n"
      "v_add_f32 v8, v8, v78\n"
      "v_mfma_f32_16x16x32_bf16 v[132:135], v[104:107], a[68:71], v[132:135]\n"
      "s_waitcnt lgkmcnt(10)\n"
      "ds_read_b64_tr_b16 a[100:101], v13 offset:53248\n"
      "v_add_f32 v8, v8, v79\n"
      "v_mfma_f32_16x16x32_bf16 v[136:139], v[84:87], a[72:75], v[136:139]\n"
      "s_add_u32 m0, s23, 36608\n"
      "ds_read_b64_tr_b16 a[102:103], v13 offset:61440\n"
      "v_add_f32 v10, v10, v80\n"
      "v_mfma_f32_16x16x32_bf16 v[136:139], v[88:91], a[76:79], v[136:139]\n"
      "buffer_load_dwordx4 v1, s[36:39], 0 offen offset:256 lds\n"
      "v_mfma_f32_16x16x32_bf16 v[140:143], v[92:95], a[72:75], v[140:143]\n"
      "ds_read_b64_tr_b16 a[64:65], v14 offset:36864\n"
      "v_add_f32 v10, v10, v81\n"
      "v_mfma_f32_16x16x32_bf16 v[140:143], v[96:99], a[76:79], v[140:143]\n"
      "ds_read_b64_tr_b16 a[66:67], v14 offset:45056\n"
      "v_add_f32 v10, v10, v82\n"
      "v_mfma_f32_16x16x32_bf16 v[144:147], v[100:103], a[72:75], v[144:147]\n"
      "ds_read_b64_tr_b16 a[68:69], v14 offset:53248\n"
      "v_add_f32 v10, v10, v83\n"
      "v_mfma_f32_16x16x32_bf16 v[144:147], v[104:107], a[76:79], v[144:147]\n"
      "s_waitcnt lgkmcnt(11)\n"
      "ds_read_b64_tr_b16 a[70:71], v14 offset:61440\n"
      "v_mfma_f32_16x16x32_bf16 v[148:151], v[84:87], a[80:83], v[148:151]\n"
      "ds_read_b64_tr_b16 a[72:73], v15 offset:36864\n"
      "v_mfma_f32_16x16x32_bf16 v[148:151], v[88:91], a[84:87], v[148:151]\n"
      "ds_read_b64_tr_b16 a[74:75], v15 offset:45056\n"
      "v_mfma_f32_16x16x32_bf16 v[152:155], v[92:95], a[80:83], v[152:155]\n"
      "s_add_u32 m0, s23, 37632\n"
      "ds_read_b64_tr_b16 a[76:77], v15 offset:53248\n"
      "v_mfma_f32_16x16x32_bf16 v[152:155], v[96:99], a[84:87], v[152:155]\n"
      "buffer_load_dwordx4 v1, s[36:39], s52 offen offset:256 lds\n"
      "v_mfma_f32_16x16x32_bf16 v[156:159], v[100:103], a[80:83], v[156:159]\n"
      "ds_read_b64_tr_b16 a[78:79], v15 offset:61440\n"
      "v_mfma_f32_16x16x32_bf16 v[156:159], v[104:107], a[84:87], v[156:159]\n"
      "s_waitcnt lgkmcnt(12)\n"
      "ds_read_b64_tr_b16 a[80:81], v12 offset:38912\n"
      "v_mfma_f32_16x16x32_bf16 v[160:163], v[84:87], a[88:91], v[160:163]\n"
      "ds_read_b64_tr_b16 a[82:83], v12 offset:47104\n"
      "v_mfma_f32_16x16x32_bf16 v[160:163], v[88:91], a[92:95], v[160:163]\n"
      "ds_read_b64_tr_b16 a[84:85], v12 offset:55296\n"
      "v_mfma_f32_16x16x32_bf16 v[164:167], v[92:95], a[88:91], v[164:167]\n"
      "ds_read_b64_tr_b16 a[86:87], v12 offset:63488\n"
      "v_mfma_f32_16x16x32_bf16 v[164:167], v[96:99], a[92:95], v[164:167]\n"
      "v_mfma_f32_16x16x32_bf16 v[168:171], v[100:103], a[88:91], v[168:171]\n"
      "v_mfma_f32_16x16x32_bf16 v[168:171], v[104:107], a[92:95], v[168:171]\n"
      "s_waitcnt lgkmcnt(12)\n"
      "ds_read_b64_tr_b16 a[88:89], v13 offset:38912\n"
      "v_mfma_f32_16x16x32_bf16 v[172:175], v[84:87], a[96:99], v[172:175]\n"
      "ds_read_b64_tr_b16 a[90:91], v13 offset:47104\n"
      "v_mfma_f32_16x16x32_bf16 v[172:175], v[88:91], a[100:103], v[172:175]\n"
      "ds_read_b64_tr_b16 a[92:93], v13 offset:55296\n"
      "v_mfma_f32_16x16x32_bf16 v[176:179], v[92:95], a[96:99], v[176:179]\n"
      "s_add_u32 m0, s23, 38528\n"
      "ds_read_b64_tr_b16 a[94:95], v13 offset:63488\n"
      "v_mfma_f32_16x16x32_bf16 v[176:179], v[96:99], a[100:103], v[176:179]\n"
      "buffer_load_dwordx4 v1, s[36:39], 0 offen offset:384 lds\n"
      "v_mfma_f32_16x16x32_bf16 v[180:183], v[100:103], a[96:99], v[180:183]\n"
      "v_mfma_f32_16x16x32_bf16 v[180:183], v[104:107], a[100:103], v[180:183]\n"
      "s_waitcnt lgkmcnt(12)\n"
      "ds_read_b64_tr_b16 a[96:97], v14 offset:38912\n"
      "v_mfma_f32_16x16x32_bf16 v[184:187], v[84:87], a[64:67], v[184:187]\n"
      "ds_read_b64_tr_b16 a[98:99], v14 offset:47104\n"
      "v_mfma_f32_16x16x32_bf16 v[184:187], v[88:91], a[68:71], v[184:187]\n"
      "ds_read_b64_tr_b16 a[100:101], v14 offset:55296\n"
      "v_mfma_f32_16x16x32_bf16 v[188:191], v[92:95], a[64:67], v[188:191]\n"
      "ds_read_b64_tr_b16 a[102:103], v14 offset:63488\n"
      "v_mfma_f32_16x16x32_bf16 v[188:191], v[96:99], a[68:71], v[188:191]\n"
      "v_mfma_f32_16x16x32_bf16 v[192:195], v[100:103], a[64:67], v[192:195]\n"
      "s_add_u32 m0, s23, 39552\n"
      "v_mfma_f32_16x16x32_bf16 v[192:195], v[104:107], a[68:71], v[192:195]\n"
      "s_waitcnt lgkmcnt(12)\n"
      "buffer_load_dwordx4 v1, s[36:39], s52 offen offset:384 lds\n"
      "v_mfma_f32_16x16x32_bf16 v[196:199], v[84:87], a[72:75], v[196:199]\n"
      "ds_read_b64_tr_b16 a[64:65], v15 offset:38912\n"
      "v_add_u32 v1, 0x8000, v1\n"
      "v_mfma_f32_16x16x32_bf16 v[196:199], v[88:91], a[76:79], v[196:199]\n"
      "ds_read_b64_tr_b16 a[66:67], v15 offset:47104\n"
      "v_mfma_f32_16x16x32_bf16 v[200:203], v[92:95], a[72:75], v[200:203]\n"
      "ds_read_b64_tr_b16 a[68:69], v15 offset:55296\n"
      "v_mfma_f32_16x16x32_bf16 v[200:203], v[96:99], a[76:79], v[200:203]\n"
      "buffer_load_dword v23, v22, s[60:63], 0 offen\n"
      "v_mfma_f32_16x16x32_bf16 v[204:207], v[100:103], a[72:75], v[204:207]\n"
      "ds_read_b64_tr_b16 a[70:71], v15 offset:63488\n"
      "v_add_u32 v22, 0x400, v22\n"
      "v_mfma_f32_16x16x32_bf16 v[204:207], v[104:107], a[76:79], v[204:207]\n"
      "s_waitcnt lgkmcnt(12)\n"
      "v_mfma_f32_16x16x32_bf16 v[208:211], v[84:87], a[80:83], v[208:211]\n"
      "v_mfma_f32_16x16x32_bf16 v[208:211], v[88:91], a[84:87], v[208:211]\n"
      "s_getreg_b32 s29, hwreg(HW_REG_IB_STS, 0, 4)\n"
      "v_mfma_f32_16x16x32_bf16 v[212:215], v[92:95], a[80:83], v[212:215]\n"
      "s_cmp_ge_u32 s29, 10\n"
      "v_mfma_f32_16x16x32_bf16 v[212:215], v[96:99], a[84:87], v[212:215]\n"
      "s_cbranch_scc1 L_topN1_%=\n"
      "s_branch L_topD1_%=\n"
      ".p2align 6\n"
      "L_topN1_%=:\n"
      "v_mfma_f32_16x16x32_bf16 v[216:219], v[100:103], a[80:83], v[216:219]\n"
      "v_mfma_f32_16x16x32_bf16 v[216:219], v[104:107], a[84:87], v[216:219]\n"
      "s_waitcnt lgkmcnt(8)\n"
      "v_mfma_f32_16x16x32_bf16 v[220:223], v[84:87], a[88:91], v[220:223]\n"
      "s_waitcnt vmcnt(17) lgkmcnt(0)\n"
      "v_mfma_f32_16x16x32_bf16 v[220:223], v[88:91], a[92:95], v[220:223]\n"
      "s_barrier\n"
      "ds_read_b128 a[0:3], v3 offset:0\n"
      "v_mfma_f32_16x16x32_bf16 v[224:227], v[92:95], a[88:91], v[224:227]\n"
      "ds_read_b128 a[4:7], v4 offset:0\n"
      "v_mfma_f32_16x16x32_bf16 v[224:227], v[96:99], a[92:95], v[224:227]\n"
      "ds_read_b128 a[8:11], v3 offset:2048\n"
      "v_mfma_f32_16x16x32_bf16 v[228:231], v[100:103], a[88:91], v[228:231]\n"
      "ds_read_b128 a[12:15], v4 offset:2048\n"
      "v_mfma_f32_16x16x32_bf16 v[228:231], v[104:107], a[92:95], v[228:231]\n"
      "ds_read_b128 a[16:19], v3 offset:4096\n"
      "v_mfma_f32_16x16x32_bf16 v[232:235], v[84:87], a[96:99], v[232:235]\n"
      "s_add_u32 m0, s23, 98304\n"
      "ds_read_b128 a[20:23], v4 offset:4096\n"
      "v_mfma_f32_16x16x32_bf16 v[232:235], v[88:91], a[100:103], v[232:235]\n"
      "buffer_load_dwordx4 v2, s[40:43], 0 offen offset:0 nt lds\n"
      "v_mfma_f32_16x16x32_bf16 v[236:239], v[92:95], a[96:99], v[236:239]\n"
      "ds_read_b128 a[24:27], v3 offset:6144\n"
      "v_mfma_f32_16x16x32_bf16 v[236:239], v[96:99], a[100:103], v[236:239]\n"
      "ds_read_b128 a[28:31], v4 offset:6144\n"
      "v_mfma_f32_16x16x32_bf16 v[240:243], v[100:103], a[96:99], v[240:243]\n"
      "ds_read_b128 a[32:35], v3 offset:8192\n"
      "v_mfma_f32_16x16x32_bf16 v[240:243], v[104:107], a[100:103], v[240:243]\n"
      "ds_read_b128 a[36:39], v4 offset:8192\n"
      "v_mfma_f32_16x16x32_bf16 v[244:247], v[84:87], a[64:67], v[244:247]\n"
      "ds_read_b128 a[40:43], v3 offset:10240\n"
      "v_mfma_f32_16x16x32_bf16 v[244:247], v[88:91], a[68:71], v[244:247]\n"
      "ds_read_b128 a[44:47], v4 offset:10240\n"
      "v_mfma_f32_16x16x32_bf16 v[248:251], v[92:95], a[64:67], v[248:251]\n"
      "s_add_u32 m0, s23, 99328\n"
      "ds_read_b128 a[48:51], v3 offset:12288\n"
      "v_mfma_f32_16x16x32_bf16 v[248:251], v[96:99], a[68:71], v[248:251]\n"
      "buffer_load_dwordx4 v2, s[40:43], s52 offen offset:0 nt lds\n"
      "v_mfma_f32_16x16x32_bf16 v[252:255], v[100:103], a[64:67], v[252:255]\n"
      "ds_read_b128 a[52:55], v4 offset:12288\n"
      "v_mfma_f32_16x16x32_bf16 v[252:255], v[104:107], a[68:71], v[252:255]\n"
      "s_waitcnt lgkmcnt(6)\n"
      "ds_read_b128 a[56:59], v3 offset:14336\n"
      "v_mfma_f32_16x16x32_bf16 v[36:39], a[0:3], a[104:107], v[24:27]\n"
      "ds_read_b128 a[60:63], v4 offset:14336\n"
      "v_mfma_f32_16x16x32_bf16 v[36:39], a[4:7], a[108:111], v[36:39]\n"
      "v_mfma_f32_16x16x32_bf16 v[36:39], a[8:11], a[112:115], v[36:39]\n"
      "v_mfma_f32_16x16x32_bf16 v[36:39], a[12:15], a[116:119], v[36:39]\n"
      "v_mfma_f32_16x16x32_bf16 v[36:39], a[16:19], a[120:123], v[36:39]\n"
      "v_mfma_f32_16x16x32_bf16 v[36:39], a[20:23], a[124:127], v[36:39]\n"
      "v_mfma_f32_16x16x32_bf16 v[36:39], a[24:27], a[128:131], v[36:39]\n"
      "v_mfma_f32_16x16x32_bf16 v[36:39], a[28:31], a[132:135], v[36:39]\n"
      "v_mfma_f32_16x16x32_bf16 v[40:43], a[0:3], a[136:139], v[28:31]\n"
      "s_add_u32 m0, s23, 100224\n"
      "v_mfma_f32_16x16x32_bf16 v[40:43], a[4:7], a[140:143], v[40:43]\n"
      "buffer_load_dwordx4 v2, s[40:43], 0 offen offset:128 nt lds\n"
      "v_mfma_f32_16x16x32_bf16 v[40:43], a[8:11], a[144:147], v[40:43]\n"
      "v_exp_f32 v36, v36\n"
      "v_mfma_f32_16x16x32_bf16 v[40:43], a[12:15], a[148:151], v[40:43]\n"
      "v_add_f32 v6, v6, v36\n"
      "v_mfma_f32_16x16x32_bf16 v[40:43], a[16:19], a[152:155], v[40:43]\n"
      "v_exp_f32 v37, v37\n"
      "v_mfma_f32_16x16x32_bf16 v[40:43], a[20:23], a[156:159], v[40:43]\n"
      "v_cvt_pk_bf16_f32 v84, v36, v37\n"
      "v_add_f32 v6, v6, v37\n"
      "v_mfma_f32_16x16x32_bf16 v[40:43], a[24:27], a[160:163], v[40:43]\n"
      "v_exp_f32 v38, v38\n"
      "v_mfma_f32_16x16x32_bf16 v[40:43], a[28:31], a[164:167], v[40:43]\n"
      "v_add_f32 v6, v6, v38\n"
      "v_mfma_f32_16x16x32_bf16 v[44:47], a[0:3], a[168:171], v[32:35]\n"
      "s_add_u32 m0, s23, 101248\n"
      "v_exp_f32 v39, v39\n"
      "v_mfma_f32_16x16x32_bf16 v[44:47], a[4:7], a[172:175], v[44:47]\n"
      "buffer_load_dwordx4 v2, s[40:43], s52 offen offset:128 nt lds\n"
      "v_mfma_f32_16x16x32_bf16 v[44:47], a[8:11], a[176:179], v[44:47]\n"
      "v_exp_f32 v40, v40\n"
      "v_mfma_f32_16x16x32_bf16 v[44:47], a[12:15], a[180:183], v[44:47]\n"
      "ds_read_b128 a[0:3], v3 offset:16384\n"
      "v_cvt_pk_bf16_f32 v85, v38, v39\n"
      "v_mfma_f32_16x16x32_bf16 v[44:47], a[16:19], a[184:187], v[44:47]\n"
      "v_exp_f32 v41, v41\n"
      "v_mfma_f32_16x16x32_bf16 v[44:47], a[20:23], a[188:191], v[44:47]\n"
      "ds_read_b128 a[4:7], v4 offset:16384\n"
      "v_cvt_pk_bf16_f32 v92, v40, v41\n"
      "v_mfma_f32_16x16x32_bf16 v[44:47], a[24:27], a[192:195], v[44:47]\n"
      "v_exp_f32 v42, v42\n"
      "v_mfma_f32_16x16x32_bf16 v[44:47], a[28:31], a[196:199], v[44:47]\n"
      "s_waitcnt lgkmcnt(2)\n"
      "ds_read_b128 a[8:11], v3 offset:18432\n"
      "v_add_f32 v6, v6, v39\n"
      "v_mfma_f32_16x16x32_bf16 v[48:51], a[32:35], a[104:107], v[24:27]\n"
      "v_exp_f32 v43, v43\n"
      "v_mfma_f32_16x16x32_bf16 v[48:51], a[36:39], a[108:111], v[48:51]\n"
      "ds_read_b128 a[12:15], v4 offset:18432\n"
      "v_cvt_pk_bf16_f32 v93, v42, v43\n"
      "v_mfma_f32_16x16x32_bf16 v[48:51], a[40:43], a[112:115], v[48:51]\n"
      "v_exp_f32 v44, v44\n"
      "v_mfma_f32_16x16x32_bf16 v[48:51], a[44:47], a[116:119], v[48:51]\n"
      "ds_read_b128 a[16:19], v3 offset:20480\n"
      "v_add_f32 v8, v8, v40\n"
      "v_mfma_f32_16x16x32_bf16 v[48:51], a[48:51], a[120:123], v[48:51]\n"
      "s_add_u32 m0, s23, 102144\n"
      "v_exp_f32 v45, v45\n"
      "v_mfma_f32_16x16x32_bf16 v[48:51], a[52:55], a[124:127], v[48:51]\n"
      "buffer_load_dwordx4 v2, s[40:43], 0 offen offset:256 nt lds\n"
      "v_mfma_f32_16x16x32_bf16 v[48:51], a[56:59], a[128:131], v[48:51]\n"
      "v_exp_f32 v46, v46\n"
      "v_mfma_f32_16x16x32_bf16 v[48:51], a[60:63], a[132:135], v[48:51]\n"
      "ds_read_b128 a[20:23], v4 offset:20480\n"
      "v_cvt_pk_bf16_f32 v100, v44, v45\n"
      "v_mfma_f32_16x16x32_bf16 v[52:55], a[32:35], a[136:139], v[28:31]\n"
      "v_exp_f32 v47, v47\n"
      "v_mfma_f32_16x16x32_bf16 v[52:55], a[36:39], a[140:143], v[52:55]\n"
      "ds_read_b128 a[24:27], v3 offset:22528\n"
      "v_cvt_pk_bf16_f32 v101, v46, v47\n"
      "v_mfma_f32_16x16x32_bf16 v[52:55], a[40:43], a[144:147], v[52:55]\n"
      "v_exp_f32 v48, v48\n"
      "v_mfma_f32_16x16x32_bf16 v[52:55], a[44:47], a[148:151], v[52:55]\n"
      "ds_read_b128 a[28:31], v4 offset:22528\n"
      "v_add_f32 v8, v8, v41\n"
      "v_mfma_f32_16x16x32_bf16 v[52:55], a[48:51], a[152:155], v[52:55]\n"
      "s_add_u32 m0, s23, 103168\n"
      "v_exp_f32 v49, v49\n"
      "v_mfma_f32_16x16x32_bf16 v[52:55], a[52:55], a[156:159], v[52:55]\n"
      "buffer_load_dwordx4 v2, s[40:43], s52 offen offset:256 nt lds\n"
      "v_mfma_f32_16x16x32_bf16 v[52:55], a[56:59], a[160:163], v[52:55]\n"
      "v_exp_f32 v50, v50\n"
      "v_mfma_f32_16x16x32_bf16 v[52:55], a[60:63], a[164:167], v[52:55]\n"
      "v_cvt_pk_bf16_f32 v86, v48, v49\n"
      "v_add_f32 v8, v8, v42\n"
      "v_mfma_f32_16x16x32_bf16 v[56:59], a[32:35], a[168:171], v[32:35]\n"
      "v_exp_f32 v51, v51\n"
      "v_mfma_f32_16x16x32_bf16 v[56:59], a[36:39], a[172:175], v[56:59]\n"
      "ds_read_b128 a[32:35], v3 offset:24576\n"
      "v_cvt_pk_bf16_f32 v87, v50, v51\n"
      "v_mfma_f32_16x16x32_bf16 v[56:59], a[40:43], a[176:179], v[56:59]\n"
      "v_exp_f32 v52, v52\n"
      "v_mfma_f32_16x16x32_bf16 v[56:59], a[44:47], a[180:183], v[56:59]\n"
      "ds_read_b128 a[36:39], v4 offset:24576\n"
      "v_add_f32 v8, v8, v43\n"
      "v_mfma_f32_16x16x32_bf16 v[56:59], a[48:51], a[184:187], v[56:59]\n"
      "v_exp_f32 v53, v53\n"
      "v_mfma_f32_16x16x32_bf16 v[56:59], a[52:55], a[188:191], v[56:59]\n"
      "ds_read_b128 a[40:43], v3 offset:26624\n"
      "v_cvt_pk_bf16_f32 v94, v52, v53\n"
      "v_mfma_f32_16x16x32_bf16 v[56:59], a[56:59], a[192:195], v[56:59]\n"
      "v_exp_f32 v54, v54\n"
      "v_mfma_f32_16x16x32_bf16 v[56:59], a[60:63], a[196:199], v[56:59]\n"
      "s_waitcnt lgkmcnt(3)\n"
      "ds_read_b128 a[44:47], v4 offset:26624\n"
      "v_add_f32 v10, v10, v44\n"
      "v_mfma_f32_16x16x32_bf16 v[60:63], a[0:3], a[104:107], v[24:27]\n"
      "s_add_u32 m0, s23, 104064\n"
      "v_exp_f32 v55, v55\n"
      "v_mfma_f32_16x16x32_bf16 v[60:63], a[4:7], a[108:111], v[60:63]\n"
      "buffer_load_dwordx4 v2, s[40:43], 0 offen offset:384 nt lds\n"
      "v_mfma_f32_16x16x32_bf16 v[60:63], a[8:11], a[112:115], v[60:63]\n"
      "v_exp_f32 v56, v56\n"
      "v_mfma_f32_16x16x32_bf16 v[60:63], a[12:15], a[116:119], v[60:63]\n"
      "ds_read_b128 a[48:51], v3 offset:28672\n"
      "v_cvt_pk_bf16_f32 v95, v54, v55\n"
      "v_mfma_f32_16x16x32_bf16 v[60:63], a[16:19], a[120:123], v[60:63]\n"
      "v_exp_f32 v57, v57\n"
      "v_mfma_f32_16x16x32_bf16 v[60:63], a[20:23], a[124:127], v[60:63]\n"
      "ds_read_b128 a[52:55], v4 offset:28672\n"
      "v_cvt_pk_bf16_f32 v102, v56, v57\n"
      "v_mfma_f32_16x16x32_bf16 v[60:63], a[24:27], a[128:131], v[60:63]\n"
      "v_exp_f32 v58, v58\n"
      "v_mfma_f32_16x16x32_bf16 v[60:63], a[28:31], a[132:135], v[60:63]\n"
      "ds_read_b128 a[56:59], v3 offset:30720\n"
      "v_add_f32 v10, v10, v45\n"
      "v_mfma_f32_16x16x32_bf16 v[64:67], a[0:3], a[136:139], v[28:31]\n"
      "s_add_u32 m0, s23, 105088\n"
      "v_exp_f32 v59, v59\n"
      "v_mfma_f32_16x16x32_bf16 v[64:67], a[4:7], a[140:143], v[64:67]\n"
      "buffer_load_dwordx4 v2, s[40:43], s52 offen offset:384 nt lds\n"
      "v_mfma_f32_16x16x32_bf16 v[64:67], a[8:11], a[144:147], v[64:67]\n"
      "v_exp_f32 v60, v60\n"
      "v_mfma_f32_16x16x32_bf16 v[64:67], a[12:15], a[148:151], v[64:67]\n"
      "ds_read_b128 a[60:63], v4 offset:30720\n"
      "v_cvt_pk_bf16_f32 v103, v58, v59\n"
      "v_mfma_f32_16x16x32_bf16 v[64:67], a[16:19], a[152:155], v[64:67]\n"
      "v_exp_f32 v61, v61\n"
      "v_mfma_f32_16x16x32_bf16 v[64:67], a[20:23], a[156:159], v[64:67]\n"
      "v_cvt_pk_bf16_f32 v88, v60, v61\n"
      "v_add_u32 v2, 0x8000, v2\n"
      "v_mfma_f32_16x16x32_bf16 v[64:67], a[24:27], a[160:163], v[64:67]\n"
      "v_exp_f32 v62, v62\n"
      "v_mfma_f32_16x16x32_bf16 v[64:67], a[28:31], a[164:167], v[64:67]\n"
      "v_add_f32 v10, v10, v46\n"
      "v_add_f32 v10, v10, v47\n"
      "v_mfma_f32_16x16x32_bf16 v[68:71], a[0:3], a[168:171], v[32:35]\n"
      "v_exp_f32 v63, v63\n"
      "v_mfma_f32_16x16x32_bf16 v[68:71], a[4:7], a[172:175], v[68:71]\n"
      "v_cvt_pk_bf16_f32 v89, v62, v63\n"
      "v_add_f32 v6, v6, v48\n"
      "v_mfma_f32_16x16x32_bf16 v[68:71], a[8:11], a[176:179], v[68:71]\n"
      "s_waitcnt vmcnt(17) lgkmcnt(0)\n"
      "v_exp_f32 v64, v64\n"
      "v_mfma_f32_16x16x32_bf16 v[68:71], a[12:15], a[180:183], v[68:71]\n"
      "s_barrier\n"
      "ds_read_b64_tr_b16 a[64:65], v12 offset:0\n"
      "v_add_f32 v6, v6, v49\n"
      "v_mfma_f32_16x16x32_bf16 v[68:71], a[16:19], a[184:187], v[68:71]\n"
      "v_exp_f32 v65, v65\n"
      "v_mfma_f32_16x16x32_bf16 v[68:71], a[20:23], a[188:191], v[68:71]\n"
      "ds_read_b64_tr_b16 a[66:67], v12 offset:8192\n"
      "v_cvt_pk_bf16_f32 v96, v64, v65\n"
      "v_mfma_f32_16x16x32_bf16 v[68:71], a[24:27], a[192:195], v[68:71]\n"
      "v_exp_f32 v66, v66\n"
      "v_mfma_f32_16x16x32_bf16 v[68:71], a[28:31], a[196:199], v[68:71]\n"
      "ds_read_b64_tr_b16 a[68:69], v12 offset:16384\n"
      "v_add_f32 v6, v6, v50\n"
      "v_mfma_f32_16x16x32_bf16 v[72:75], a[32:35], a[104:107], v[24:27]\n"
      "v_exp_f32 v67, v67\n"
      "v_mfma_f32_16x16x32_bf16 v[72:75], a[36:39], a[108:111], v[72:75]\n"
      "ds_read_b64_tr_b16 a[70:71], v12 offset:24576\n"
      "v_cvt_pk_bf16_f32 v97, v66, v67\n"
      "v_mfma_f32_16x16x32_bf16 v[72:75], a[40:43], a[112:115], v[72:75]\n"
      "v_exp_f32 v68, v68\n"
      "v_mfma_f32_16x16x32_bf16 v[72:75], a[44:47], a[116:119], v[72:75]\n"
      "ds_read_b64_tr_b16 a[72:73], v13 offset:0\n"
      "v_add_f32 v6, v6, v51\n"
      "v_mfma_f32_16x16x32_bf16 v[72:75], a[48:51], a[120:123], v[72:75]\n"
      "v_exp_f32 v69, v69\n"
      "v_mfma_f32_16x16x32_bf16 v[72:75], a[52:55], a[124:127], v[72:75]\n"
      "ds_read_b64_tr_b16 a[74:75], v13 offset:8192\n"
      "v_cvt_pk_bf16_f32 v104, v68, v69\n"
      "v_mfma_f32_16x16x32_bf16 v[72:75], a[56:59], a[128:131], v[72:75]\n"
      "v_exp_f32 v70, v70\n"
      "v_mfma_f32_16x16x32_bf16 v[72:75], a[60:63], a[132:135], v[72:75]\n"
      "ds_read_b64_tr_b16 a[76:77], v13 offset:16384\n"
      "v_add_f32 v8, v8, v52\n"
      "v_mfma_f32_16x16x32_bf16 v[76:79], a[32:35], a[136:139], v[28:31]\n"
      "v_exp_f32 v71, v71\n"
      "v_mfma_f32_16x16x32_bf16 v[76:79], a[36:39], a[140:143], v[76:79]\n"
      "ds_read_b64_tr_b16 a[78:79], v13 offset:24576\n"
      "v_cvt_pk_bf16_f32 v105, v70, v71\n"
      "v_mfma_f32_16x16x32_bf16 v[76:79], a[40:43], a[144:147], v[76:79]\n"
      "v_exp_f32 v72, v72\n"
      "v_mfma_f32_16x16x32_bf16 v[76:79], a[44:47], a[148:151], v[76:79]\n"
      "ds_read_b64_tr_b16 a[80:81], v14 offset:0\n"
      "v_add_f32 v8, v8, v53\n"
      "v_mfma_f32_16x16x32_bf16 v[76:79], a[48:51], a[152:155], v[76:79]\n"
      "v_exp_f32 v73, v73\n"
      "v_mfma_f32_16x16x32_bf16 v[76:79], a[52:55], a[156:159], v[76:79]\n"
      "ds_read_b64_tr_b16 a[82:83], v14 offset:8192\n"
      "v_cvt_pk_bf16_f32 v90, v72, v73\n"
      "v_mfma_f32_16x16x32_bf16 v[76:79], a[56:59], a[160:163], v[76:79]\n"
      "v_exp_f32 v74, v74\n"
      "v_mfma_f32_16x16x32_bf16 v[76:79], a[60:63], a[164:167], v[76:79]\n"
      "ds_read_b64_tr_b16 a[84:85], v14 offset:16384\n"
      "v_add_f32 v8, v8, v54\n"
      "v_mfma_f32_16x16x32_bf16 v[80:83], a[32:35], a[168:171], v[32:35]\n"
      "v_exp_f32 v75, v75\n"
      "v_mfma_f32_16x16x32_bf16 v[80:83], a[36:39], a[172:175], v[80:83]\n"
      "ds_read_b64_tr_b16 a[86:87], v14 offset:24576\n"
      "v_cvt_pk_bf16_f32 v91, v74, v75\n"
      "v_mfma_f32_16x16x32_bf16 v[80:83], a[40:43], a[176:179], v[80:83]\n"
      "v_exp_f32 v76, v76\n"
      "v_mfma_f32_16x16x32_bf16 v[80:83], a[44:47], a[180:183], v[80:83]\n"
      "v_add_f32 v8, v8, v55\n"
      "v_mfma_f32_16x16x32_bf16 v[80:83], a[48:51], a[184:187], v[80:83]\n"
      "v_exp_f32 v77, v77\n"
      "v_mfma_f32_16x16x32_bf16 v[80:83], a[52:55], a[188:191], v[80:83]\n"
      "v_cvt_pk_bf16_f32 v98, v76, v77\n"
      "v_mfma_f32_16x16x32_bf16 v[80:83], a[56:59], a[192:195], v[80:83]\n"
      "v_exp_f32 v78, v78\n"
      "v_mfma_f32_16x16x32_bf16 v[80:83], a[60:63], a[196:199], v[80:83]\n"
      "s_waitcnt lgkmcnt(8)\n"
      "L_loopN1_%=:\n"
      "v_mfma_f32_16x16x32_bf16 a[200:203], v[84:87], a[64:67], a[200:203]\n"
      "s_add_u32 m0, s23, 0\n"
      "v_exp_f32 v79, v79\n"
      "v_mfma_f32_16x16x32_bf16 a[200:203], v[88:91], a[68:71], a[200:203]\n"
      "s_waitcnt lgkmcnt(4)\n"
      "buffer_load_dwordx4 v1, s[36:39], 0 offen offset:0 nt lds\n"
      "v_mfma_f32_16x16x32_bf16 a[212:215], v[84:87], a[72:75], a[212:215]\n"
      "v_exp_f32 v80, v80\n"
      "v_mfma_f32_16x16x32_bf16 a[212:215], v[88:91], a[76:79], a[212:215]\n"
      "s_waitcnt lgkmcnt(0)\n"
      "ds_read_b64_tr_b16 a[88:89], v15 offset:0\n"
      "v_cvt_pk_bf16_f32 v99, v78, v79\n"
      "v_mfma_f32_16x16x32_bf16 a[224:227], v[84:87], a[80:83], a[224:227]\n"
      "v_exp_f32 v81, v81\n"
      "v_mfma_f32_16x16x32_bf16 a[224:227], v[88:91], a[84:87], a[224:227]\n"
      "ds_read_b64_tr_b16 a[90:91], v15 offset:8192\n"
      "v_cvt_pk_bf16_f32 v106, v80, v81\n"
      "v_mfma_f32_16x16x32_bf16 a[204:207], v[92:95], a[64:67], a[204:207]\n"
      "v_exp_f32 v82, v82\n"
      "v_mfma_f32_16x16x32_bf16 a[204:207], v[96:99], a[68:71], a[204:207]\n"
      "ds_read_b64_tr_b16 a[92:93], v15 offset:16384\n"
      "v_add_f32 v10, v10, v56\n"
      "v_mfma_f32_16x16x32_bf16 a[216:219], v[92:95], a[72:75], a[216:219]\n"
      "s_add_u32 m0, s23, 1024\n"
      "v_exp_f32 v83, v83\n"
      "v_mfma_f32_16x16x32_bf16 a[216:219], v[96:99], a[76:79], a[216:219]\n"
      "buffer_load_dwordx4 v1, s[36:39], s52 offen offset:0 nt lds\n"
      "v_mfma_f32_16x16x32_bf16 a[228:231], v[92:95], a[80:83], a[228:231]\n"
      "ds_read_b64_tr_b16 a[94:95], v15 offset:24576\n"
      "v_cvt_pk_bf16_f32 v107, v82, v83\n"
      "v_mfma_f32_16x16x32_bf16 a[228:231], v[96:99], a[84:87], a[228:231]\n"
      "ds_read_b64_tr_b16 a[96:97], v12 offset:2048\n"
      "v_add_f32 v10, v10, v57\n"
      "v_mfma_f32_16x16x32_bf16 a[208:211], v[100:103], a[64:67], a[208:211]\n"
      "ds_read_b64_tr_b16 a[98:99], v12 offset:10240\n"
      "v_add_f32 v10, v10, v58\n"
      "v_mfma_f32_16x16x32_bf16 a[208:211], v[104:107], a[68:71], a[208:211]\n"
      "ds_read_b64_tr_b16 a[100:101], v12 offset:18432\n"
      "v_add_f32 v10, v10, v59\n"
      "v_mfma_f32_16x16x32_bf16 a[220:223], v[100:103], a[72:75], a[220:223]\n"
      "ds_read_b64_tr_b16 a[102:103], v12 offset:26624\n"
      "v_add_f32 v6, v6, v60\n"
      "v_mfma_f32_16x16x32_bf16 a[220:223], v[104:107], a[76:79], a[220:223]\n"
      "ds_read_b64_tr_b16 a[64:65], v13 offset:2048\n"
      "v_add_f32 v6, v6, v61\n"
      "v_mfma_f32_16x16x32_bf16 a[232:235], v[100:103], a[80:83], a[232:235]\n"
      "ds_read_b64_tr_b16 a[66:67], v13 offset:10240\n"
      "v_add_f32 v6, v6, v62\n"
      "v_mfma_f32_16x16x32_bf16 a[232:235], v[104:107], a[84:87], a[232:235]\n"
      "s_waitcnt lgkmcnt(6)\n"
      "ds_read_b64_tr_b16 a[68:69], v13 offset:18432\n"
      "v_add_f32 v6, v6, v63\n"
      "v_mfma_f32_16x16x32_bf16 a[236:239], v[84:87], a[88:91], a[236:239]\n"
      "ds_read_b64_tr_b16 a[70:71], v13 offset:26624\n"
      "v_add_f32 v8, v8, v64\n"
      "v_mfma_f32_16x16x32_bf16 a[236:239], v[88:91], a[92:95], a[236:239]\n"
      "ds_read_b64_tr_b16 a[72:73], v14 offset:2048\n"
      "v_add_f32 v8, v8, v65\n"
      "v_mfma_f32_16x16x32_bf16 a[240:243], v[92:95], a[88:91], a[240:243]\n"
      "s_add_u32 m0, s23, 1920\n"
      "ds_read_b64_tr_b16 a[74:75], v14 offset:10240\n"
      "v_add_f32 v8, v8, v66\n"
      "v_mfma_f32_16x16x32_bf16 a[240:243], v[96:99], a[92:95], a[240:243]\n"
      "buffer_load_dwordx4 v1, s[36:39], 0 offen offset:128 nt lds\n"
      "v_mfma_f32_16x16x32_bf16 v[108:111], v[100:103], a[88:91], v[108:111]\n"
      "ds_read_b64_tr_b16 a[76:77], v14 offset:18432\n"
      "v_add_f32 v8, v8, v67\n"
      "v_mfma_f32_16x16x32_bf16 v[108:111], v[104:107], a[92:95], v[108:111]\n"
      "s_waitcnt lgkmcnt(7)\n"
      "ds_read_b64_tr_b16 a[78:79], v14 offset:26624\n"
      "v_add_f32 v10, v10, v68\n"
      "v_mfma_f32_16x16x32_bf16 v[112:115], v[84:87], a[96:99], v[112:115]\n"
      "ds_read_b64_tr_b16 a[80:81], v15 offset:2048\n"
      "v_add_f32 v10, v10, v69\n"
      "v_mfma_f32_16x16x32_bf16 v[112:115], v[88:91], a[100:103], v[112:115]\n"
      "ds_read_b64_tr_b16 a[82:83], v15 offset:10240\n"
      "v_add_f32 v10, v10, v70\n"
      "v_mfma_f32_16x16x32_bf16 v[116:119], v[92:95], a[96:99], v[116:119]\n"
      "ds_read_b64_tr_b16 a[84:85], v15 offset:18432\n"
      "v_add_f32 v10, v10, v71\n"
      "v_mfma_f32_16x16x32_bf16 v[116:119], v[96:99], a[100:103], v[116:119]\n"
      "ds_read_b64_tr_b16 a[86:87], v15 offset:26624\n"
      "v_add_f32 v6, v6, v72\n"
      "v_mfma_f32_16x16x32_bf16 v[120:123], v[100:103], a[96:99], v[120:123]\n"
      "s_add_u32 m0, s23, 2944\n"
      "ds_read_b64_tr_b16 a[88:89], v12 offset:4096\n"
      "v_add_f32 v6, v6, v73\n"
      "v_mfma_f32_16x16x32_bf16 v[120:123], v[104:107], a[100:103], v[120:123]\n"
      "s_waitcnt lgkmcnt(9)\n"
      "buffer_load_dwordx4 v1, s[36:39], s52 offen offset:128 nt lds\n"
      "v_mfma_f32_16x16x32_bf16 v[124:127], v[84:87], a[64:67], v[124:127]\n"
      "ds_read_b64_tr_b16 a[90:91], v12 offset:12288\n"
      "v_add_f32 v6, v6, v74\n"
      "v_mfma_f32_16x16x32_bf16 v[124:127], v[88:91], a[68:71], v[124:127]\n"
      "ds_read_b64_tr_b16 a[92:93], v12 offset:20480\n"
      "v_add_f32 v6, v6, v75\n"
      "v_mfma_f32_16x16x32_bf16 v[128:131], v[92:95], a[64:67], v[128:131]\n"
      "ds_read_b64_tr_b16 a[94:95], v12 offset:28672\n"
      "v_add_f32 v8, v8, v76\n"
      "v_mfma_f32_16x16x32_bf16 v[128:131], v[96:99], a[68:71], v[128:131]\n"
      "ds_read_b64_tr_b16 a[96:97], v13 offset:4096\n"
      "v_add_f32 v8, v8, v77\n"
      "v_mfma_f32_16x16x32_bf16 v[132:135], v[100:103], a[64:67], v[132:135]\n"
      "ds_read_b64_tr_b16 a[98:99], v13 offset:12288\n"
      "v_add_f32 v8, v8, v78\n"
      "v_mfma_f32_16x16x32_bf16 v[132:135], v[104:107], a[68:71], v[132:135]\n"
      "s_waitcnt lgkmcnt(10)\n"
      "ds_read_b64_tr_b16 a[100:101], v13 offset:20480\n"
      "v_add_f32 v8, v8, v79\n"
      "v_mfma_f32_16x16x32_bf16 v[136:139], v[84:87], a[72:75], v[136:139]\n"
      "s_add_u32 m0, s23, 3840\n"
      "ds_read_b64_tr_b16 a[102:103], v13 offset:28672\n"
      "v_add_f32 v10, v10, v80\n"
      "v_mfma_f32_16x16x32_bf16 v[136:139], v[88:91], a[76:79], v[136:139]\n"
      "buffer_load_dwordx4 v1, s[36:39], 0 offen offset:256 nt lds\n"
      "v_mfma_f32_16x16x32_bf16 v[140:143], v[92:95], a[72:75], v[140:143]\n"
      "ds_read_b64_tr_b16 a[64:65], v14 offset:4096\n"
      "v_add_f32 v10, v10, v81\n"
      "v_mfma_f32_16x16x32_bf16 v[140:143], v[96:99], a[76:79], v[140:143]\n"
      "ds_read_b64_tr_b16 a[66:67], v14 offset:12288\n"
      "v_add_f32 v10, v10, v82\n"
      "v_mfma_f32_16x16x32_bf16 v[144:147], v[100:103], a[72:75], v[144:147]\n"
      "ds_read_b64_tr_b16 a[68:69], v14 offset:20480\n"
      "v_add_f32 v10, v10, v83\n"
      "v_mfma_f32_16x16x32_bf16 v[144:147], v[104:107], a[76:79], v[144:147]\n"
      "s_waitcnt lgkmcnt(11)\n"
      "ds_read_b64_tr_b16 a[70:71], v14 offset:28672\n"
      "v_mfma_f32_16x16x32_bf16 v[148:151], v[84:87], a[80:83], v[148:151]\n"
      "ds_read_b64_tr_b16 a[72:73], v15 offset:4096\n"
      "v_mfma_f32_16x16x32_bf16 v[148:151], v[88:91], a[84:87], v[148:151]\n"
      "ds_read_b64_tr_b16 a[74:75], v15 offset:12288\n"
      "v_mfma_f32_16x16x32_bf16 v[152:155], v[92:95], a[80:83], v[152:155]\n"
      "s_add_u32 m0, s23, 4864\n"
      "ds_read_b64_tr_b16 a[76:77], v15 offset:20480\n"
      "v_mfma_f32_16x16x32_bf16 v[152:155], v[96:99], a[84:87], v[152:155]\n"
      "buffer_load_dwordx4 v1, s[36:39], s52 offen offset:256 nt lds\n"
      "v_mfma_f32_16x16x32_bf16 v[156:159], v[100:103], a[80:83], v[156:159]\n"
      "ds_read_b64_tr_b16 a[78:79], v15 offset:28672\n"
      "v_mfma_f32_16x16x32_bf16 v[156:159], v[104:107], a[84:87], v[156:159]\n"
      "s_waitcnt lgkmcnt(12)\n"
      "ds_read_b64_tr_b16 a[80:81], v12 offset:6144\n"
      "v_mfma_f32_16x16x32_bf16 v[160:163], v[84:87], a[88:91], v[160:163]\n"
      "ds_read_b64_tr_b16 a[82:83], v12 offset:14336\n"
      "v_mfma_f32_16x16x32_bf16 v[160:163], v[88:91], a[92:95], v[160:163]\n"
      "ds_read_b64_tr_b16 a[84:85], v12 offset:22528\n"
      "v_mfma_f32_16x16x32_bf16 v[164:167], v[92:95], a[88:91], v[164:167]\n"
      "ds_read_b64_tr_b16 a[86:87], v12 offset:30720\n"
      "v_mfma_f32_16x16x32_bf16 v[164:167], v[96:99], a[92:95], v[164:167]\n"
      "v_mfma_f32_16x16x32_bf16 v[168:171], v[100:103], a[88:91], v[168:171]\n"
      "v_mfma_f32_16x16x32_bf16 v[168:171], v[104:107], a[92:95], v[168:171]\n"
      "s_waitcnt lgkmcnt(12)\n"
      "ds_read_b64_tr_b16 a[88:89], v13 offset:6144\n"
      "v_mfma_f32_16x16x32_bf16 v[172:175], v[84:87], a[96:99], v[172:175]\n"
      "ds_read_b64_tr_b16 a[90:91], v13 offset:14336\n"
      "v_mfma_f32_16x16x32_bf16 v[172:175], v[88:91], a[100:103], v[172:175]\n"
      "ds_read_b64_tr_b16 a[92:93], v13 offset:22528\n"
      "v_mfma_f32_16x16x32_bf16 v[176:179], v[92:95], a[96:99], v[176:179]\n"
      "s_add_u32 m0, s23, 5760\n"
      "ds_read_b64_tr_b16 a[94:95], v13 offset:30720\n"
      "v_mfma_f32_16x16x32_bf16 v[176:179], v[96:99], a[100:103], v[176:179]\n"
      "buffer_load_dwordx4 v1, s[36:39], 0 offen offset:384 nt lds\n"
      "v_mfma_f32_16x16x32_bf16 v[180:183], v[100:103], a[96:99], v[180:183]\n"
      "v_mfma_f32_16x16x32_bf16 v[180:183], v[104:107], a[100:103], v[180:183]\n"
      "s_waitcnt lgkmcnt(12)\n"
      "ds_read_b64_tr_b16 a[96:97], v14 offset:6144\n"
      "v_mfma_f32_16x16x32_bf16 v[184:187], v[84:87], a[64:67], v[184:187]\n"
      "ds_read_b64_tr_b16 a[98:99], v14 offset:14336\n"
      "v_mfma_f32_16x16x32_bf16 v[184:187], v[88:91], a[68:71], v[184:187]\n"
      "ds_read_b64_tr_b16 a[100:101], v14 offset:22528\n"
      "v_mfma_f32_16x16x32_bf16 v[188:191], v[92:95], a[64:67], v[188:191]\n"
      "ds_read_b64_tr_b16 a[102:103], v14 offset:30720\n"
      "v_mfma_f32_16x16x32_bf16 v[188:191], v[96:99], a[68:71], v[188:191]\n"
      "v_mfma_f32_16x16x32_bf16 v[192:195], v[100:103], a[64:67], v[192:195]\n"
      "s_add_u32 m0, s23, 6784\n"
      "v_mfma_f32_16x16x32_bf16 v[192:195], v[104:107], a[68:71], v[192:195]\n"
      "s_waitcnt lgkmcnt(12)\n"
      "buffer_load_dwordx4 v1, s[36:39], s52 offen offset:384 nt lds\n"
      "v_mfma_f32_16x16x32_bf16 v[196:199], v[84:87], a[72:75], v[196:199]\n"
      "ds_read_b64_tr_b16 a[64:65], v15 offset:6144\n"
      "v_add_u32 v1, 0x8000, v1\n"
      "v_mfma_f32_16x16x32_bf16 v[196:199], v[88:91], a[76:79], v[196:199]\n"
      "ds_read_b64_tr_b16 a[66:67], v15 offset:14336\n"
      "v_mfma_f32_16x16x32_bf16 v[200:203], v[92:95], a[72:75], v[200:203]\n"
      "ds_read_b64_tr_b16 a[68:69], v15 offset:22528\n"
      "v_mfma_f32_16x16x32_bf16 v[200:203], v[96:99], a[76:79], v[200:203]\n"
      "buffer_load_dword v23, v22, s[60:63], 0 offen\n"
      "v_mfma_f32_16x16x32_bf16 v[204:207], v[100:103], a[72:75], v[204:207]\n"
      "ds_read_b64_tr_b16 a[70:71], v15 offset:30720\n"
      "v_add_u32 v22, 0x400, v22\n"
      "v_mfma_f32_16x16x32_bf16 v[204:207], v[104:107], a[76:79], v[204:207]\n"
      "s_waitcnt lgkmcnt(12)\n"
      "v_mfma_f32_16x16x32_bf16 v[208:211], v[84:87], a[80:83], v[208:211]\n"
      "v_mfma_f32_16x16x32_bf16 v[208:211], v[88:91], a[84:87], v[208:211]\n"
      "v_mfma_f32_16x16x32_bf16 v[212:215], v[92:95], a[80:83], v[212:215]\n"
      "v_mfma_f32_16x16x32_bf16 v[212:215], v[96:99], a[84:87], v[212:215]\n"
      "L_midN1_%=:\n"
      "v_mfma_f32_16x16x32_bf16 v[216:219], v[100:103], a[80:83], v[216:219]\n"
      "v_mfma_f32_16x16x32_bf16 v[216:219], v[104:107], a[84:87], v[216:219]\n"
      "s_waitcnt lgkmcnt(8)\n"
      "v_mfma_f32_16x16x32_bf16 v[220:223], v[84:87], a[88:91], v[220:223]\n"
      "s_waitcnt vmcnt(17) lgkmcnt(0)\n"
      "v_mfma_f32_16x16x32_bf16 v[220:223], v[88:91], a[92:95], v[220:223]\n"
      "s_barrier\n"
      "ds_read_b128 a[0:3], v3 offset:32768\n"
      "v_mfma_f32_16x16x32_bf16 v[224:227], v[92:95], a[88:91], v[224:227]\n"
      "ds_read_b128 a[4:7], v4 offset:32768\n"
      "v_mfma_f32_16x16x32_bf16 v[224:227], v[96:99], a[92:95], v[224:227]\n"
      "ds_read_b128 a[8:11], v3 offset:34816\n"
      "v_mfma_f32_16x16x32_bf16 v[228:231], v[100:103], a[88:91], v[228:231]\n"
      "ds_read_b128 a[12:15], v4 offset:34816\n"
      "v_mfma_f32_16x16x32_bf16 v[228:231], v[104:107], a[92:95], v[228:231]\n"
      "ds_read_b128 a[16:19], v3 offset:36864\n"
      "v_mfma_f32_16x16x32_bf16 v[232:235], v[84:87], a[96:99], v[232:235]\n"
      "s_add_u32 m0, s23, 65536\n"
      "ds_read_b128 a[20:23], v4 offset:36864\n"
      "v_mfma_f32_16x16x32_bf16 v[232:235], v[88:91], a[100:103], v[232:235]\n"
      "buffer_load_dwordx4 v2, s[40:43], 0 offen offset:0 nt lds\n"
      "v_mfma_f32_16x16x32_bf16 v[236:239], v[92:95], a[96:99], v[236:239]\n"
      "ds_read_b128 a[24:27], v3 offset:38912\n"
      "v_mfma_f32_16x16x32_bf16 v[236:239], v[96:99], a[100:103], v[236:239]\n"
      "ds_read_b128 a[28:31], v4 offset:38912\n"
      "v_mfma_f32_16x16x32_bf16 v[240:243], v[100:103], a[96:99], v[240:243]\n"
      "ds_read_b128 a[32:35], v3 offset:40960\n"
      "v_mfma_f32_16x16x32_bf16 v[240:243], v[104:107], a[100:103], v[240:243]\n"
      "ds_read_b128 a[36:39], v4 offset:40960\n"
      "v_mfma_f32_16x16x32_bf16 v[244:247], v[84:87], a[64:67], v[244:247]\n"
      "ds_read_b128 a[40:43], v3 offset:43008\n"
      "v_mfma_f32_16x16x32_bf16 v[244:247], v[88:91], a[68:71], v[244:247]\n"
      "ds_read_b128 a[44:47], v4 offset:43008\n"
      "v_mfma_f32_16x16x32_bf16 v[248:251], v[92:95], a[64:67], v[248:251]\n"
      "s_add_u32 m0, s23, 66560\n"
      "ds_read_b128 a[48:51], v3 offset:45056\n"
      "v_mfma_f32_16x16x32_bf16 v[248:251], v[96:99], a[68:71], v[248:251]\n"
      "buffer_load_dwordx4 v2, s[40:43], s52 offen offset:0 nt lds\n"
      "v_mfma_f32_16x16x32_bf16 v[252:255], v[100:103], a[64:67], v[252:255]\n"
      "ds_read_b128 a[52:55], v4 offset:45056\n"
      "v_mfma_f32_16x16x32_bf16 v[252:255], v[104:107], a[68:71], v[252:255]\n"
      "s_waitcnt lgkmcnt(6)\n"
      "ds_read_b128 a[56:59], v3 offset:47104\n"
      "v_mfma_f32_16x16x32_bf16 v[36:39], a[0:3], a[104:107], v[24:27]\n"
      "ds_read_b128 a[60:63], v4 offset:47104\n"
      "v_mfma_f32_16x16x32_bf16 v[36:39], a[4:7], a[108:111], v[36:39]\n"
      "v_mfma_f32_16x16x32_bf16 v[36:39], a[8:11], a[112:115], v[36:39]\n"
      "v_mfma_f32_16x16x32_bf16 v[36:39], a[12:15], a[116:119], v[36:39]\n"
      "v_mfma_f32_16x16x32_bf16 v[36:39], a[16:19], a[120:123], v[36:39]\n"
      "v_mfma_f32_16x16x32_bf16 v[36:39], a[20:23], a[124:127], v[36:39]\n"
      "v_mfma_f32_16x16x32_bf16 v[36:39], a[24:27], a[128:131], v[36:39]\n"
      "v_mfma_f32_16x16x32_bf16 v[36:39], a[28:31], a[132:135], v[36:39]\n"
      "v_mfma_f32_16x16x32_bf16 v[40:43], a[0:3], a[136:139], v[28:31]\n"
      "s_add_u32 m0, s23, 67456\n"
      "v_mfma_f32_16x16x32_bf16 v[40:43], a[4:7], a[140:143], v[40:43]\n"
      "buffer_load_dwordx4 v2, s[40:43], 0 offen offset:128 nt lds\n"
      "v_mfma_f32_16x16x32_bf16 v[40:43], a[8:11], a[144:147], v[40:43]\n"
      "v_exp_f32 v36, v36\n"
      "v_mfma_f32_16x16x32_bf16 v[40:43], a[12:15], a[148:151], v[40:43]\n"
      "v_add_f32 v6, v6, v36\n"
      "v_mfma_f32_16x16x32_bf16 v[40:43], a[16:19], a[152:155], v[40:43]\n"
      "v_exp_f32 v37, v37\n"
      "v_mfma_f32_16x16x32_bf16 v[40:43], a[20:23], a[156:159], v[40:43]\n"
      "v_cvt_pk_bf16_f32 v84, v36, v37\n"
      "v_add_f32 v6, v6, v37\n"
      "v_mfma_f32_16x16x32_bf16 v[40:43], a[24:27], a[160:163], v[40:43]\n"
      "v_exp_f32 v38, v38\n"
      "v_mfma_f32_16x16x32_bf16 v[40:43], a[28:31], a[164:167], v[40:43]\n"
      "v_add_f32 v6, v6, v38\n"
      "v_mfma_f32_16x16x32_bf16 v[44:47], a[0:3], a[168:171], v[32:35]\n"
      "s_add_u32 m0, s23, 68480\n"
      "v_exp_f32 v39, v39\n"
      "v_mfma_f32_16x16x32_bf16 v[44:47], a[4:7], a[172:175], v[44:47]\n"
      "buffer_load_dwordx4 v2, s[40:43], s52 offen offset:128 nt lds\n"
      "v_mfma_f32_16x16x32_bf16 v[44:47], a[8:11], a[176:179], v[44:47]\n"
      "v_exp_f32 v40, v40\n"
      "v_mfma_f32_16x16x32_bf16 v[44:47], a[12:15], a[180:183], v[44:47]\n"
      "ds_read_b128 a[0:3], v3 offset:49152\n"
      "v_cvt_pk_bf16_f32 v85, v38, v39\n"
      "v_mfma_f32_16x16x32_bf16 v[44:47], a[16:19], a[184:187], v[44:47]\n"
      "v_exp_f32 v41, v41\n"
      "v_mfma_f32_16x16x32_bf16 v[44:47], a[20:23], a[188:191], v[44:47]\n"
      "ds_read_b128 a[4:7], v4 offset:49152\n"
      "v_cvt_pk_bf16_f32 v92, v40, v41\n"
      "v_mfma_f32_16x16x32_bf16 v[44:47], a[24:27], a[192:195], v[44:47]\n"
      "v_exp_f32 v42, v42\n"
      "v_mfma_f32_16x16x32_bf16 v[44:47], a[28:31], a[196:199], v[44:47]\n"
      "s_waitcnt lgkmcnt(2)\n"
      "ds_read_b128 a[8:11], v3 offset:51200\n"
      "v_add_f32 v6, v6, v39\n"
      "v_mfma_f32_16x16x32_bf16 v[48:51], a[32:35], a[104:107], v[24:27]\n"
      "v_exp_f32 v43, v43\n"
      "v_mfma_f32_16x16x32_bf16 v[48:51], a[36:39], a[108:111], v[48:51]\n"
      "ds_read_b128 a[12:15], v4 offset:51200\n"
      "v_cvt_pk_bf16_f32 v93, v42, v43\n"
      "v_mfma_f32_16x16x32_bf16 v[48:51], a[40:43], a[112:115], v[48:51]\n"
      "v_exp_f32 v44, v44\n"
      "v_mfma_f32_16x16x32_bf16 v[48:51], a[44:47], a[116:119], v[48:51]\n"
      "ds_read_b128 a[16:19], v3 offset:53248\n"
      "v_add_f32 v8, v8, v40\n"
      "v_mfma_f32_16x16x32_bf16 v[48:51], a[48:51], a[120:123], v[48:51]\n"
      "s_add_u32 m0, s23, 69376\n"
      "v_exp_f32 v45, v45\n"
      "v_mfma_f32_16x16x32_bf16 v[48:51], a[52:55], a[124:127], v[48:51]\n"
      "buffer_load_dwordx4 v2, s[40:43], 0 offen offset:256 nt lds\n"
      "v_mfma_f32_16x16x32_bf16 v[48:51], a[56:59], a[128:131], v[48:51]\n"
      "v_exp_f32 v46, v46\n"
      "v_mfma_f32_16x16x32_bf16 v[48:51], a[60:63], a[132:135], v[48:51]\n"
      "ds_read_b128 a[20:23], v4 offset:53248\n"
      "v_cvt_pk_bf16_f32 v100, v44, v45\n"
      "v_mfma_f32_16x16x32_bf16 v[52:55], a[32:35], a[136:139], v[28:31]\n"
      "v_exp_f32 v47, v47\n"
      "v_mfma_f32_16x16x32_bf16 v[52:55], a[36:39], a[140:143], v[52:55]\n"
      "ds_read_b128 a[24:27], v3 offset:55296\n"
      "v_cvt_pk_bf16_f32 v101, v46, v47\n"
      "v_mfma_f32_16x16x32_bf16 v[52:55], a[40:43], a[144:147], v[52:55]\n"
      "v_exp_f32 v48, v48\n"
      "v_mfma_f32_16x16x32_bf16 v[52:55], a[44:47], a[148:151], v[52:55]\n"
      "ds_read_b128 a[28:31], v4 offset:55296\n"
      "v_add_f32 v8, v8, v41\n"
      "v_mfma_f32_16x16x32_bf16 v[52:55], a[48:51], a[152:155], v[52:55]\n"
      "s_add_u32 m0, s23, 70400\n"
      "v_exp_f32 v49, v49\n"
      "v_mfma_f32_16x16x32_bf16 v[52:55], a[52:55], a[156:159], v[52:55]\n"
      "buffer_load_dwordx4 v2, s[40:43], s52 offen offset:256 nt lds\n"
      "v_mfma_f32_16x16x32_bf16 v[52:55], a[56:59], a[160:163], v[52:55]\n"
      "v_exp_f32 v50, v50\n"
      "v_mfma_f32_16x16x32_bf16 v[52:55], a[60:63], a[164:167], v[52:55]\n"
      "v_cvt_pk_bf16_f32 v86, v48, v49\n"
      "v_add_f32 v8, v8, v42\n"
      "v_mfma_f32_16x16x32_bf16 v[56:59], a[32:35], a[168:171], v[32:35]\n"
      "v_exp_f32 v51, v51\n"
      "v_mfma_f32_16x16x32_bf16 v[56:59], a[36:39], a[172:175], v[56:59]\n"
      "ds_read_b128 a[32:35], v3 offset:57344\n"
      "v_cvt_pk_bf16_f32 v87, v50, v51\n"
      "v_mfma_f32_16x16x32_bf16 v[56:59], a[40:43], a[176:179], v[56:59]\n"
      "v_exp_f32 v52, v52\n"
      "v_mfma_f32_16x16x32_bf16 v[56:59], a[44:47], a[180:183], v[56:59]\n"
      "ds_read_b128 a[36:39], v4 offset:57344\n"
      "v_add_f32 v8, v8, v43\n"
      "v_mfma_f32_16x16x32_bf16 v[56:59], a[48:51], a[184:187], v[56:59]\n"
      "v_exp_f32 v53, v53\n"
      "v_mfma_f32_16x16x32_bf16 v[56:59], a[52:55], a[188:191], v[56:59]\n"
      "ds_read_b128 a[40:43], v3 offset:59392\n"
      "v_cvt_pk_bf16_f32 v94, v52, v53\n"
      "v_mfma_f32_16x16x32_bf16 v[56:59], a[56:59], a[192:195], v[56:59]\n"
      "v_exp_f32 v54, v54\n"
      "v_mfma_f32_16x16x32_bf16 v[56:59], a[60:63], a[196:199], v[56:59]\n"
      "s_waitcnt lgkmcnt(3)\n"
      "ds_read_b128 a[44:47], v4 offset:59392\n"
      "v_add_f32 v10, v10, v44\n"
      "v_mfma_f32_16x16x32_bf16 v[60:63], a[0:3], a[104:107], v[24:27]\n"
      "s_add_u32 m0, s23, 71296\n"
      "v_exp_f32 v55, v55\n"
      "v_mfma_f32_16x16x32_bf16 v[60:63], a[4:7], a[108:111], v[60:63]\n"
      "buffer_load_dwordx4 v2, s[40:43], 0 offen offset:384 nt lds\n"
      "v_mfma_f32_16x16x32_bf16 v[60:63], a[8:11], a[112:115], v[60:63]\n"
      "v_exp_f32 v56, v56\n"
      "v_mfma_f32_16x16x32_bf16 v[60:63], a[12:15], a[116:119], v[60:63]\n"
      "ds_read_b128 a[48:51], v3 offset:61440\n"
      "v_cvt_pk_bf16_f32 v95, v54, v55\n"
      "v_mfma_f32_16x16x32_bf16 v[60:63], a[16:19], a[120:123], v[60:63]\n"
      "v_exp_f32 v57, v57\n"
      "v_mfma_f32_16x16x32_bf16 v[60:63], a[20:23], a[124:127], v[60:63]\n"
      "ds_read_b128 a[52:55], v4 offset:61440\n"
      "v_cvt_pk_bf16_f32 v102, v56, v57\n"
      "v_mfma_f32_16x16x32_bf16 v[60:63], a[24:27], a[128:131], v[60:63]\n"
      "v_exp_f32 v58, v58\n"
      "v_mfma_f32_16x16x32_bf16 v[60:63], a[28:31], a[132:135], v[60:63]\n"
      "ds_read_b128 a[56:59], v3 offset:63488\n"
      "v_add_f32 v10, v10, v45\n"
      "v_mfma_f32_16x16x32_bf16 v[64:67], a[0:3], a[136:139], v[28:31]\n"
      "s_add_u32 m0, s23, 72320\n"
      "v_exp_f32 v59, v59\n"
      "v_mfma_f32_16x16x32_bf16 v[64:67], a[4:7], a[140:143], v[64:67]\n"
      "buffer_load_dwordx4 v2, s[40:43], s52 offen offset:384 nt lds\n"
      "v_mfma_f32_16x16x32_bf16 v[64:67], a[8:11], a[144:147], v[64:67]\n"
      "v_exp_f32 v60, v60\n"
      "v_mfma_f32_16x16x32_bf16 v[64:67], a[12:15], a[148:151], v[64:67]\n"
      "ds_read_b128 a[60:63], v4 offset:63488\n"
      "v_cvt_pk_bf16_f32 v103, v58, v59\n"
      "v_mfma_f32_16x16x32_bf16 v[64:67], a[16:19], a[152:155], v[64:67]\n"
      "v_exp_f32 v61, v61\n"
      "v_mfma_f32_16x16x32_bf16 v[64:67], a[20:23], a[156:159], v[64:67]\n"
      "v_cvt_pk_bf16_f32 v88, v60, v61\n"
      "v_add_u32 v2, 0x8000, v2\n"
      "v_mfma_f32_16x16x32_bf16 v[64:67], a[24:27], a[160:163], v[64:67]\n"
      "v_exp_f32 v62, v62\n"
      "v_mfma_f32_16x16x32_bf16 v[64:67], a[28:31], a[164:167], v[64:67]\n"
      "v_add_f32 v10, v10, v46\n"
      "v_add_f32 v10, v10, v47\n"
      "v_mfma_f32_16x16x32_bf16 v[68:71], a[0:3], a[168:171], v[32:35]\n"
      "v_exp_f32 v63, v63\n"
      "v_mfma_f32_16x16x32_bf16 v[68:71], a[4:7], a[172:175], v[68:71]\n"
      "v_cvt_pk_bf16_f32 v89, v62, v63\n"
      "v_add_f32 v6, v6, v48\n"
      "v_mfma_f32_16x16x32_bf16 v[68:71], a[8:11], a[176:179], v[68:71]\n"
      "s_waitcnt vmcnt(17) lgkmcnt(0)\n"
      "v_exp_f32 v64, v64\n"
      "v_mfma_f32_16x16x32_bf16 v[68:71], a[12:15], a[180:183], v[68:71]\n"
      "s_barrier\n"
      "ds_read_b64_tr_b16 a[64:65], v12 offset:32768\n"
      "v_add_f32 v6, v6, v49\n"
      "v_mfma_f32_16x16x32_bf16 v[68:71], a[16:19], a[184:187], v[68:71]\n"
      "v_exp_f32 v65, v65\n"
      "v_mfma_f32_16x16x32_bf16 v[68:71], a[20:23], a[188:191], v[68:71]\n"
      "ds_read_b64_tr_b16 a[66:67], v12 offset:40960\n"
      "v_cvt_pk_bf16_f32 v96, v64, v65\n"
      "v_mfma_f32_16x16x32_bf16 v[68:71], a[24:27], a[192:195], v[68:71]\n"
      "v_exp_f32 v66, v66\n"
      "v_mfma_f32_16x16x32_bf16 v[68:71], a[28:31], a[196:199], v[68:71]\n"
      "ds_read_b64_tr_b16 a[68:69], v12 offset:49152\n"
      "v_add_f32 v6, v6, v50\n"
      "v_mfma_f32_16x16x32_bf16 v[72:75], a[32:35], a[104:107], v[24:27]\n"
      "v_exp_f32 v67, v67\n"
      "v_mfma_f32_16x16x32_bf16 v[72:75], a[36:39], a[108:111], v[72:75]\n"
      "ds_read_b64_tr_b16 a[70:71], v12 offset:57344\n"
      "v_cvt_pk_bf16_f32 v97, v66, v67\n"
      "v_mfma_f32_16x16x32_bf16 v[72:75], a[40:43], a[112:115], v[72:75]\n"
      "v_exp_f32 v68, v68\n"
      "v_mfma_f32_16x16x32_bf16 v[72:75], a[44:47], a[116:119], v[72:75]\n"
      "ds_read_b64_tr_b16 a[72:73], v13 offset:32768\n"
      "v_add_f32 v6, v6, v51\n"
      "v_mfma_f32_16x16x32_bf16 v[72:75], a[48:51], a[120:123], v[72:75]\n"
      "v_exp_f32 v69, v69\n"
      "v_mfma_f32_16x16x32_bf16 v[72:75], a[52:55], a[124:127], v[72:75]\n"
      "ds_read_b64_tr_b16 a[74:75], v13 offset:40960\n"
      "v_cvt_pk_bf16_f32 v104, v68, v69\n"
      "v_mfma_f32_16x16x32_bf16 v[72:75], a[56:59], a[128:131], v[72:75]\n"
      "v_exp_f32 v70, v70\n"
      "v_mfma_f32_16x16x32_bf16 v[72:75], a[60:63], a[132:135], v[72:75]\n"
      "ds_read_b64_tr_b16 a[76:77], v13 offset:49152\n"
      "v_add_f32 v8, v8, v52\n"
      "v_mfma_f32_16x16x32_bf16 v[76:79], a[32:35], a[136:139], v[28:31]\n"
      "v_exp_f32 v71, v71\n"
      "v_mfma_f32_16x16x32_bf16 v[76:79], a[36:39], a[140:143], v[76:79]\n"
      "ds_read_b64_tr_b16 a[78:79], v13 offset:57344\n"
      "v_cvt_pk_bf16_f32 v105, v70, v71\n"
      "v_mfma_f32_16x16x32_bf16 v[76:79], a[40:43], a[144:147], v[76:79]\n"
      "v_exp_f32 v72, v72\n"
      "v_mfma_f32_16x16x32_bf16 v[76:79], a[44:47], a[148:151], v[76:79]\n"
      "ds_read_b64_tr_b16 a[80:81], v14 offset:32768\n"
      "v_add_f32 v8, v8, v53\n"
      "v_mfma_f32_16x16x32_bf16 v[76:79], a[48:51], a[152:155], v[76:79]\n"
      "v_exp_f32 v73, v73\n"
      "v_mfma_f32_16x16x32_bf16 v[76:79], a[52:55], a[156:159], v[76:79]\n"
      "ds_read_b64_tr_b16 a[82:83], v14 offset:40960\n"
      "v_cvt_pk_bf16_f32 v90, v72, v73\n"
      "v_mfma_f32_16x16x32_bf16 v[76:79], a[56:59], a[160:163], v[76:79]\n"
      "v_exp_f32 v74, v74\n"
      "v_mfma_f32_16x16x32_bf16 v[76:79], a[60:63], a[164:167], v[76:79]\n"
      "ds_read_b64_tr_b16 a[84:85], v14 offset:49152\n"
      "v_add_f32 v8, v8, v54\n"
      "v_mfma_f32_16x16x32_bf16 v[80:83], a[32:35], a[168:171], v[32:35]\n"
      "v_exp_f32 v75, v75\n"
      "v_mfma_f32_16x16x32_bf16 v[80:83], a[36:39], a[172:175], v[80:83]\n"
      "ds_read_b64_tr_b16 a[86:87], v14 offset:57344\n"
      "v_cvt_pk_bf16_f32 v91, v74, v75\n"
      "v_mfma_f32_16x16x32_bf16 v[80:83], a[40:43], a[176:179], v[80:83]\n"
      "v_exp_f32 v76, v76\n"
      "v_mfma_f32_16x16x32_bf16 v[80:83], a[44:47], a[180:183], v[80:83]\n"
      "v_add_f32 v8, v8, v55\n"
      "v_mfma_f32_16x16x32_bf16 v[80:83], a[48:51], a[184:187], v[80:83]\n"
      "v_exp_f32 v77, v77\n"
      "v_mfma_f32_16x16x32_bf16 v[80:83], a[52:55], a[188:191], v[80:83]\n"
      "v_cvt_pk_bf16_f32 v98, v76, v77\n"
      "v_mfma_f32_16x16x32_bf16 v[80:83], a[56:59], a[192:195], v[80:83]\n"
      "s_sub_u32 s20, s20, 1\n"
      "v_exp_f32 v78, v78\n"
      "v_mfma_f32_16x16x32_bf16 v[80:83], a[60:63], a[196:199], v[80:83]\n"
      "s_waitcnt lgkmcnt(8)\n"
      "s_cbranch_scc1 L_tail_%=\n"
      "v_mfma_f32_16x16x32_bf16 a[200:203], v[84:87], a[64:67], a[200:203]\n"
      "s_add_u32 m0, s23, 32768\n"
      "v_exp_f32 v79, v79\n"
      "v_mfma_f32_16x16x32_bf16 a[200:203], v[88:91], a[68:71], a[200:203]\n"
      "s_waitcnt lgkmcnt(4)\n"
      "buffer_load_dwordx4 v1, s[36:39], 0 offen offset:0 nt lds\n"
      "v_mfma_f32_16x16x32_bf16 a[212:215], v[84:87], a[72:75], a[212:215]\n"
      "v_exp_f32 v80, v80\n"
      "v_mfma_f32_16x16x32_bf16 a[212:215], v[88:91], a[76:79], a[212:215]\n"
      "s_waitcnt lgkmcnt(0)\n"
      "ds_read_b64_tr_b16 a[88:89], v15 offset:32768\n"
      "v_cvt_pk_bf16_f32 v99, v78, v79\n"
      "v_mfma_f32_16x16x32_bf16 a[224:227], v[84:87], a[80:83], a[224:227]\n"
      "v_exp_f32 v81, v81\n"
      "v_mfma_f32_16x16x32_bf16 a[224:227], v[88:91], a[84:87], a[224:227]\n"
      "ds_read_b64_tr_b16 a[90:91], v15 offset:40960\n"
      "v_cvt_pk_bf16_f32 v106, v80, v81\n"
      "v_mfma_f32_16x16x32_bf16 a[204:207], v[92:95], a[64:67], a[204:207]\n"
      "v_exp_f32 v82, v82\n"
      "v_mfma_f32_16x16x32_bf16 a[204:207], v[96:99], a[68:71], a[204:207]\n"
      "ds_read_b64_tr_b16 a[92:93], v15 offset:49152\n"
      "v_add_f32 v10, v10, v56\n"
      "v_mfma_f32_16x16x32_bf16 a[216:219], v[92:95], a[72:75], a[216:219]\n"
      "s_add_u32 m0, s23, 33792\n"
      "v_exp_f32 v83, v83\n"
      "v_mfma_f32_16x16x32_bf16 a[216:219], v[96:99], a[76:79], a[216:219]\n"
      "buffer_load_dwordx4 v1, s[36:39], s52 offen offset:0 nt lds\n"
      "v_mfma_f32_16x16x32_bf16 a[228:231], v[92:95], a[80:83], a[228:231]\n"
      "ds_read_b64_tr_b16 a[94:95], v15 offset:57344\n"
      "v_cvt_pk_bf16_f32 v107, v82, v83\n"
      "v_mfma_f32_16x16x32_bf16 a[228:231], v[96:99], a[84:87], a[228:231]\n"
      "ds_read_b64_tr_b16 a[96:97], v12 offset:34816\n"
      "v_add_f32 v10, v10, v57\n"
      "v_mfma_f32_16x16x32_bf16 a[208:211], v[100:103], a[64:67], a[208:211]\n"
      "ds_read_b64_tr_b16 a[98:99], v12 offset:43008\n"
      "v_add_f32 v10, v10, v58\n"
      "v_mfma_f32_16x16x32_bf16 a[208:211], v[104:107], a[68:71], a[208:211]\n"
      "ds_read_b64_tr_b16 a[100:101], v12 offset:51200\n"
      "v_add_f32 v10, v10, v59\n"
      "v_mfma_f32_16x16x32_bf16 a[220:223], v[100:103], a[72:75], a[220:223]\n"
      "ds_read_b64_tr_b16 a[102:103], v12 offset:59392\n"
      "v_add_f32 v6, v6, v60\n"
      "v_mfma_f32_16x16x32_bf16 a[220:223], v[104:107], a[76:79], a[220:223]\n"
      "ds_read_b64_tr_b16 a[64:65], v13 offset:34816\n"
      "v_add_f32 v6, v6, v61\n"
      "v_mfma_f32_16x16x32_bf16 a[232:235], v[100:103], a[80:83], a[232:235]\n"
      "ds_read_b64_tr_b16 a[66:67], v13 offset:43008\n"
      "v_add_f32 v6, v6, v62\n"
      "v_mfma_f32_16x16x32_bf16 a[232:235], v[104:107], a[84:87], a[232:235]\n"
      "s_waitcnt lgkmcnt(6)\n"
      "ds_read_b64_tr_b16 a[68:69], v13 offset:51200\n"
      "v_add_f32 v6, v6, v63\n"
      "v_mfma_f32_16x16x32_bf16 a[236:239], v[84:87], a[88:91], a[236:239]\n"
      "ds_read_b64_tr_b16 a[70:71], v13 offset:59392\n"
      "v_add_f32 v8, v8, v64\n"
      "v_mfma_f32_16x16x32_bf16 a[236:239], v[88:91], a[92:95], a[236:239]\n"
      "ds_read_b64_tr_b16 a[72:73], v14 offset:34816\n"
      "v_add_f32 v8, v8, v65\n"
      "v_mfma_f32_16x16x32_bf16 a[240:243], v[92:95], a[88:91], a[240:243]\n"
      "s_add_u32 m0, s23, 34688\n"
      "ds_read_b64_tr_b16 a[74:75], v14 offset:43008\n"
      "v_add_f32 v8, v8, v66\n"
      "v_mfma_f32_16x16x32_bf16 a[240:243], v[96:99], a[92:95], a[240:243]\n"
      "buffer_load_dwordx4 v1, s[36:39], 0 offen offset:128 nt lds\n"
      "v_mfma_f32_16x16x32_bf16 v[108:111], v[100:103], a[88:91], v[108:111]\n"
      "ds_read_b64_tr_b16 a[76:77], v14 offset:51200\n"
      "v_add_f32 v8, v8, v67\n"
      "v_mfma_f32_16x16x32_bf16 v[108:111], v[104:107], a[92:95], v[108:111]\n"
      "s_waitcnt lgkmcnt(7)\n"
      "ds_read_b64_tr_b16 a[78:79], v14 offset:59392\n"
      "v_add_f32 v10, v10, v68\n"
      "v_mfma_f32_16x16x32_bf16 v[112:115], v[84:87], a[96:99], v[112:115]\n"
      "ds_read_b64_tr_b16 a[80:81], v15 offset:34816\n"
      "v_add_f32 v10, v10, v69\n"
      "v_mfma_f32_16x16x32_bf16 v[112:115], v[88:91], a[100:103], v[112:115]\n"
      "ds_read_b64_tr_b16 a[82:83], v15 offset:43008\n"
      "v_add_f32 v10, v10, v70\n"
      "v_mfma_f32_16x16x32_bf16 v[116:119], v[92:95], a[96:99], v[116:119]\n"
      "ds_read_b64_tr_b16 a[84:85], v15 offset:51200\n"
      "v_add_f32 v10, v10, v71\n"
      "v_mfma_f32_16x16x32_bf16 v[116:119], v[96:99], a[100:103], v[116:119]\n"
      "ds_read_b64_tr_b16 a[86:87], v15 offset:59392\n"
      "v_add_f32 v6, v6, v72\n"
      "v_mfma_f32_16x16x32_bf16 v[120:123], v[100:103], a[96:99], v[120:123]\n"
      "s_add_u32 m0, s23, 35712\n"
      "ds_read_b64_tr_b16 a[88:89], v12 offset:36864\n"
      "v_add_f32 v6, v6, v73\n"
      "v_mfma_f32_16x16x32_bf16 v[120:123], v[104:107], a[100:103], v[120:123]\n"
      "s_waitcnt lgkmcnt(9)\n"
      "buffer_load_dwordx4 v1, s[36:39], s52 offen offset:128 nt lds\n"
      "v_mfma_f32_16x16x32_bf16 v[124:127], v[84:87], a[64:67], v[124:127]\n"
      "ds_read_b64_tr_b16 a[90:91], v12 offset:45056\n"
      "v_add_f32 v6, v6, v74\n"
      "v_mfma_f32_16x16x32_bf16 v[124:127], v[88:91], a[68:71], v[124:127]\n"
      "ds_read_b64_tr_b16 a[92:93], v12 offset:53248\n"
      "v_add_f32 v6, v6, v75\n"
      "v_mfma_f32_16x16x32_bf16 v[128:131], v[92:95], a[64:67], v[128:131]\n"
      "ds_read_b64_tr_b16 a[94:95], v12 offset:61440\n"
      "v_add_f32 v8, v8, v76\n"
      "v_mfma_f32_16x16x32_bf16 v[128:131], v[96:99], a[68:71], v[128:131]\n"
      "ds_read_b64_tr_b16 a[96:97], v13 offset:36864\n"
      "v_add_f32 v8, v8, v77\n"
      "v_mfma_f32_16x16x32_bf16 v[132:135], v[100:103], a[64:67], v[132:135]\n"
      "ds_read_b64_tr_b16 a[98:99], v13 offset:45056\n"
      "v_add_f32 v8, v8, v78\n"
      "v_mfma_f32_16x16x32_bf16 v[132:135], v[104:107], a[68:71], v[132:135]\n"
      "s_waitcnt lgkmcnt(10)\n"
      "ds_read_b64_tr_b16 a[100:101], v13 offset:53248\n"
      "v_add_f32 v8, v8, v79\n"
      "v_mfma_f32_16x16x32_bf16 v[136:139], v[84:87], a[72:75], v[136:139]\n"
      "s_add_u32 m0, s23, 36608\n"
      "ds_read_b64_tr_b16 a[102:103], v13 offset:61440\n"
      "v_add_f32 v10, v10, v80\n"
      "v_mfma_f32_16x16x32_bf16 v[136:139], v[88:91], a[76:79], v[136:139]\n"
      "buffer_load_dwordx4 v1, s[36:39], 0 offen offset:256 nt lds\n"
      "v_mfma_f32_16x16x32_bf16 v[140:143], v[92:95], a[72:75], v[140:143]\n"
      "ds_read_b64_tr_b16 a[64:65], v14 offset:36864\n"
      "v_add_f32 v10, v10, v81\n"
      "v_mfma_f32_16x16x32_bf16 v[140:143], v[96:99], a[76:79], v[140:143]\n"
      "ds_read_b64_tr_b16 a[66:67], v14 offset:45056\n"
      "v_add_f32 v10, v10, v82\n"
      "v_mfma_f32_16x16x32_bf16 v[144:147], v[100:103], a[72:75], v[144:147]\n"
      "ds_read_b64_tr_b16 a[68:69], v14 offset:53248\n"
      "v_add_f32 v10, v10, v83\n"
      "v_mfma_f32_16x16x32_bf16 v[144:147], v[104:107], a[76:79], v[144:147]\n"
      "s_waitcnt lgkmcnt(11)\n"
      "ds_read_b64_tr_b16 a[70:71], v14 offset:61440\n"
      "v_mfma_f32_16x16x32_bf16 v[148:151], v[84:87], a[80:83], v[148:151]\n"
      "ds_read_b64_tr_b16 a[72:73], v15 offset:36864\n"
      "v_mfma_f32_16x16x32_bf16 v[148:151], v[88:91], a[84:87], v[148:151]\n"
      "ds_read_b64_tr_b16 a[74:75], v15 offset:45056\n"
      "v_mfma_f32_16x16x32_bf16 v[152:155], v[92:95], a[80:83], v[152:155]\n"
      "s_add_u32 m0, s23, 37632\n"
      "ds_read_b64_tr_b16 a[76:77], v15 offset:53248\n"
      "v_mfma_f32_16x16x32_bf16 v[152:155], v[96:99], a[84:87], v[152:155]\n"
      "buffer_load_dwordx4 v1, s[36:39], s52 offen offset:256 nt lds\n"
      "v_mfma_f32_16x16x32_bf16 v[156:159], v[100:103], a[80:83], v[156:159]\n"
      "ds_read_b64_tr_b16 a[78:79], v15 offset:61440\n"
      "v_mfma_f32_16x16x32_bf16 v[156:159], v[104:107], a[84:87], v[156:159]\n"
      "s_waitcnt lgkmcnt(12)\n"
      "ds_read_b64_tr_b16 a[80:81], v12 offset:38912\n"
      "v_mfma_f32_16x16x32_bf16 v[160:163], v[84:87], a[88:91], v[160:163]\n"
      "ds_read_b64_tr_b16 a[82:83], v12 offset:47104\n"
      "v_mfma_f32_16x16x32_bf16 v[160:163], v[88:91], a[92:95], v[160:163]\n"
      "ds_read_b64_tr_b16 a[84:85], v12 offset:55296\n"
      "v_mfma_f32_16x16x32_bf16 v[164:167], v[92:95], a[88:91], v[164:167]\n"
      "ds_read_b64_tr_b16 a[86:87], v12 offset:63488\n"
      "v_mfma_f32_16x16x32_bf16 v[164:167], v[96:99], a[92:95], v[164:167]\n"
      "v_mfma_f32_16x16x32_bf16 v[168:171], v[100:103], a[88:91], v[168:171]\n"
      "v_mfma_f32_16x16x32_bf16 v[168:171], v[104:107], a[92:95], v[168:171]\n"
      "s_waitcnt lgkmcnt(12)\n"
      "ds_read_b64_tr_b16 a[88:89], v13 offset:38912\n"
      "v_mfma_f32_16x16x32_bf16 v[172:175], v[84:87], a[96:99], v[172:175]\n"
      "ds_read_b64_tr_b16 a[90:91], v13 offset:47104\n"
      "v_mfma_f32_16x16x32_bf16 v[172:175], v[88:91], a[100:103], v[172:175]\n"
      "ds_read_b64_tr_b16 a[92:93], v13 offset:55296\n"
      "v_mfma_f32_16x16x32_bf16 v[176:179], v[92:95], a[96:99], v[176:179]\n"
      "s_add_u32 m0, s23, 38528\n"
      "ds_read_b64_tr_b16 a[94:95], v13 offset:63488\n"
      "v_mfma_f32_16x16x32_bf16 v[176:179], v[96:99], a[100:103], v[176:179]\n"
      "buffer_load_dwordx4 v1, s[36:39], 0 offen offset:384 nt lds\n"
      "v_mfma_f32_16x16x32_bf16 v[180:183], v[100:103], a[96:99], v[180:183]\n"
      "v_mfma_f32_16x16x32_bf16 v[180:183], v[104:107], a[100:103], v[180:183]\n"
      "s_waitcnt lgkmcnt(12)\n"
      "ds_read_b64_tr_b16 a[96:97], v14 offset:38912\n"
      "v_mfma_f32_16x16x32_bf16 v[184:187], v[84:87], a[64:67], v[184:187]\n"
      "ds_read_b64_tr_b16 a[98:99], v14 offset:47104\n"
      "v_mfma_f32_16x16x32_bf16 v[184:187], v[88:91], a[68:71], v[184:187]\n"
      "ds_read_b64_tr_b16 a[100:101], v14 offset:55296\n"
      "v_mfma_f32_16x16x32_bf16 v[188:191], v[92:95], a[64:67], v[188:191]\n"
      "ds_read_b64_tr_b16 a[102:103], v14 offset:63488\n"
      "v_mfma_f32_16x16x32_bf16 v[188:191], v[96:99], a[68:71], v[188:191]\n"
      "v_mfma_f32_16x16x32_bf16 v[192:195], v[100:103], a[64:67], v[192:195]\n"
      "s_add_u32 m0, s23, 39552\n"
      "v_mfma_f32_16x16x32_bf16 v[192:195], v[104:107], a[68:71], v[192:195]\n"
      "s_waitcnt lgkmcnt(12)\n"
      "buffer_load_dwordx4 v1, s[36:39], s52 offen offset:384 nt lds\n"
      "v_mfma_f32_16x16x32_bf16 v[196:199], v[84:87], a[72:75], v[196:199]\n"
      "ds_read_b64_tr_b16 a[64:65], v15 offset:38912\n"
      "v_add_u32 v1, 0x8000, v1\n"
      "v_mfma_f32_16x16x32_bf16 v[196:199], v[88:91], a[76:79], v[196:199]\n"
      "ds_read_b64_tr_b16 a[66:67], v15 offset:47104\n"
      "v_mfma_f32_16x16x32_bf16 v[200:203], v[92:95], a[72:75], v[200:203]\n"
      "ds_read_b64_tr_b16 a[68:69], v15 offset:55296\n"
      "v_mfma_f32_16x16x32_bf16 v[200:203], v[96:99], a[76:79], v[200:203]\n"
      "buffer_load_dword v23, v22, s[60:63], 0 offen\n"
      "v_mfma_f32_16x16x32_bf16 v[204:207], v[100:103], a[72:75], v[204:207]\n"
      "ds_read_b64_tr_b16 a[70:71], v15 offset:63488\n"
      "v_add_u32 v22, 0x400, v22\n"
      "v_mfma_f32_16x16x32_bf16 v[204:207], v[104:107], a[76:79], v[204:207]\n"
      "s_waitcnt lgkmcnt(12)\n"
      "v_mfma_f32_16x16x32_bf16 v[208:211], v[84:87], a[80:83], v[208:211]\n"
      "v_mfma_f32_16x16x32_bf16 v[208:211], v[88:91], a[84:87], v[208:211]\n"
      "v_mfma_f32_16x16x32_bf16 v[212:215], v[92:95], a[80:83], v[212:215]\n"
      "s_cmp_eq_u32 s20, 14\n"
      "v_mfma_f32_16x16x32_bf16 v[212:215], v[96:99], a[84:87], v[212:215]\n"
      "s_cbranch_scc1 L_topD1_%=\n"
      "s_branch L_topN1_%=\n"
      ".p2align 6\n"
      "L_topD2_%=:\n"
      "v_mfma_f32_16x16x32_bf16 v[216:219], v[100:103], a[80:83], v[216:219]\n"
      "v_mfma_f32_16x16x32_bf16 v[216:219], v[104:107], a[84:87], v[216:219]\n"
      "s_waitcnt lgkmcnt(8)\n"
      "v_mfma_f32_16x16x32_bf16 v[220:223], v[84:87], a[88:91], v[220:223]\n"
      "s_waitcnt vmcnt(17) lgkmcnt(0)\n"
      "v_mfma_f32_16x16x32_bf16 v[220:223], v[88:91], a[92:95], v[220:223]\n"
      "s_barrier\n"
      "ds_read_b128 a[0:3], v3 offset:0\n"
      "v_mfma_f32_16x16x32_bf16 v[224:227], v[92:95], a[88:91], v[224:227]\n"
      "ds_read_b128 a[4:7], v4 offset:0\n"
      "v_mfma_f32_16x16x32_bf16 v[224:227], v[96:99], a[92:95], v[224:227]\n"
      "s_add_u32 m0, s23, 98304\n"
      "ds_read_b128 a[8:11], v3 offset:2048\n"
      "v_mfma_f32_16x16x32_bf16 v[228:231], v[100:103], a[88:91], v[228:231]\n"
      "buffer_load_dwordx4 v2, s[40:43], 0 offen offset:0 lds\n"
      "v_mfma_f32_16x16x32_bf16 v[228:231], v[104:107], a[92:95], v[228:231]\n"
      "ds_read_b128 a[12:15], v4 offset:2048\n"
      "v_mfma_f32_16x16x32_bf16 v[232:235], v[84:87], a[96:99], v[232:235]\n"
      "ds_read_b128 a[16:19], v3 offset:4096\n"
      "v_mfma_f32_16x16x32_bf16 v[232:235], v[88:91], a[100:103], v[232:235]\n"
      "ds_read_b128 a[20:23], v4 offset:4096\n"
      "v_mfma_f32_16x16x32_bf16 v[236:239], v[92:95], a[96:99], v[236:239]\n"
      "ds_read_b128 a[24:27], v3 offset:6144\n"
      "v_mfma_f32_16x16x32_bf16 v[236:239], v[96:99], a[100:103], v[236:239]\n"
      "ds_read_b128 a[28:31], v4 offset:6144\n"
      "v_mfma_f32_16x16x32_bf16 v[240:243], v[100:103], a[96:99], v[240:243]\n"
      "ds_read_b128 a[32:35], v3 offset:8192\n"
      "v_mfma_f32_16x16x32_bf16 v[240:243], v[104:107], a[100:103], v[240:243]\n"
      "ds_read_b128 a[36:39], v4 offset:8192\n"
      "v_mfma_f32_16x16x32_bf16 v[244:247], v[84:87], a[64:67], v[244:247]\n"
      "ds_read_b128 a[40:43], v3 offset:10240\n"
      "v_mfma_f32_16x16x32_bf16 v[244:247], v[88:91], a[68:71], v[244:247]\n"
      "ds_read_b128 a[44:47], v4 offset:10240\n"
      "v_mfma_f32_16x16x32_bf16 v[248:251], v[92:95], a[64:67], v[248:251]\n"
      "ds_read_b128 a[48:51], v3 offset:12288\n"
      "v_mfma_f32_16x16x32_bf16 v[248:251], v[96:99], a[68:71], v[248:251]\n"
      "s_add_u32 m0, s23, 99328\n"
      "ds_read_b128 a[52:55], v4 offset:12288\n"
      "v_mfma_f32_16x16x32_bf16 v[252:255], v[100:103], a[64:67], v[252:255]\n"
      "buffer_load_dwordx4 v2, s[40:43], s52 offen offset:0 lds\n"
      "v_mfma_f32_16x16x32_bf16 v[252:255], v[104:107], a[68:71], v[252:255]\n"
      "s_waitcnt lgkmcnt(6)\n"
      "ds_read_b128 a[56:59], v3 offset:14336\n"
      "v_mfma_f32_16x16x32_bf16 v[36:39], a[0:3], a[104:107], v[24:27]\n"
      "ds_read_b128 a[60:63], v4 offset:14336\n"
      "v_mfma_f32_16x16x32_bf16 v[36:39], a[4:7], a[108:111], v[36:39]\n"
      "v_mfma_f32_16x16x32_bf16 v[36:39], a[8:11], a[112:115], v[36:39]\n"
      "v_mfma_f32_16x16x32_bf16 v[36:39], a[12:15], a[116:119], v[36:39]\n"
      "v_mfma_f32_16x16x32_bf16 v[36:39], a[16:19], a[120:123], v[36:39]\n"
      "v_mfma_f32_16x16x32_bf16 v[36:39], a[20:23], a[124:127], v[36:39]\n"
      "s_add_u32 m0, s23, 100224\n"
      "v_mfma_f32_16x16x32_bf16 v[36:39], a[24:27], a[128:131], v[36:39]\n"
      "buffer_load_dwordx4 v2, s[40:43], 0 offen offset:128 lds\n"
      "v_mfma_f32_16x16x32_bf16 v[36:39], a[28:31], a[132:135], v[36:39]\n"
      "v_mfma_f32_16x16x32_bf16 v[40:43], a[0:3], a[136:139], v[28:31]\n"
      "v_mfma_f32_16x16x32_bf16 v[40:43], a[4:7], a[140:143], v[40:43]\n"
      "v_mfma_f32_16x16x32_bf16 v[40:43], a[8:11], a[144:147], v[40:43]\n"
      "v_mfma_f32_16x16x32_bf16 v[40:43], a[12:15], a[148:151], v[40:43]\n"
      "v_exp_f32 v36, v36\n"
      "v_mfma_f32_16x16x32_bf16 v[40:43], a[16:19], a[152:155], v[40:43]\n"
      "v_add_f32 v6, v6, v36\n"
      "v_mfma_f32_16x16x32_bf16 v[40:43], a[20:23], a[156:159], v[40:43]\n"
      "v_exp_f32 v37, v37\n"
      "v_mfma_f32_16x16x32_bf16 v[40:43], a[24:27], a[160:163], v[40:43]\n"
      "v_cvt_pk_bf16_f32 v84, v36, v37\n"
      "v_add_f32 v6, v6, v37\n"
      "v_mfma_f32_16x16x32_bf16 v[40:43], a[28:31], a[164:167], v[40:43]\n"
      "v_exp_f32 v38, v38\n"
      "v_mfma_f32_16x16x32_bf16 v[44:47], a[0:3], a[168:171], v[32:35]\n"
      "ds_read_b128 a[0:3], v3 offset:16384\n"
      "v_add_f32 v6, v6, v38\n"
      "v_mfma_f32_16x16x32_bf16 v[44:47], a[4:7], a[172:175], v[44:47]\n"
      "s_add_u32 m0, s23, 101248\n"
      "v_exp_f32 v39, v39\n"
      "v_mfma_f32_16x16x32_bf16 v[44:47], a[8:11], a[176:179], v[44:47]\n"
      "buffer_load_dwordx4 v2, s[40:43], s52 offen offset:128 lds\n"
      "v_mfma_f32_16x16x32_bf16 v[44:47], a[12:15], a[180:183], v[44:47]\n"
      "v_exp_f32 v40, v40\n"
      "v_mfma_f32_16x16x32_bf16 v[44:47], a[16:19], a[184:187], v[44:47]\n"
      "ds_read_b128 a[4:7], v4 offset:16384\n"
      "v_cvt_pk_bf16_f32 v85, v38, v39\n"
      "v_mfma_f32_16x16x32_bf16 v[44:47], a[20:23], a[188:191], v[44:47]\n"
      "v_exp_f32 v41, v41\n"
      "v_mfma_f32_16x16x32_bf16 v[44:47], a[24:27], a[192:195], v[44:47]\n"
      "ds_read_b128 a[8:11], v3 offset:18432\n"
      "v_cvt_pk_bf16_f32 v92, v40, v41\n"
      "v_mfma_f32_16x16x32_bf16 v[44:47], a[28:31], a[196:199], v[44:47]\n"
      "s_waitcnt lgkmcnt(3)\n"
      "v_exp_f32 v42, v42\n"
      "v_mfma_f32_16x16x32_bf16 v[48:51], a[32:35], a[104:107], v[24:27]\n"
      "ds_read_b128 a[12:15], v4 offset:18432\n"
      "v_add_f32 v6, v6, v39\n"
      "v_mfma_f32_16x16x32_bf16 v[48:51], a[36:39], a[108:111], v[48:51]\n"
      "s_add_u32 m0, s23, 102144\n"
      "v_exp_f32 v43, v43\n"
      "v_mfma_f32_16x16x32_bf16 v[48:51], a[40:43], a[112:115], v[48:51]\n"
      "buffer_load_dwordx4 v2, s[40:43], 0 offen offset:256 lds\n"
      "v_mfma_f32_16x16x32_bf16 v[48:51], a[44:47], a[116:119], v[48:51]\n"
      "v_exp_f32 v44, v44\n"
      "v_mfma_f32_16x16x32_bf16 v[48:51], a[48:51], a[120:123], v[48:51]\n"
      "ds_read_b128 a[16:19], v3 offset:20480\n"
      "v_cvt_pk_bf16_f32 v93, v42, v43\n"
      "v_mfma_f32_16x16x32_bf16 v[48:51], a[52:55], a[124:127], v[48:51]\n"
      "v_exp_f32 v45, v45\n"
      "v_mfma_f32_16x16x32_bf16 v[48:51], a[56:59], a[128:131], v[48:51]\n"
      "ds_read_b128 a[20:23], v4 offset:20480\n"
      "v_cvt_pk_bf16_f32 v100, v44, v45\n"
      "v_mfma_f32_16x16x32_bf16 v[48:51], a[60:63], a[132:135], v[48:51]\n"
      "v_exp_f32 v46, v46\n"
      "v_mfma_f32_16x16x32_bf16 v[52:55], a[32:35], a[136:139], v[28:31]\n"
      "ds_read_b128 a[24:27], v3 offset:22528\n"
      "v_add_f32 v8, v8, v40\n"
      "v_mfma_f32_16x16x32_bf16 v[52:55], a[36:39], a[140:143], v[52:55]\n"
      "v_exp_f32 v47, v47\n"
      "v_mfma_f32_16x16x32_bf16 v[52:55], a[40:43], a[144:147], v[52:55]\n"
      "ds_read_b128 a[28:31], v4 offset:22528\n"
      "v_cvt_pk_bf16_f32 v101, v46, v47\n"
      "v_mfma_f32_16x16x32_bf16 v[52:55], a[44:47], a[148:151], v[52:55]\n"
      "v_exp_f32 v48, v48\n"
      "v_mfma_f32_16x16x32_bf16 v[52:55], a[48:51], a[152:155], v[52:55]\n"
      "v_add_f32 v8, v8, v41\n"
      "v_add_f32 v8, v8, v42\n"
      "v_mfma_f32_16x16x32_bf16 v[52:55], a[52:55], a[156:159], v[52:55]\n"
      "s_add_u32 m0, s23, 103168\n"
      "v_exp_f32 v49, v49\n"
      "v_mfma_f32_16x16x32_bf16 v[52:55], a[56:59], a[160:163], v[52:55]\n"
      "buffer_load_dwordx4 v2, s[40:43], s52 offen offset:256 lds\n"
      "v_mfma_f32_16x16x32_bf16 v[52:55], a[60:63], a[164:167], v[52:55]\n"
      "v_exp_f32 v50, v50\n"
      "v_mfma_f32_16x16x32_bf16 v[56:59], a[32:35], a[168:171], v[32:35]\n"
      "ds_read_b128 a[32:35], v3 offset:24576\n"
      "v_cvt_pk_bf16_f32 v86, v48, v49\n"
      "v_mfma_f32_16x16x32_bf16 v[56:59], a[36:39], a[172:175], v[56:59]\n"
      "v_exp_f32 v51, v51\n"
      "v_mfma_f32_16x16x32_bf16 v[56:59], a[40:43], a[176:179], v[56:59]\n"
      "ds_read_b128 a[36:39], v4 offset:24576\n"
      "v_cvt_pk_bf16_f32 v87, v50, v51\n"
      "v_mfma_f32_16x16x32_bf16 v[56:59], a[44:47], a[180:183], v[56:59]\n"
      "v_exp_f32 v52, v52\n"
      "v_mfma_f32_16x16x32_bf16 v[56:59], a[48:51], a[184:187], v[56:59]\n"
      "ds_read_b128 a[40:43], v3 offset:26624\n"
      "v_add_f32 v8, v8, v43\n"
      "v_mfma_f32_16x16x32_bf16 v[56:59], a[52:55], a[188:191], v[56:59]\n"
      "s_add_u32 m0, s23, 104064\n"
      "v_exp_f32 v53, v53\n"
      "v_mfma_f32_16x16x32_bf16 v[56:59], a[56:59], a[192:195], v[56:59]\n"
      "buffer_load_dwordx4 v2, s[40:43], 0 offen offset:384 lds\n"
      "v_mfma_f32_16x16x32_bf16 v[56:59], a[60:63], a[196:199], v[56:59]\n"
      "s_waitcnt lgkmcnt(3)\n"
      "v_exp_f32 v54, v54\n"
      "v_mfma_f32_16x16x32_bf16 v[60:63], a[0:3], a[104:107], v[24:27]\n"
      "ds_read_b128 a[44:47], v4 offset:26624\n"
      "v_cvt_pk_bf16_f32 v94, v52, v53\n"
      "v_mfma_f32_16x16x32_bf16 v[60:63], a[4:7], a[108:111], v[60:63]\n"
      "v_exp_f32 v55, v55\n"
      "v_mfma_f32_16x16x32_bf16 v[60:63], a[8:11], a[112:115], v[60:63]\n"
      "ds_read_b128 a[48:51], v3 offset:28672\n"
      "v_cvt_pk_bf16_f32 v95, v54, v55\n"
      "v_mfma_f32_16x16x32_bf16 v[60:63], a[12:15], a[116:119], v[60:63]\n"
      "v_exp_f32 v56, v56\n"
      "v_mfma_f32_16x16x32_bf16 v[60:63], a[16:19], a[120:123], v[60:63]\n"
      "ds_read_b128 a[52:55], v4 offset:28672\n"
      "v_add_f32 v10, v10, v44\n"
      "v_mfma_f32_16x16x32_bf16 v[60:63], a[20:23], a[124:127], v[60:63]\n"
      "v_exp_f32 v57, v57\n"
      "v_mfma_f32_16x16x32_bf16 v[60:63], a[24:27], a[128:131], v[60:63]\n"
      "ds_read_b128 a[56:59], v3 offset:30720\n"
      "v_cvt_pk_bf16_f32 v102, v56, v57\n"
      "v_mfma_f32_16x16x32_bf16 v[60:63], a[28:31], a[132:135], v[60:63]\n"
      "v_exp_f32 v58, v58\n"
      "v_mfma_f32_16x16x32_bf16 v[64:67], a[0:3], a[136:139], v[28:31]\n"
      "ds_read_b128 a[60:63], v4 offset:30720\n"
      "v_add_f32 v10, v10, v45\n"
      "v_mfma_f32_16x16x32_bf16 v[64:67], a[4:7], a[140:143], v[64:67]\n"
      "s_add_u32 m0, s23, 105088\n"
      "v_exp_f32 v59, v59\n"
      "v_mfma_f32_16x16x32_bf16 v[64:67], a[8:11], a[144:147], v[64:67]\n"
      "buffer_load_dwordx4 v2, s[40:43], s52 offen offset:384 lds\n"
      "v_mfma_f32_16x16x32_bf16 v[64:67], a[12:15], a[148:151], v[64:67]\n"
      "v_exp_f32 v60, v60\n"
      "v_mfma_f32_16x16x32_bf16 v[64:67], a[16:19], a[152:155], v[64:67]\n"
      "v_cvt_pk_bf16_f32 v103, v58, v59\n"
      "v_add_u32 v2, 0x8000, v2\n"
      "v_mfma_f32_16x16x32_bf16 v[64:67], a[20:23], a[156:159], v[64:67]\n"
      "v_exp_f32 v61, v61\n"
      "v_mfma_f32_16x16x32_bf16 v[64:67], a[24:27], a[160:163], v[64:67]\n"
      "v_cvt_pk_bf16_f32 v88, v60, v61\n"
      "v_add_f32 v10, v10, v46\n"
      "v_mfma_f32_16x16x32_bf16 v[64:67], a[28:31], a[164:167], v[64:67]\n"
      "v_exp_f32 v62, v62\n"
      "v_mfma_f32_16x16x32_bf16 v[68:71], a[0:3], a[168:171], v[32:35]\n"
      "v_add_f32 v10, v10, v47\n"
      "v_add_f32 v6, v6, v48\n"
      "v_mfma_f32_16x16x32_bf16 v[68:71], a[4:7], a[172:175], v[68:71]\n"
      "v_exp_f32 v63, v63\n"
      "v_mfma_f32_16x16x32_bf16 v[68:71], a[8:11], a[176:179], v[68:71]\n"
      "s_waitcnt vmcnt(17) lgkmcnt(0)\n"
      "v_cvt_pk_bf16_f32 v89, v62, v63\n"
      "v_add_f32 v6, v6, v49\n"
      "v_mfma_f32_16x16x32_bf16 v[68:71], a[12:15], a[180:183], v[68:71]\n"
      "s_barrier\n"
      "v_exp_f32 v64, v64\n"
      "v_mfma_f32_16x16x32_bf16 v[68:71], a[16:19], a[184:187], v[68:71]\n"
      "ds_read_b64_tr_b16 a[64:65], v12 offset:0\n"
      "v_add_f32 v6, v6, v50\n"
      "v_mfma_f32_16x16x32_bf16 v[68:71], a[20:23], a[188:191], v[68:71]\n"
      "v_exp_f32 v65, v65\n"
      "v_mfma_f32_16x16x32_bf16 v[68:71], a[24:27], a[192:195], v[68:71]\n"
      "ds_read_b64_tr_b16 a[66:67], v12 offset:8192\n"
      "v_cvt_pk_bf16_f32 v96, v64, v65\n"
      "v_mfma_f32_16x16x32_bf16 v[68:71], a[28:31], a[196:199], v[68:71]\n"
      "v_exp_f32 v66, v66\n"
      "v_mfma_f32_16x16x32_bf16 v[72:75], a[32:35], a[104:107], v[24:27]\n"
      "ds_read_b64_tr_b16 a[68:69], v12 offset:16384\n"
      "v_add_f32 v6, v6, v51\n"
      "v_mfma_f32_16x16x32_bf16 v[72:75], a[36:39], a[108:111], v[72:75]\n"
      "v_exp_f32 v67, v67\n"
      "v_mfma_f32_16x16x32_bf16 v[72:75], a[40:43], a[112:115], v[72:75]\n"
      "ds_read_b64_tr_b16 a[70:71], v12 offset:24576\n"
      "v_cvt_pk_bf16_f32 v97, v66, v67\n"
      "v_mfma_f32_16x16x32_bf16 v[72:75], a[44:47], a[116:119], v[72:75]\n"
      "v_exp_f32 v68, v68\n"
      "v_mfma_f32_16x16x32_bf16 v[72:75], a[48:51], a[120:123], v[72:75]\n"
      "ds_read_b64_tr_b16 a[72:73], v13 offset:0\n"
      "v_add_f32 v8, v8, v52\n"
      "v_mfma_f32_16x16x32_bf16 v[72:75], a[52:55], a[124:127], v[72:75]\n"
      "v_exp_f32 v69, v69\n"
      "v_mfma_f32_16x16x32_bf16 v[72:75], a[56:59], a[128:131], v[72:75]\n"
      "ds_read_b64_tr_b16 a[74:75], v13 offset:8192\n"
      "v_cvt_pk_bf16_f32 v104, v68, v69\n"
      "v_mfma_f32_16x16x32_bf16 v[72:75], a[60:63], a[132:135], v[72:75]\n"
      "v_exp_f32 v70, v70\n"
      "v_mfma_f32_16x16x32_bf16 v[76:79], a[32:35], a[136:139], v[28:31]\n"
      "ds_read_b64_tr_b16 a[76:77], v13 offset:16384\n"
      "v_add_f32 v8, v8, v53\n"
      "v_mfma_f32_16x16x32_bf16 v[76:79], a[36:39], a[140:143], v[76:79]\n"
      "v_exp_f32 v71, v71\n"
      "v_mfma_f32_16x16x32_bf16 v[76:79], a[40:43], a[144:147], v[76:79]\n"
      "ds_read_b64_tr_b16 a[78:79], v13 offset:24576\n"
      "v_cvt_pk_bf16_f32 v105, v70, v71\n"
      "v_mfma_f32_16x16x32_bf16 v[76:79], a[44:47], a[148:151], v[76:79]\n"
      "v_exp_f32 v72, v72\n"
      "v_mfma_f32_16x16x32_bf16 v[76:79], a[48:51], a[152:155], v[76:79]\n"
      "ds_read_b64_tr_b16 a[80:81], v14 offset:0\n"
      "v_add_f32 v8, v8, v54\n"
      "v_mfma_f32_16x16x32_bf16 v[76:79], a[52:55], a[156:159], v[76:79]\n"
      "v_exp_f32 v73, v73\n"
      "v_mfma_f32_16x16x32_bf16 v[76:79], a[56:59], a[160:163], v[76:79]\n"
      "ds_read_b64_tr_b16 a[82:83], v14 offset:8192\n"
      "v_cvt_pk_bf16_f32 v90, v72, v73\n"
      "v_mfma_f32_16x16x32_bf16 v[76:79], a[60:63], a[164:167], v[76:79]\n"
      "v_exp_f32 v74, v74\n"
      "v_mfma_f32_16x16x32_bf16 v[80:83], a[32:35], a[168:171], v[32:35]\n"
      "ds_read_b64_tr_b16 a[84:85], v14 offset:16384\n"
      "v_add_f32 v8, v8, v55\n"
      "v_mfma_f32_16x16x32_bf16 v[80:83], a[36:39], a[172:175], v[80:83]\n"
      "v_exp_f32 v75, v75\n"
      "v_mfma_f32_16x16x32_bf16 v[80:83], a[40:43], a[176:179], v[80:83]\n"
      "ds_read_b64_tr_b16 a[86:87], v14 offset:24576\n"
      "v_cvt_pk_bf16_f32 v91, v74, v75\n"
      "v_mfma_f32_16x16x32_bf16 v[80:83], a[44:47], a[180:183], v[80:83]\n"
      "v_exp_f32 v76, v76\n"
      "v_mfma_f32_16x16x32_bf16 v[80:83], a[48:51], a[184:187], v[80:83]\n"
      "v_mfma_f32_16x16x32_bf16 v[80:83], a[52:55], a[188:191], v[80:83]\n"
      "v_exp_f32 v77, v77\n"
      "v_mfma_f32_16x16x32_bf16 v[80:83], a[56:59], a[192:195], v[80:83]\n"
      "v_cvt_pk_bf16_f32 v98, v76, v77\n"
      "v_mfma_f32_16x16x32_bf16 v[80:83], a[60:63], a[196:199], v[80:83]\n"
      "s_waitcnt lgkmcnt(8)\n"
      "v_exp_f32 v78, v78\n"
      "L_loopD2_%=:\n"
      "v_mfma_f32_16x16x32_bf16 a[200:203], v[84:87], a[64:67], a[200:203]\n"
      "s_add_u32 m0, s23, 0\n"
      "ds_read_b64_tr_b16 a[88:89], v15 offset:0\n"
      "v_add_f32 v10, v10, v56\n"
      "v_mfma_f32_16x16x32_bf16 a[200:203], v[88:91], a[68:71], a[200:203]\n"
      "s_waitcnt lgkmcnt(5)\n"
      "v_exp_f32 v79, v79\n"
      "v_mfma_f32_16x16x32_bf16 a[212:215], v[84:87], a[72:75], a[212:215]\n"
      "buffer_load_dwordx4 v1, s[36:39], 0 offen offset:0 lds\n"
      "v_mfma_f32_16x16x32_bf16 a[212:215], v[88:91], a[76:79], a[212:215]\n"
      "s_waitcnt lgkmcnt(1)\n"
      "v_exp_f32 v80, v80\n"
      "v_mfma_f32_16x16x32_bf16 a[224:227], v[84:87], a[80:83], a[224:227]\n"
      "ds_read_b64_tr_b16 a[90:91], v15 offset:8192\n"
      "v_cvt_pk_bf16_f32 v99, v78, v79\n"
      "v_mfma_f32_16x16x32_bf16 a[224:227], v[88:91], a[84:87], a[224:227]\n"
      "v_exp_f32 v81, v81\n"
      "v_mfma_f32_16x16x32_bf16 a[204:207], v[92:95], a[64:67], a[204:207]\n"
      "ds_read_b64_tr_b16 a[92:93], v15 offset:16384\n"
      "v_cvt_pk_bf16_f32 v106, v80, v81\n"
      "v_mfma_f32_16x16x32_bf16 a[204:207], v[96:99], a[68:71], a[204:207]\n"
      "v_exp_f32 v82, v82\n"
      "v_mfma_f32_16x16x32_bf16 a[216:219], v[92:95], a[72:75], a[216:219]\n"
      "ds_read_b64_tr_b16 a[94:95], v15 offset:24576\n"
      "v_add_f32 v10, v10, v57\n"
      "v_mfma_f32_16x16x32_bf16 a[216:219], v[96:99], a[76:79], a[216:219]\n"
      "s_add_u32 m0, s23, 1024\n"
      "v_exp_f32 v83, v83\n"
      "v_mfma_f32_16x16x32_bf16 a[228:231], v[92:95], a[80:83], a[228:231]\n"
      "buffer_load_dwordx4 v1, s[36:39], s52 offen offset:0 lds\n"
      "v_mfma_f32_16x16x32_bf16 a[228:231], v[96:99], a[84:87], a[228:231]\n"
      "ds_read_b64_tr_b16 a[96:97], v12 offset:2048\n"
      "v_cvt_pk_bf16_f32 v107, v82, v83\n"
      "v_mfma_f32_16x16x32_bf16 a[208:211], v[100:103], a[64:67], a[208:211]\n"
      "ds_read_b64_tr_b16 a[98:99], v12 offset:10240\n"
      "v_add_f32 v10, v10, v58\n"
      "v_mfma_f32_16x16x32_bf16 a[208:211], v[104:107], a[68:71], a[208:211]\n"
      "ds_read_b64_tr_b16 a[100:101], v12 offset:18432\n"
      "v_add_f32 v10, v10, v59\n"
      "v_mfma_f32_16x16x32_bf16 a[220:223], v[100:103], a[72:75], a[220:223]\n"
      "ds_read_b64_tr_b16 a[102:103], v12 offset:26624\n"
      "v_add_f32 v6, v6, v60\n"
      "v_mfma_f32_16x16x32_bf16 a[220:223], v[104:107], a[76:79], a[220:223]\n"
      "ds_read_b64_tr_b16 a[64:65], v13 offset:2048\n"
      "v_add_f32 v6, v6, v61\n"
      "v_mfma_f32_16x16x32_bf16 a[232:235], v[100:103], a[80:83], a[232:235]\n"
      "s_add_u32 m0, s23, 1920\n"
      "ds_read_b64_tr_b16 a[66:67], v13 offset:10240\n"
      "v_add_f32 v6, v6, v62\n"
      "v_mfma_f32_16x16x32_bf16 a[232:235], v[104:107], a[84:87], a[232:235]\n"
      "s_waitcnt lgkmcnt(6)\n"
      "ds_read_b64_tr_b16 a[68:69], v13 offset:18432\n"
      "v_add_f32 v6, v6, v63\n"
      "v_mfma_f32_16x16x32_bf16 a[236:239], v[84:87], a[88:91], a[236:239]\n"
      "buffer_load_dwordx4 v1, s[36:39], 0 offen offset:128 lds\n"
      "v_mfma_f32_16x16x32_bf16 a[236:239], v[88:91], a[92:95], a[236:239]\n"
      "ds_read_b64_tr_b16 a[70:71], v13 offset:26624\n"
      "v_add_f32 v8, v8, v64\n"
      "v_mfma_f32_16x16x32_bf16 a[240:243], v[92:95], a[88:91], a[240:243]\n"
      "ds_read_b64_tr_b16 a[72:73], v14 offset:2048\n"
      "v_add_f32 v8, v8, v65\n"
      "v_mfma_f32_16x16x32_bf16 a[240:243], v[96:99], a[92:95], a[240:243]\n"
      "ds_read_b64_tr_b16 a[74:75], v14 offset:10240\n"
      "v_add_f32 v8, v8, v66\n"
      "v_mfma_f32_16x16x32_bf16 v[108:111], v[100:103], a[88:91], v[108:111]\n"
      "ds_read_b64_tr_b16 a[76:77], v14 offset:18432\n"
      "v_add_f32 v8, v8, v67\n"
      "v_mfma_f32_16x16x32_bf16 v[108:111], v[104:107], a[92:95], v[108:111]\n"
      "s_waitcnt lgkmcnt(7)\n"
      "ds_read_b64_tr_b16 a[78:79], v14 offset:26624\n"
      "v_add_f32 v10, v10, v68\n"
      "v_mfma_f32_16x16x32_bf16 v[112:115], v[84:87], a[96:99], v[112:115]\n"
      "ds_read_b64_tr_b16 a[80:81], v15 offset:2048\n"
      "v_add_f32 v10, v10, v69\n"
      "v_mfma_f32_16x16x32_bf16 v[112:115], v[88:91], a[100:103], v[112:115]\n"
      "ds_read_b64_tr_b16 a[82:83], v15 offset:10240\n"
      "v_add_f32 v10, v10, v70\n"
      "v_mfma_f32_16x16x32_bf16 v[116:119], v[92:95], a[96:99], v[116:119]\n"
      "ds_read_b64_tr_b16 a[84:85], v15 offset:18432\n"
      "v_add_f32 v10, v10, v71\n"
      "v_mfma_f32_16x16x32_bf16 v[116:119], v[96:99], a[100:103], v[116:119]\n"
      "ds_read_b64_tr_b16 a[86:87], v15 offset:26624\n"
      "v_add_f32 v6, v6, v72\n"
      "v_mfma_f32_16x16x32_bf16 v[120:123], v[100:103], a[96:99], v[120:123]\n"
      "s_add_u32 m0, s23, 2944\n"
      "ds_read_b64_tr_b16 a[88:89], v12 offset:4096\n"
      "v_add_f32 v6, v6, v73\n"
      "v_mfma_f32_16x16x32_bf16 v[120:123], v[104:107], a[100:103], v[120:123]\n"
      "s_waitcnt lgkmcnt(9)\n"
      "ds_read_b64_tr_b16 a[90:91], v12 offset:12288\n"
      "v_add_f32 v6, v6, v74\n"
      "v_mfma_f32_16x16x32_bf16 v[124:127], v[84:87], a[64:67], v[124:127]\n"
      "buffer_load_dwordx4 v1, s[36:39], s52 offen offset:128 lds\n"
      "v_mfma_f32_16x16x32_bf16 v[124:127], v[88:91], a[68:71], v[124:127]\n"
      "ds_read_b64_tr_b16 a[92:93], v12 offset:20480\n"
      "v_add_f32 v6, v6, v75\n"
      "v_mfma_f32_16x16x32_bf16 v[128:131], v[92:95], a[64:67], v[128:131]\n"
      "ds_read_b64_tr_b16 a[94:95], v12 offset:28672\n"
      "v_add_f32 v8, v8, v76\n"
      "v_mfma_f32_16x16x32_bf16 v[128:131], v[96:99], a[68:71], v[128:131]\n"
      "ds_read_b64_tr_b16 a[96:97], v13 offset:4096\n"
      "v_add_f32 v8, v8, v77\n"
      "v_mfma_f32_16x16x32_bf16 v[132:135], v[100:103], a[64:67], v[132:135]\n"
      "ds_read_b64_tr_b16 a[98:99], v13 offset:12288\n"
      "v_add_f32 v8, v8, v78\n"
      "v_mfma_f32_16x16x32_bf16 v[132:135], v[104:107], a[68:71], v[132:135]\n"
      "s_waitcnt lgkmcnt(10)\n"
      "ds_read_b64_tr_b16 a[100:101], v13 offset:20480\n"
      "v_add_f32 v8, v8, v79\n"
      "v_mfma_f32_16x16x32_bf16 v[136:139], v[84:87], a[72:75], v[136:139]\n"
      "ds_read_b64_tr_b16 a[102:103], v13 offset:28672\n"
      "v_add_f32 v10, v10, v80\n"
      "v_mfma_f32_16x16x32_bf16 v[136:139], v[88:91], a[76:79], v[136:139]\n"
      "s_add_u32 m0, s23, 3840\n"
      "ds_read_b64_tr_b16 a[64:65], v14 offset:4096\n"
      "v_add_f32 v10, v10, v81\n"
      "v_mfma_f32_16x16x32_bf16 v[140:143], v[92:95], a[72:75], v[140:143]\n"
      "buffer_load_dwordx4 v1, s[36:39], 0 offen offset:256 lds\n"
      "v_mfma_f32_16x16x32_bf16 v[140:143], v[96:99], a[76:79], v[140:143]\n"
      "ds_read_b64_tr_b16 a[66:67], v14 offset:12288\n"
      "v_add_f32 v10, v10, v82\n"
      "v_mfma_f32_16x16x32_bf16 v[144:147], v[100:103], a[72:75], v[144:147]\n"
      "ds_read_b64_tr_b16 a[68:69], v14 offset:20480\n"
      "v_add_f32 v10, v10, v83\n"
      "v_mfma_f32_16x16x32_bf16 v[144:147], v[104:107], a[76:79], v[144:147]\n"
      "s_waitcnt lgkmcnt(11)\n"
      "ds_read_b64_tr_b16 a[70:71], v14 offset:28672\n"
      "v_mfma_f32_16x16x32_bf16 v[148:151], v[84:87], a[80:83], v[148:151]\n"
      "ds_read_b64_tr_b16 a[72:73], v15 offset:4096\n"
      "v_mfma_f32_16x16x32_bf16 v[148:151], v[88:91], a[84:87], v[148:151]\n"
      "ds_read_b64_tr_b16 a[74:75], v15 offset:12288\n"
      "v_mfma_f32_16x16x32_bf16 v[152:155], v[92:95], a[80:83], v[152:155]\n"
      "ds_read_b64_tr_b16 a[76:77], v15 offset:20480\n"
      "v_mfma_f32_16x16x32_bf16 v[152:155], v[96:99], a[84:87], v[152:155]\n"
      "s_add_u32 m0, s23, 4864\n"
      "ds_read_b64_tr_b16 a[78:79], v15 offset:28672\n"
      "v_mfma_f32_16x16x32_bf16 v[156:159], v[100:103], a[80:83], v[156:159]\n"
      "buffer_load_dwordx4 v1, s[36:39], s52 offen offset:256 lds\n"
      "v_mfma_f32_16x16x32_bf16 v[156:159], v[104:107], a[84:87], v[156:159]\n"
      "s_waitcnt lgkmcnt(12)\n"
      "ds_read_b64_tr_b16 a[80:81], v12 offset:6144\n"
      "v_mfma_f32_16x16x32_bf16 v[160:163], v[84:87], a[88:91], v[160:163]\n"
      "ds_read_b64_tr_b16 a[82:83], v12 offset:14336\n"
      "v_mfma_f32_16x16x32_bf16 v[160:163], v[88:91], a[92:95], v[160:163]\n"
      "ds_read_b64_tr_b16 a[84:85], v12 offset:22528\n"
      "v_mfma_f32_16x16x32_bf16 v[164:167], v[92:95], a[88:91], v[164:167]\n"
      "ds_read_b64_tr_b16 a[86:87], v12 offset:30720\n"
      "v_mfma_f32_16x16x32_bf16 v[164:167], v[96:99], a[92:95], v[164:167]\n"
      "v_mfma_f32_16x16x32_bf16 v[168:171], v[100:103], a[88:91], v[168:171]\n"
      "s_add_u32 m0, s23, 5760\n"
      "v_mfma_f32_16x16x32_bf16 v[168:171], v[104:107], a[92:95], v[168:171]\n"
      "s_waitcnt lgkmcnt(12)\n"
      "ds_read_b64_tr_b16 a[88:89], v13 offset:6144\n"
      "v_mfma_f32_16x16x32_bf16 v[172:175], v[84:87], a[96:99], v[172:175]\n"
      "buffer_load_dwordx4 v1, s[36:39], 0 offen offset:384 lds\n"
      "v_mfma_f32_16x16x32_bf16 v[172:175], v[88:91], a[100:103], v[172:175]\n"
      "ds_read_b64_tr_b16 a[90:91], v13 offset:14336\n"
      "v_mfma_f32_16x16x32_bf16 v[176:179], v[92:95], a[96:99], v[176:179]\n"
      "ds_read_b64_tr_b16 a[92:93], v13 offset:22528\n"
      "v_mfma_f32_16x16x32_bf16 v[176:179], v[96:99], a[100:103], v[176:179]\n"
      "ds_read_b64_tr_b16 a[94:95], v13 offset:30720\n"
      "v_mfma_f32_16x16x32_bf16 v[180:183], v[100:103], a[96:99], v[180:183]\n"
      "v_mfma_f32_16x16x32_bf16 v[180:183], v[104:107], a[100:103], v[180:183]\n"
      "s_waitcnt lgkmcnt(12)\n"
      "ds_read_b64_tr_b16 a[96:97], v14 offset:6144\n"
      "v_mfma_f32_16x16x32_bf16 v[184:187], v[84:87], a[64:67], v[184:187]\n"
      "ds_read_b64_tr_b16 a[98:99], v14 offset:14336\n"
      "v_mfma_f32_16x16x32_bf16 v[184:187], v[88:91], a[68:71], v[184:187]\n"
      "ds_read_b64_tr_b16 a[100:101], v14 offset:22528\n"
      "v_mfma_f32_16x16x32_bf16 v[188:191], v[92:95], a[64:67], v[188:191]\n"
      "ds_read_b64_tr_b16 a[102:103], v14 offset:30720\n"
      "v_mfma_f32_16x16x32_bf16 v[188:191], v[96:99], a[68:71], v[188:191]\n"
      "v_mfma_f32_16x16x32_bf16 v[192:195], v[100:103], a[64:67], v[192:195]\n"
      "s_add_u32 m0, s23, 6784\n"
      "v_mfma_f32_16x16x32_bf16 v[192:195], v[104:107], a[68:71], v[192:195]\n"
      "s_waitcnt lgkmcnt(12)\n"
      "ds_read_b64_tr_b16 a[64:65], v15 offset:6144\n"
      "v_mfma_f32_16x16x32_bf16 v[196:199], v[84:87], a[72:75], v[196:199]\n"
      "buffer_load_dwordx4 v1, s[36:39], s52 offen offset:384 lds\n"
      "v_mfma_f32_16x16x32_bf16 v[196:199], v[88:91], a[76:79], v[196:199]\n"
      "ds_read_b64_tr_b16 a[66:67], v15 offset:14336\n"
      "v_add_u32 v1, 0x8000, v1\n"
      "v_mfma_f32_16x16x32_bf16 v[200:203], v[92:95], a[72:75], v[200:203]\n"
      "ds_read_b64_tr_b16 a[68:69], v15 offset:22528\n"
      "v_mfma_f32_16x16x32_bf16 v[200:203], v[96:99], a[76:79], v[200:203]\n"
      "ds_read_b64_tr_b16 a[70:71], v15 offset:30720\n"
      "v_mfma_f32_16x16x32_bf16 v[204:207], v[100:103], a[72:75], v[204:207]\n"
      "buffer_load_dword v23, v22, s[60:63], 0 offen\n"
      "v_mfma_f32_16x16x32_bf16 v[204:207], v[104:107], a[76:79], v[204:207]\n"
      "s_waitcnt lgkmcnt(12)\n"
      "v_add_u32 v22, 0x400, v22\n"
      "v_mfma_f32_16x16x32_bf16 v[208:211], v[84:87], a[80:83], v[208:211]\n"
      "v_mfma_f32_16x16x32_bf16 v[208:211], v[88:91], a[84:87], v[208:211]\n"
      "s_getreg_b32 s29, hwreg(HW_REG_IB_STS, 0, 4)\n"
      "v_mfma_f32_16x16x32_bf16 v[212:215], v[92:95], a[80:83], v[212:215]\n"
      "s_cmp_ge_u32 s29, 10\n"
      "v_mfma_f32_16x16x32_bf16 v[212:215], v[96:99], a[84:87], v[212:215]\n"
      "s_cbranch_scc1 L_midN2_%=\n"
      "v_mfma_f32_16x16x32_bf16 v[216:219], v[100:103], a[80:83], v[216:219]\n"
      "v_mfma_f32_16x16x32_bf16 v[216:219], v[104:107], a[84:87], v[216:219]\n"
      "s_waitcnt lgkmcnt(8)\n"
      "v_mfma_f32_16x16x32_bf16 v[220:223], v[84:87], a[88:91], v[220:223]\n"
      "s_waitcnt vmcnt(17) lgkmcnt(0)\n"
      "v_mfma_f32_16x16x32_bf16 v[220:223], v[88:91], a[92:95], v[220:223]\n"
      "s_barrier\n"
      "ds_read_b128 a[0:3], v3 offset:32768\n"
      "v_mfma_f32_16x16x32_bf16 v[224:227], v[92:95], a[88:91], v[224:227]\n"
      "ds_read_b128 a[4:7], v4 offset:32768\n"
      "v_mfma_f32_16x16x32_bf16 v[224:227], v[96:99], a[92:95], v[224:227]\n"
      "s_add_u32 m0, s23, 65536\n"
      "ds_read_b128 a[8:11], v3 offset:34816\n"
      "v_mfma_f32_16x16x32_bf16 v[228:231], v[100:103], a[88:91], v[228:231]\n"
      "buffer_load_dwordx4 v2, s[40:43], 0 offen offset:0 lds\n"
      "v_mfma_f32_16x16x32_bf16 v[228:231], v[104:107], a[92:95], v[228:231]\n"
      "ds_read_b128 a[12:15], v4 offset:34816\n"
      "v_mfma_f32_16x16x32_bf16 v[232:235], v[84:87], a[96:99], v[232:235]\n"
      "ds_read_b128 a[16:19], v3 offset:36864\n"
      "v_mfma_f32_16x16x32_bf16 v[232:235], v[88:91], a[100:103], v[232:235]\n"
      "ds_read_b128 a[20:23], v4 offset:36864\n"
      "v_mfma_f32_16x16x32_bf16 v[236:239], v[92:95], a[96:99], v[236:239]\n"
      "ds_read_b128 a[24:27], v3 offset:38912\n"
      "v_mfma_f32_16x16x32_bf16 v[236:239], v[96:99], a[100:103], v[236:239]\n"
      "ds_read_b128 a[28:31], v4 offset:38912\n"
      "v_mfma_f32_16x16x32_bf16 v[240:243], v[100:103], a[96:99], v[240:243]\n"
      "ds_read_b128 a[32:35], v3 offset:40960\n"
      "v_mfma_f32_16x16x32_bf16 v[240:243], v[104:107], a[100:103], v[240:243]\n"
      "ds_read_b128 a[36:39], v4 offset:40960\n"
      "v_mfma_f32_16x16x32_bf16 v[244:247], v[84:87], a[64:67], v[244:247]\n"
      "ds_read_b128 a[40:43], v3 offset:43008\n"
      "v_mfma_f32_16x16x32_bf16 v[244:247], v[88:91], a[68:71], v[244:247]\n"
      "ds_read_b128 a[44:47], v4 offset:43008\n"
      "v_mfma_f32_16x16x32_bf16 v[248:251], v[92:95], a[64:67], v[248:251]\n"
      "ds_read_b128 a[48:51], v3 offset:45056\n"
      "v_mfma_f32_16x16x32_bf16 v[248:251], v[96:99], a[68:71], v[248:251]\n"
      "s_add_u32 m0, s23, 66560\n"
      "ds_read_b128 a[52:55], v4 offset:45056\n"
      "v_mfma_f32_16x16x32_bf16 v[252:255], v[100:103], a[64:67], v[252:255]\n"
      "buffer_load_dwordx4 v2, s[40:43], s52 offen offset:0 lds\n"
      "v_mfma_f32_16x16x32_bf16 v[252:255], v[104:107], a[68:71], v[252:255]\n"
      "s_waitcnt lgkmcnt(6)\n"
      "ds_read_b128 a[56:59], v3 offset:47104\n"
      "v_mfma_f32_16x16x32_bf16 v[36:39], a[0:3], a[104:107], v[24:27]\n"
      "ds_read_b128 a[60:63], v4 offset:47104\n"
      "v_mfma_f32_16x16x32_bf16 v[36:39], a[4:7], a[108:111], v[36:39]\n"
      "v_mfma_f32_16x16x32_bf16 v[36:39], a[8:11], a[112:115], v[36:39]\n"
      "v_mfma_f32_16x16x32_bf16 v[36:39], a[12:15], a[116:119], v[36:39]\n"
      "v_mfma_f32_16x16x32_bf16 v[36:39], a[16:19], a[120:123], v[36:39]\n"
      "v_mfma_f32_16x16x32_bf16 v[36:39], a[20:23], a[124:127], v[36:39]\n"
      "s_add_u32 m0, s23, 67456\n"
      "v_mfma_f32_16x16x32_bf16 v[36:39], a[24:27], a[128:131], v[36:39]\n"
      "buffer_load_dwordx4 v2, s[40:43], 0 offen offset:128 lds\n"
      "v_mfma_f32_16x16x32_bf16 v[36:39], a[28:31], a[132:135], v[36:39]\n"
      "v_mfma_f32_16x16x32_bf16 v[40:43], a[0:3], a[136:139], v[28:31]\n"
      "v_mfma_f32_16x16x32_bf16 v[40:43], a[4:7], a[140:143], v[40:43]\n"
      "v_mfma_f32_16x16x32_bf16 v[40:43], a[8:11], a[144:147], v[40:43]\n"
      "v_mfma_f32_16x16x32_bf16 v[40:43], a[12:15], a[148:151], v[40:43]\n"
      "v_exp_f32 v36, v36\n"
      "v_mfma_f32_16x16x32_bf16 v[40:43], a[16:19], a[152:155], v[40:43]\n"
      "v_add_f32 v6, v6, v36\n"
      "v_mfma_f32_16x16x32_bf16 v[40:43], a[20:23], a[156:159], v[40:43]\n"
      "v_exp_f32 v37, v37\n"
      "v_mfma_f32_16x16x32_bf16 v[40:43], a[24:27], a[160:163], v[40:43]\n"
      "v_cvt_pk_bf16_f32 v84, v36, v37\n"
      "v_add_f32 v6, v6, v37\n"
      "v_mfma_f32_16x16x32_bf16 v[40:43], a[28:31], a[164:167], v[40:43]\n"
      "v_exp_f32 v38, v38\n"
      "v_mfma_f32_16x16x32_bf16 v[44:47], a[0:3], a[168:171], v[32:35]\n"
      "ds_read_b128 a[0:3], v3 offset:49152\n"
      "v_add_f32 v6, v6, v38\n"
      "v_mfma_f32_16x16x32_bf16 v[44:47], a[4:7], a[172:175], v[44:47]\n"
      "s_add_u32 m0, s23, 68480\n"
      "v_exp_f32 v39, v39\n"
      "v_mfma_f32_16x16x32_bf16 v[44:47], a[8:11], a[176:179], v[44:47]\n"
      "buffer_load_dwordx4 v2, s[40:43], s52 offen offset:128 lds\n"
      "v_mfma_f32_16x16x32_bf16 v[44:47], a[12:15], a[180:183], v[44:47]\n"
      "v_exp_f32 v40, v40\n"
      "v_mfma_f32_16x16x32_bf16 v[44:47], a[16:19], a[184:187], v[44:47]\n"
      "ds_read_b128 a[4:7], v4 offset:49152\n"
      "v_cvt_pk_bf16_f32 v85, v38, v39\n"
      "v_mfma_f32_16x16x32_bf16 v[44:47], a[20:23], a[188:191], v[44:47]\n"
      "v_exp_f32 v41, v41\n"
      "v_mfma_f32_16x16x32_bf16 v[44:47], a[24:27], a[192:195], v[44:47]\n"
      "ds_read_b128 a[8:11], v3 offset:51200\n"
      "v_cvt_pk_bf16_f32 v92, v40, v41\n"
      "v_mfma_f32_16x16x32_bf16 v[44:47], a[28:31], a[196:199], v[44:47]\n"
      "s_waitcnt lgkmcnt(3)\n"
      "v_exp_f32 v42, v42\n"
      "v_mfma_f32_16x16x32_bf16 v[48:51], a[32:35], a[104:107], v[24:27]\n"
      "ds_read_b128 a[12:15], v4 offset:51200\n"
      "v_add_f32 v6, v6, v39\n"
      "v_mfma_f32_16x16x32_bf16 v[48:51], a[36:39], a[108:111], v[48:51]\n"
      "s_add_u32 m0, s23, 69376\n"
      "v_exp_f32 v43, v43\n"
      "v_mfma_f32_16x16x32_bf16 v[48:51], a[40:43], a[112:115], v[48:51]\n"
      "buffer_load_dwordx4 v2, s[40:43], 0 offen offset:256 lds\n"
      "v_mfma_f32_16x16x32_bf16 v[48:51], a[44:47], a[116:119], v[48:51]\n"
      "v_exp_f32 v44, v44\n"
      "v_mfma_f32_16x16x32_bf16 v[48:51], a[48:51], a[120:123], v[48:51]\n"
      "ds_read_b128 a[16:19], v3 offset:53248\n"
      "v_cvt_pk_bf16_f32 v93, v42, v43\n"
      "v_mfma_f32_16x16x32_bf16 v[48:51], a[52:55], a[124:127], v[48:51]\n"
      "v_exp_f32 v45, v45\n"
      "v_mfma_f32_16x16x32_bf16 v[48:51], a[56:59], a[128:131], v[48:51]\n"
      "ds_read_b128 a[20:23], v4 offset:53248\n"
      "v_cvt_pk_bf16_f32 v100, v44, v45\n"
      "v_mfma_f32_16x16x32_bf16 v[48:51], a[60:63], a[132:135], v[48:51]\n"
      "v_exp_f32 v46, v46\n"
      "v_mfma_f32_16x16x32_bf16 v[52:55], a[32:35], a[136:139], v[28:31]\n"
      "ds_read_b128 a[24:27], v3 offset:55296\n"
      "v_add_f32 v8, v8, v40\n"
      "v_mfma_f32_16x16x32_bf16 v[52:55], a[36:39], a[140:143], v[52:55]\n"
      "v_exp_f32 v47, v47\n"
      "v_mfma_f32_16x16x32_bf16 v[52:55], a[40:43], a[144:147], v[52:55]\n"
      "ds_read_b128 a[28:31], v4 offset:55296\n"
      "v_cvt_pk_bf16_f32 v101, v46, v47\n"
      "v_mfma_f32_16x16x32_bf16 v[52:55], a[44:47], a[148:151], v[52:55]\n"
      "v_exp_f32 v48, v48\n"
      "v_mfma_f32_16x16x32_bf16 v[52:55], a[48:51], a[152:155], v[52:55]\n"
      "v_add_f32 v8, v8, v41\n"
      "v_add_f32 v8, v8, v42\n"
      "v_mfma_f32_16x16x32_bf16 v[52:55], a[52:55], a[156:159], v[52:55]\n"
      "s_add_u32 m0, s23, 70400\n"
      "v_exp_f32 v49, v49\n"
      "v_mfma_f32_16x16x32_bf16 v[52:55], a[56:59], a[160:163], v[52:55]\n"
      "buffer_load_dwordx4 v2, s[40:43], s52 offen offset:256 lds\n"
      "v_mfma_f32_16x16x32_bf16 v[52:55], a[60:63], a[164:167], v[52:55]\n"
      "v_exp_f32 v50, v50\n"
      "v_mfma_f32_16x16x32_bf16 v[56:59], a[32:35], a[168:171], v[32:35]\n"
      "ds_read_b128 a[32:35], v3 offset:57344\n"
      "v_cvt_pk_bf16_f32 v86, v48, v49\n"
      "v_mfma_f32_16x16x32_bf16 v[56:59], a[36:39], a[172:175], v[56:59]\n"
      "v_exp_f32 v51, v51\n"
      "v_mfma_f32_16x16x32_bf16 v[56:59], a[40:43], a[176:179], v[56:59]\n"
      "ds_read_b128 a[36:39], v4 offset:57344\n"
      "v_cvt_pk_bf16_f32 v87, v50, v51\n"
      "v_mfma_f32_16x16x32_bf16 v[56:59], a[44:47], a[180:183], v[56:59]\n"
      "v_exp_f32 v52, v52\n"
      "v_mfma_f32_16x16x32_bf16 v[56:59], a[48:51], a[184:187], v[56:59]\n"
      "ds_read_b128 a[40:43], v3 offset:59392\n"
      "v_add_f32 v8, v8, v43\n"
      "v_mfma_f32_16x16x32_bf16 v[56:59], a[52:55], a[188:191], v[56:59]\n"
      "s_add_u32 m0, s23, 71296\n"
      "v_exp_f32 v53, v53\n"
      "v_mfma_f32_16x16x32_bf16 v[56:59], a[56:59], a[192:195], v[56:59]\n"
      "buffer_load_dwordx4 v2, s[40:43], 0 offen offset:384 lds\n"
      "v_mfma_f32_16x16x32_bf16 v[56:59], a[60:63], a[196:199], v[56:59]\n"
      "s_waitcnt lgkmcnt(3)\n"
      "v_exp_f32 v54, v54\n"
      "v_mfma_f32_16x16x32_bf16 v[60:63], a[0:3], a[104:107], v[24:27]\n"
      "ds_read_b128 a[44:47], v4 offset:59392\n"
      "v_cvt_pk_bf16_f32 v94, v52, v53\n"
      "v_mfma_f32_16x16x32_bf16 v[60:63], a[4:7], a[108:111], v[60:63]\n"
      "v_exp_f32 v55, v55\n"
      "v_mfma_f32_16x16x32_bf16 v[60:63], a[8:11], a[112:115], v[60:63]\n"
      "ds_read_b128 a[48:51], v3 offset:61440\n"
      "v_cvt_pk_bf16_f32 v95, v54, v55\n"
      "v_mfma_f32_16x16x32_bf16 v[60:63], a[12:15], a[116:119], v[60:63]\n"
      "v_exp_f32 v56, v56\n"
      "v_mfma_f32_16x16x32_bf16 v[60:63], a[16:19], a[120:123], v[60:63]\n"
      "ds_read_b128 a[52:55], v4 offset:61440\n"
      "v_add_f32 v10, v10, v44\n"
      "v_mfma_f32_16x16x32_bf16 v[60:63], a[20:23], a[124:127], v[60:63]\n"
      "v_exp_f32 v57, v57\n"
      "v_mfma_f32_16x16x32_bf16 v[60:63], a[24:27], a[128:131], v[60:63]\n"
      "ds_read_b128 a[56:59], v3 offset:63488\n"
      "v_cvt_pk_bf16_f32 v102, v56, v57\n"
      "v_mfma_f32_16x16x32_bf16 v[60:63], a[28:31], a[132:135], v[60:63]\n"
      "v_exp_f32 v58, v58\n"
      "v_mfma_f32_16x16x32_bf16 v[64:67], a[0:3], a[136:139], v[28:31]\n"
      "ds_read_b128 a[60:63], v4 offset:63488\n"
      "v_add_f32 v10, v10, v45\n"
      "v_mfma_f32_16x16x32_bf16 v[64:67], a[4:7], a[140:143], v[64:67]\n"
      "s_add_u32 m0, s23, 72320\n"
      "v_exp_f32 v59, v59\n"
      "v_mfma_f32_16x16x32_bf16 v[64:67], a[8:11], a[144:147], v[64:67]\n"
      "buffer_load_dwordx4 v2, s[40:43], s52 offen offset:384 lds\n"
      "v_mfma_f32_16x16x32_bf16 v[64:67], a[12:15], a[148:151], v[64:67]\n"
      "v_exp_f32 v60, v60\n"
      "v_mfma_f32_16x16x32_bf16 v[64:67], a[16:19], a[152:155], v[64:67]\n"
      "v_cvt_pk_bf16_f32 v103, v58, v59\n"
      "v_add_u32 v2, 0x8000, v2\n"
      "v_mfma_f32_16x16x32_bf16 v[64:67], a[20:23], a[156:159], v[64:67]\n"
      "v_exp_f32 v61, v61\n"
      "v_mfma_f32_16x16x32_bf16 v[64:67], a[24:27], a[160:163], v[64:67]\n"
      "v_cvt_pk_bf16_f32 v88, v60, v61\n"
      "v_add_f32 v10, v10, v46\n"
      "v_mfma_f32_16x16x32_bf16 v[64:67], a[28:31], a[164:167], v[64:67]\n"
      "v_exp_f32 v62, v62\n"
      "v_mfma_f32_16x16x32_bf16 v[68:71], a[0:3], a[168:171], v[32:35]\n"
      "v_add_f32 v10, v10, v47\n"
      "v_add_f32 v6, v6, v48\n"
      "v_mfma_f32_16x16x32_bf16 v[68:71], a[4:7], a[172:175], v[68:71]\n"
      "v_exp_f32 v63, v63\n"
      "v_mfma_f32_16x16x32_bf16 v[68:71], a[8:11], a[176:179], v[68:71]\n"
      "s_waitcnt vmcnt(17) lgkmcnt(0)\n"
      "v_cvt_pk_bf16_f32 v89, v62, v63\n"
      "v_add_f32 v6, v6, v49\n"
      "v_mfma_f32_16x16x32_bf16 v[68:71], a[12:15], a[180:183], v[68:71]\n"
      "s_barrier\n"
      "v_exp_f32 v64, v64\n"
      "v_mfma_f32_16x16x32_bf16 v[68:71], a[16:19], a[184:187], v[68:71]\n"
      "ds_read_b64_tr_b16 a[64:65], v12 offset:32768\n"
      "v_add_f32 v6, v6, v50\n"
      "v_mfma_f32_16x16x32_bf16 v[68:71], a[20:23], a[188:191], v[68:71]\n"
      "v_exp_f32 v65, v65\n"
      "v_mfma_f32_16x16x32_bf16 v[68:71], a[24:27], a[192:195], v[68:71]\n"
      "ds_read_b64_tr_b16 a[66:67], v12 offset:40960\n"
      "v_cvt_pk_bf16_f32 v96, v64, v65\n"
      "v_mfma_f32_16x16x32_bf16 v[68:71], a[28:31], a[196:199], v[68:71]\n"
      "v_exp_f32 v66, v66\n"
      "v_mfma_f32_16x16x32_bf16 v[72:75], a[32:35], a[104:107], v[24:27]\n"
      "ds_read_b64_tr_b16 a[68:69], v12 offset:49152\n"
      "v_add_f32 v6, v6, v51\n"
      "v_mfma_f32_16x16x32_bf16 v[72:75], a[36:39], a[108:111], v[72:75]\n"
      "v_exp_f32 v67, v67\n"
      "v_mfma_f32_16x16x32_bf16 v[72:75], a[40:43], a[112:115], v[72:75]\n"
      "ds_read_b64_tr_b16 a[70:71], v12 offset:57344\n"
      "v_cvt_pk_bf16_f32 v97, v66, v67\n"
      "v_mfma_f32_16x16x32_bf16 v[72:75], a[44:47], a[116:119], v[72:75]\n"
      "v_exp_f32 v68, v68\n"
      "v_mfma_f32_16x16x32_bf16 v[72:75], a[48:51], a[120:123], v[72:75]\n"
      "ds_read_b64_tr_b16 a[72:73], v13 offset:32768\n"
      "v_add_f32 v8, v8, v52\n"
      "v_mfma_f32_16x16x32_bf16 v[72:75], a[52:55], a[124:127], v[72:75]\n"
      "v_exp_f32 v69, v69\n"
      "v_mfma_f32_16x16x32_bf16 v[72:75], a[56:59], a[128:131], v[72:75]\n"
      "ds_read_b64_tr_b16 a[74:75], v13 offset:40960\n"
      "v_cvt_pk_bf16_f32 v104, v68, v69\n"
      "v_mfma_f32_16x16x32_bf16 v[72:75], a[60:63], a[132:135], v[72:75]\n"
      "v_exp_f32 v70, v70\n"
      "v_mfma_f32_16x16x32_bf16 v[76:79], a[32:35], a[136:139], v[28:31]\n"
      "ds_read_b64_tr_b16 a[76:77], v13 offset:49152\n"
      "v_add_f32 v8, v8, v53\n"
      "v_mfma_f32_16x16x32_bf16 v[76:79], a[36:39], a[140:143], v[76:79]\n"
      "v_exp_f32 v71, v71\n"
      "v_mfma_f32_16x16x32_bf16 v[76:79], a[40:43], a[144:147], v[76:79]\n"
      "ds_read_b64_tr_b16 a[78:79], v13 offset:57344\n"
      "v_cvt_pk_bf16_f32 v105, v70, v71\n"
      "v_mfma_f32_16x16x32_bf16 v[76:79], a[44:47], a[148:151], v[76:79]\n"
      "v_exp_f32 v72, v72\n"
      "v_mfma_f32_16x16x32_bf16 v[76:79], a[48:51], a[152:155], v[76:79]\n"
      "ds_read_b64_tr_b16 a[80:81], v14 offset:32768\n"
      "v_add_f32 v8, v8, v54\n"
      "v_mfma_f32_16x16x32_bf16 v[76:79], a[52:55], a[156:159], v[76:79]\n"
      "v_exp_f32 v73, v73\n"
      "v_mfma_f32_16x16x32_bf16 v[76:79], a[56:59], a[160:163], v[76:79]\n"
      "ds_read_b64_tr_b16 a[82:83], v14 offset:40960\n"
      "v_cvt_pk_bf16_f32 v90, v72, v73\n"
      "v_mfma_f32_16x16x32_bf16 v[76:79], a[60:63], a[164:167], v[76:79]\n"
      "v_exp_f32 v74, v74\n"
      "v_mfma_f32_16x16x32_bf16 v[80:83], a[32:35], a[168:171], v[32:35]\n"
      "ds_read_b64_tr_b16 a[84:85], v14 offset:49152\n"
      "v_add_f32 v8, v8, v55\n"
      "v_mfma_f32_16x16x32_bf16 v[80:83], a[36:39], a[172:175], v[80:83]\n"
      "v_exp_f32 v75, v75\n"
      "v_mfma_f32_16x16x32_bf16 v[80:83], a[40:43], a[176:179], v[80:83]\n"
      "ds_read_b64_tr_b16 a[86:87], v14 offset:57344\n"
      "v_cvt_pk_bf16_f32 v91, v74, v75\n"
      "v_mfma_f32_16x16x32_bf16 v[80:83], a[44:47], a[180:183], v[80:83]\n"
      "v_exp_f32 v76, v76\n"
      "v_mfma_f32_16x16x32_bf16 v[80:83], a[48:51], a[184:187], v[80:83]\n"
      "v_mfma_f32_16x16x32_bf16 v[80:83], a[52:55], a[188:191], v[80:83]\n"
      "v_exp_f32 v77, v77\n"
      "v_mfma_f32_16x16x32_bf16 v[80:83], a[56:59], a[192:195], v[80:83]\n"
      "s_sub_u32 s20, s20, 1\n"
      "v_cvt_pk_bf16_f32 v98, v76, v77\n"
      "v_mfma_f32_16x16x32_bf16 v[80:83], a[60:63], a[196:199], v[80:83]\n"
      "s_waitcnt lgkmcnt(8)\n"
      "v_exp_f32 v78, v78\n"
      "s_cbranch_scc1 L_tail_%=\n"
      "v_mfma_f32_16x16x32_bf16 a[200:203], v[84:87], a[64:67], a[200:203]\n"
      "s_add_u32 m0, s23, 32768\n"
      "ds_read_b64_tr_b16 a[88:89], v15 offset:32768\n"
      "v_add_f32 v10, v10, v56\n"
      "v_mfma_f32_16x16x32_bf16 a[200:203], v[88:91], a[68:71], a[200:203]\n"
      "s_waitcnt lgkmcnt(5)\n"
      "v_exp_f32 v79, v79\n"
      "v_mfma_f32_16x16x32_bf16 a[212:215], v[84:87], a[72:75], a[212:215]\n"
      "buffer_load_dwordx4 v1, s[36:39], 0 offen offset:0 lds\n"
      "v_mfma_f32_16x16x32_bf16 a[212:215], v[88:91], a[76:79], a[212:215]\n"
      "s_waitcnt lgkmcnt(1)\n"
      "v_exp_f32 v80, v80\n"
      "v_mfma_f32_16x16x32_bf16 a[224:227], v[84:87], a[80:83], a[224:227]\n"
      "ds_read_b64_tr_b16 a[90:91], v15 offset:40960\n"
      "v_cvt_pk_bf16_f32 v99, v78, v79\n"
      "v_mfma_f32_16x16x32_bf16 a[224:227], v[88:91], a[84:87], a[224:227]\n"
      "v_exp_f32 v81, v81\n"
      "v_mfma_f32_16x16x32_bf16 a[204:207], v[92:95], a[64:67], a[204:207]\n"
      "ds_read_b64_tr_b16 a[92:93], v15 offset:49152\n"
      "v_cvt_pk_bf16_f32 v106, v80, v81\n"
      "v_mfma_f32_16x16x32_bf16 a[204:207], v[96:99], a[68:71], a[204:207]\n"
      "v_exp_f32 v82, v82\n"
      "v_mfma_f32_16x16x32_bf16 a[216:219], v[92:95], a[72:75], a[216:219]\n"
      "ds_read_b64_tr_b16 a[94:95], v15 offset:57344\n"
      "v_add_f32 v10, v10, v57\n"
      "v_mfma_f32_16x16x32_bf16 a[216:219], v[96:99], a[76:79], a[216:219]\n"
      "s_add_u32 m0, s23, 33792\n"
      "v_exp_f32 v83, v83\n"
      "v_mfma_f32_16x16x32_bf16 a[228:231], v[92:95], a[80:83], a[228:231]\n"
      "buffer_load_dwordx4 v1, s[36:39], s52 offen offset:0 lds\n"
      "v_mfma_f32_16x16x32_bf16 a[228:231], v[96:99], a[84:87], a[228:231]\n"
      "ds_read_b64_tr_b16 a[96:97], v12 offset:34816\n"
      "v_cvt_pk_bf16_f32 v107, v82, v83\n"
      "v_mfma_f32_16x16x32_bf16 a[208:211], v[100:103], a[64:67], a[208:211]\n"
      "ds_read_b64_tr_b16 a[98:99], v12 offset:43008\n"
      "v_add_f32 v10, v10, v58\n"
      "v_mfma_f32_16x16x32_bf16 a[208:211], v[104:107], a[68:71], a[208:211]\n"
      "ds_read_b64_tr_b16 a[100:101], v12 offset:51200\n"
      "v_add_f32 v10, v10, v59\n"
      "v_mfma_f32_16x16x32_bf16 a[220:223], v[100:103], a[72:75], a[220:223]\n"
      "ds_read_b64_tr_b16 a[102:103], v12 offset:59392\n"
      "v_add_f32 v6, v6, v60\n"
      "v_mfma_f32_16x16x32_bf16 a[220:223], v[104:107], a[76:79], a[220:223]\n"
      "ds_read_b64_tr_b16 a[64:65], v13 offset:34816\n"
      "v_add_f32 v6, v6, v61\n"
      "v_mfma_f32_16x16x32_bf16 a[232:235], v[100:103], a[80:83], a[232:235]\n"
      "s_add_u32 m0, s23, 34688\n"
      "ds_read_b64_tr_b16 a[66:67], v13 offset:43008\n"
      "v_add_f32 v6, v6, v62\n"
      "v_mfma_f32_16x16x32_bf16 a[232:235], v[104:107], a[84:87], a[232:235]\n"
      "s_waitcnt lgkmcnt(6)\n"
      "ds_read_b64_tr_b16 a[68:69], v13 offset:51200\n"
      "v_add_f32 v6, v6, v63\n"
      "v_mfma_f32_16x16x32_bf16 a[236:239], v[84:87], a[88:91], a[236:239]\n"
      "buffer_load_dwordx4 v1, s[36:39], 0 offen offset:128 lds\n"
      "v_mfma_f32_16x16x32_bf16 a[236:239], v[88:91], a[92:95], a[236:239]\n"
      "ds_read_b64_tr_b16 a[70:71], v13 offset:59392\n"
      "v_add_f32 v8, v8, v64\n"
      "v_mfma_f32_16x16x32_bf16 a[240:243], v[92:95], a[88:91], a[240:243]\n"
      "ds_read_b64_tr_b16 a[72:73], v14 offset:34816\n"
      "v_add_f32 v8, v8, v65\n"
      "v_mfma_f32_16x16x32_bf16 a[240:243], v[96:99], a[92:95], a[240:243]\n"
      "ds_read_b64_tr_b16 a[74:75], v14 offset:43008\n"
      "v_add_f32 v8, v8, v66\n"
      "v_mfma_f32_16x16x32_bf16 v[108:111], v[100:103], a[88:91], v[108:111]\n"
      "ds_read_b64_tr_b16 a[76:77], v14 offset:51200\n"
      "v_add_f32 v8, v8, v67\n"
      "v_mfma_f32_16x16x32_bf16 v[108:111], v[104:107], a[92:95], v[108:111]\n"
      "s_waitcnt lgkmcnt(7)\n"
      "ds_read_b64_tr_b16 a[78:79], v14 offset:59392\n"
      "v_add_f32 v10, v10, v68\n"
      "v_mfma_f32_16x16x32_bf16 v[112:115], v[84:87], a[96:99], v[112:115]\n"
      "ds_read_b64_tr_b16 a[80:81], v15 offset:34816\n"
      "v_add_f32 v10, v10, v69\n"
      "v_mfma_f32_16x16x32_bf16 v[112:115], v[88:91], a[100:103], v[112:115]\n"
      "ds_read_b64_tr_b16 a[82:83], v15 offset:43008\n"
      "v_add_f32 v10, v10, v70\n"
      "v_mfma_f32_16x16x32_bf16 v[116:119], v[92:95], a[96:99], v[116:119]\n"
      "ds_read_b64_tr_b16 a[84:85], v15 offset:51200\n"
      "v_add_f32 v10, v10, v71\n"
      "v_mfma_f32_16x16x32_bf16 v[116:119], v[96:99], a[100:103], v[116:119]\n"
      "ds_read_b64_tr_b16 a[86:87], v15 offset:59392\n"
      "v_add_f32 v6, v6, v72\n"
      "v_mfma_f32_16x16x32_bf16 v[120:123], v[100:103], a[96:99], v[120:123]\n"
      "s_add_u32 m0, s23, 35712\n"
      "ds_read_b64_tr_b16 a[88:89], v12 offset:36864\n"
      "v_add_f32 v6, v6, v73\n"
      "v_mfma_f32_16x16x32_bf16 v[120:123], v[104:107], a[100:103], v[120:123]\n"
      "s_waitcnt lgkmcnt(9)\n"
      "ds_read_b64_tr_b16 a[90:91], v12 offset:45056\n"
      "v_add_f32 v6, v6, v74\n"
      "v_mfma_f32_16x16x32_bf16 v[124:127], v[84:87], a[64:67], v[124:127]\n"
      "buffer_load_dwordx4 v1, s[36:39], s52 offen offset:128 lds\n"
      "v_mfma_f32_16x16x32_bf16 v[124:127], v[88:91], a[68:71], v[124:127]\n"
      "ds_read_b64_tr_b16 a[92:93], v12 offset:53248\n"
      "v_add_f32 v6, v6, v75\n"
      "v_mfma_f32_16x16x32_bf16 v[128:131], v[92:95], a[64:67], v[128:131]\n"
      "ds_read_b64_tr_b16 a[94:95], v12 offset:61440\n"
      "v_add_f32 v8, v8, v76\n"
      "v_mfma_f32_16x16x32_bf16 v[128:131], v[96:99], a[68:71], v[128:131]\n"
      "ds_read_b64_tr_b16 a[96:97], v13 offset:36864\n"
      "v_add_f32 v8, v8, v77\n"
      "v_mfma_f32_16x16x32_bf16 v[132:135], v[100:103], a[64:67], v[132:135]\n"
      "ds_read_b64_tr_b16 a[98:99], v13 offset:45056\n"
      "v_add_f32 v8, v8, v78\n"
      "v_mfma_f32_16x16x32_bf16 v[132:135], v[104:107], a[68:71], v[132:135]\n"
      "s_waitcnt lgkmcnt(10)\n"
      "ds_read_b64_tr_b16 a[100:101], v13 offset:53248\n"
      "v_add_f32 v8, v8, v79\n"
      "v_mfma_f32_16x16x32_bf16 v[136:139], v[84:87], a[72:75], v[136:139]\n"
      "ds_read_b64_tr_b16 a[102:103], v13 offset:61440\n"
      "v_add_f32 v10, v10, v80\n"
      "v_mfma_f32_16x16x32_bf16 v[136:139], v[88:91], a[76:79], v[136:139]\n"
      "s_add_u32 m0, s23, 36608\n"
      "ds_read_b64_tr_b16 a[64:65], v14 offset:36864\n"
      "v_add_f32 v10, v10, v81\n"
      "v_mfma_f32_16x16x32_bf16 v[140:143], v[92:95], a[72:75], v[140:143]\n"
      "buffer_load_dwordx4 v1, s[36:39], 0 offen offset:256 lds\n"
      "v_mfma_f32_16x16x32_bf16 v[140:143], v[96:99], a[76:79], v[140:143]\n"
      "ds_read_b64_tr_b16 a[66:67], v14 offset:45056\n"
      "v_add_f32 v10, v10, v82\n"
      "v_mfma_f32_16x16x32_bf16 v[144:147], v[100:103], a[72:75], v[144:147]\n"
      "ds_read_b64_tr_b16 a[68:69], v14 offset:53248\n"
      "v_add_f32 v10, v10, v83\n"
      "v_mfma_f32_16x16x32_bf16 v[144:147], v[104:107], a[76:79], v[144:147]\n"
      "s_waitcnt lgkmcnt(11)\n"
      "ds_read_b64_tr_b16 a[70:71], v14 offset:61440\n"
      "v_mfma_f32_16x16x32_bf16 v[148:151], v[84:87], a[80:83], v[148:151]\n"
      "ds_read_b64_tr_b16 a[72:73], v15 offset:36864\n"
      "v_mfma_f32_16x16x32_bf16 v[148:151], v[88:91], a[84:87], v[148:151]\n"
      "ds_read_b64_tr_b16 a[74:75], v15 offset:45056\n"
      "v_mfma_f32_16x16x32_bf16 v[152:155], v[92:95], a[80:83], v[152:155]\n"
      "ds_read_b64_tr_b16 a[76:77], v15 offset:53248\n"
      "v_mfma_f32_16x16x32_bf16 v[152:155], v[96:99], a[84:87], v[152:155]\n"
      "s_add_u32 m0, s23, 37632\n"
      "ds_read_b64_tr_b16 a[78:79], v15 offset:61440\n"
      "v_mfma_f32_16x16x32_bf16 v[156:159], v[100:103], a[80:83], v[156:159]\n"
      "buffer_load_dwordx4 v1, s[36:39], s52 offen offset:256 lds\n"
      "v_mfma_f32_16x16x32_bf16 v[156:159], v[104:107], a[84:87], v[156:159]\n"
      "s_waitcnt lgkmcnt(12)\n"
      "ds_read_b64_tr_b16 a[80:81], v12 offset:38912\n"
      "v_mfma_f32_16x16x32_bf16 v[160:163], v[84:87], a[88:91], v[160:163]\n"
      "ds_read_b64_tr_b16 a[82:83], v12 offset:47104\n"
      "v_mfma_f32_16x16x32_bf16 v[160:163], v[88:91], a[92:95], v[160:163]\n"
      "ds_read_b64_tr_b16 a[84:85], v12 offset:55296\n"
      "v_mfma_f32_16x16x32_bf16 v[164:167], v[92:95], a[88:91], v[164:167]\n"
      "ds_read_b64_tr_b16 a[86:87], v12 offset:63488\n"
      "v_mfma_f32_16x16x32_bf16 v[164:167], v[96:99], a[92:95], v[164:167]\n"
      "v_mfma_f32_16x16x32_bf16 v[168:171], v[100:103], a[88:91], v[168:171]\n"
      "s_add_u32 m0, s23, 38528\n"
      "v_mfma_f32_16x16x32_bf16 v[168:171], v[104:107], a[92:95], v[168:171]\n"
      "s_waitcnt lgkmcnt(12)\n"
      "ds_read_b64_tr_b16 a[88:89], v13 offset:38912\n"
      "v_mfma_f32_16x16x32_bf16 v[172:175], v[84:87], a[96:99], v[172:175]\n"
      "buffer_load_dwordx4 v1, s[36:39], 0 offen offset:384 lds\n"
      "v_mfma_f32_16x16x32_bf16 v[172:175], v[88:91], a[100:103], v[172:175]\n"
      "ds_read_b64_tr_b16 a[90:91], v13 offset:47104\n"
      "v_mfma_f32_16x16x32_bf16 v[176:179], v[92:95], a[96:99], v[176:179]\n"
      "ds_read_b64_tr_b16 a[92:93], v13 offset:55296\n"
      "v_mfma_f32_16x16x32_bf16 v[176:179], v[96:99], a[100:103], v[176:179]\n"
      "ds_read_b64_tr_b16 a[94:95], v13 offset:63488\n"
      "v_mfma_f32_16x16x32_bf16 v[180:183], v[100:103], a[96:99], v[180:183]\n"
      "v_mfma_f32_16x16x32_bf16 v[180:183], v[104:107], a[100:103], v[180:183]\n"
      "s_waitcnt lgkmcnt(12)\n"
      "ds_read_b64_tr_b16 a[96:97], v14 offset:38912\n"
      "v_mfma_f32_16x16x32_bf16 v[184:187], v[84:87], a[64:67], v[184:187]\n"
      "ds_read_b64_tr_b16 a[98:99], v14 offset:47104\n"
      "v_mfma_f32_16x16x32_bf16 v[184:187], v[88:91], a[68:71], v[184:187]\n"
      "ds_read_b64_tr_b16 a[100:101], v14 offset:55296\n"
      "v_mfma_f32_16x16x32_bf16 v[188:191], v[92:95], a[64:67], v[188:191]\n"
      "ds_read_b64_tr_b16 a[102:103], v14 offset:63488\n"
      "v_mfma_f32_16x16x32_bf16 v[188:191], v[96:99], a[68:71], v[188:191]\n"
      "v_mfma_f32_16x16x32_bf16 v[192:195], v[100:103], a[64:67], v[192:195]\n"
      "s_add_u32 m0, s23, 39552\n"
      "v_mfma_f32_16x16x32_bf16 v[192:195], v[104:107], a[68:71], v[192:195]\n"
      "s_waitcnt lgkmcnt(12)\n"
      "ds_read_b64_tr_b16 a[64:65], v15 offset:38912\n"
      "v_mfma_f32_16x16x32_bf16 v[196:199], v[84:87], a[72:75], v[196:199]\n"
      "buffer_load_dwordx4 v1, s[36:39], s52 offen offset:384 lds\n"
      "v_mfma_f32_16x16x32_bf16 v[196:199], v[88:91], a[76:79], v[196:199]\n"
      "ds_read_b64_tr_b16 a[66:67], v15 offset:47104\n"
      "v_add_u32 v1, 0x8000, v1\n"
      "v_mfma_f32_16x16x32_bf16 v[200:203], v[92:95], a[72:75], v[200:203]\n"
      "ds_read_b64_tr_b16 a[68:69], v15 offset:55296\n"
      "v_mfma_f32_16x16x32_bf16 v[200:203], v[96:99], a[76:79], v[200:203]\n"
      "ds_read_b64_tr_b16 a[70:71], v15 offset:63488\n"
      "v_mfma_f32_16x16x32_bf16 v[204:207], v[100:103], a[72:75], v[204:207]\n"
      "buffer_load_dword v23, v22, s[60:63], 0 offen\n"
      "v_mfma_f32_16x16x32_bf16 v[204:207], v[104:107], a[76:79], v[204:207]\n"
      "s_waitcnt lgkmcnt(12)\n"
      "v_add_u32 v22, 0x400, v22\n"
      "v_mfma_f32_16x16x32_bf16 v[208:211], v[84:87], a[80:83], v[208:211]\n"
      "v_mfma_f32_16x16x32_bf16 v[208:211], v[88:91], a[84:87], v[208:211]\n"
      "s_getreg_b32 s29, hwreg(HW_REG_IB_STS, 0, 4)\n"
      "v_mfma_f32_16x16x32_bf16 v[212:215], v[92:95], a[80:83], v[212:215]\n"
      "s_cmp_ge_u32 s29, 10\n"
      "v_mfma_f32_16x16x32_bf16 v[212:215], v[96:99], a[84:87], v[212:215]\n"
      "s_cbranch_scc1 L_topN2_%=\n"
      "s_branch L_topD2_%=\n"
      ".p2align 6\n"
      "L_topN2_%=:\n"
      "v_mfma_f32_16x16x32_bf16 v[216:219], v[100:103], a[80:83], v[216:219]\n"
      "v_mfma_f32_16x16x32_bf16 v[216:219], v[104:107], a[84:87], v[216:219]\n"
      "s_waitcnt lgkmcnt(8)\n"
      "v_mfma_f32_16x16x32_bf16 v[220:223], v[84:87], a[88:91], v[220:223]\n"
      "s_waitcnt vmcnt(17) lgkmcnt(0)\n"
      "v_mfma_f32_16x16x32_bf16 v[220:223], v[88:91], a[92:95], v[220:223]\n"
      "s_barrier\n"
      "ds_read_b128 a[0:3], v3 offset:0\n"
      "v_mfma_f32_16x16x32_bf16 v[224:227], v[92:95], a[88:91], v[224:227]\n"
      "ds_read_b128 a[4:7], v4 offset:0\n"
      "v_mfma_f32_16x16x32_bf16 v[224:227], v[96:99], a[92:95], v[224:227]\n"
      "s_add_u32 m0, s23, 98304\n"
      "ds_read_b128 a[8:11], v3 offset:2048\n"
      "v_mfma_f32_16x16x32_bf16 v[228:231], v[100:103], a[88:91], v[228:231]\n"
      "buffer_load_dwordx4 v2, s[40:43], 0 offen offset:0 nt lds\n"
      "v_mfma_f32_16x16x32_bf16 v[228:231], v[104:107], a[92:95], v[228:231]\n"
      "ds_read_b128 a[12:15], v4 offset:2048\n"
      "v_mfma_f32_16x16x32_bf16 v[232:235], v[84:87], a[96:99], v[232:235]\n"
      "ds_read_b128 a[16:19], v3 offset:4096\n"
      "v_mfma_f32_16x16x32_bf16 v[232:235], v[88:91], a[100:103], v[232:235]\n"
      "ds_read_b128 a[20:23], v4 offset:4096\n"
      "v_mfma_f32_16x16x32_bf16 v[236:239], v[92:95], a[96:99], v[236:239]\n"
      "ds_read_b128 a[24:27], v3 offset:6144\n"
      "v_mfma_f32_16x16x32_bf16 v[236:239], v[96:99], a[100:103], v[236:239]\n"
      "ds_read_b128 a[28:31], v4 offset:6144\n"
      "v_mfma_f32_16x16x32_bf16 v[240:243], v[100:103], a[96:99], v[240:243]\n"
      "ds_read_b128 a[32:35], v3 offset:8192\n"
      "v_mfma_f32_16x16x32_bf16 v[240:243], v[104:107], a[100:103], v[240:243]\n"
      "ds_read_b128 a[36:39], v4 offset:8192\n"
      "v_mfma_f32_16x16x32_bf16 v[244:247], v[84:87], a[64:67], v[244:247]\n"
      "ds_read_b128 a[40:43], v3 offset:10240\n"
      "v_mfma_f32_16x16x32_bf16 v[244:247], v[88:91], a[68:71], v[244:247]\n"
      "ds_read_b128 a[44:47], v4 offset:10240\n"
      "v_mfma_f32_16x16x32_bf16 v[248:251], v[92:95], a[64:67], v[248:251]\n"
      "ds_read_b128 a[48:51], v3 offset:12288\n"
      "v_mfma_f32_16x16x32_bf16 v[248:251], v[96:99], a[68:71], v[248:251]\n"
      "s_add_u32 m0, s23, 99328\n"
      "ds_read_b128 a[52:55], v4 offset:12288\n"
      "v_mfma_f32_16x16x32_bf16 v[252:255], v[100:103], a[64:67], v[252:255]\n"
      "buffer_load_dwordx4 v2, s[40:43], s52 offen offset:0 nt lds\n"
      "v_mfma_f32_16x16x32_bf16 v[252:255], v[104:107], a[68:71], v[252:255]\n"
      "s_waitcnt lgkmcnt(6)\n"
      "ds_read_b128 a[56:59], v3 offset:14336\n"
      "v_mfma_f32_16x16x32_bf16 v[36:39], a[0:3], a[104:107], v[24:27]\n"
      "ds_read_b128 a[60:63], v4 offset:14336\n"
      "v_mfma_f32_16x16x32_bf16 v[36:39], a[4:7], a[108:111], v[36:39]\n"
      "v_mfma_f32_16x16x32_bf16 v[36:39], a[8:11], a[112:115], v[36:39]\n"
      "v_mfma_f32_16x16x32_bf16 v[36:39], a[12:15], a[116:119], v[36:39]\n"
      "v_mfma_f32_16x16x32_bf16 v[36:39], a[16:19], a[120:123], v[36:39]\n"
      "v_mfma_f32_16x16x32_bf16 v[36:39], a[20:23], a[124:127], v[36:39]\n"
      "s_add_u32 m0, s23, 100224\n"
      "v_mfma_f32_16x16x32_bf16 v[36:39], a[24:27], a[128:131], v[36:39]\n"
      "buffer_load_dwordx4 v2, s[40:43], 0 offen offset:128 nt lds\n"
      "v_mfma_f32_16x16x32_bf16 v[36:39], a[28:31], a[132:135], v[36:39]\n"
      "v_mfma_f32_16x16x32_bf16 v[40:43], a[0:3], a[136:139], v[28:31]\n"
      "v_mfma_f32_16x16x32_bf16 v[40:43], a[4:7], a[140:143], v[40:43]\n"
      "v_mfma_f32_16x16x32_bf16 v[40:43], a[8:11], a[144:147], v[40:43]\n"
      "v_mfma_f32_16x16x32_bf16 v[40:43], a[12:15], a[148:151], v[40:43]\n"
      "v_exp_f32 v36, v36\n"
      "v_mfma_f32_16x16x32_bf16 v[40:43], a[16:19], a[152:155], v[40:43]\n"
      "v_add_f32 v6, v6, v36\n"
      "v_mfma_f32_16x16x32_bf16 v[40:43], a[20:23], a[156:159], v[40:43]\n"
      "v_exp_f32 v37, v37\n"
      "v_mfma_f32_16x16x32_bf16 v[40:43], a[24:27], a[160:163], v[40:43]\n"
      "v_cvt_pk_bf16_f32 v84, v36, v37\n"
      "v_add_f32 v6, v6, v37\n"
      "v_mfma_f32_16x16x32_bf16 v[40:43], a[28:31], a[164:167], v[40:43]\n"
      "v_exp_f32 v38, v38\n"
      "v_mfma_f32_16x16x32_bf16 v[44:47], a[0:3], a[168:171], v[32:35]\n"
      "ds_read_b128 a[0:3], v3 offset:16384\n"
      "v_add_f32 v6, v6, v38\n"
      "v_mfma_f32_16x16x32_bf16 v[44:47], a[4:7], a[172:175], v[44:47]\n"
      "s_add_u32 m0, s23, 101248\n"
      "v_exp_f32 v39, v39\n"
      "v_mfma_f32_16x16x32_bf16 v[44:47], a[8:11], a[176:179], v[44:47]\n"
      "buffer_load_dwordx4 v2, s[40:43], s52 offen offset:128 nt lds\n"
      "v_mfma_f32_16x16x32_bf16 v[44:47], a[12:15], a[180:183], v[44:47]\n"
      "v_exp_f32 v40, v40\n"
      "v_mfma_f32_16x16x32_bf16 v[44:47], a[16:19], a[184:187], v[44:47]\n"
      "ds_read_b128 a[4:7], v4 offset:16384\n"
      "v_cvt_pk_bf16_f32 v85, v38, v39\n"
      "v_mfma_f32_16x16x32_bf16 v[44:47], a[20:23], a[188:191], v[44:47]\n"
      "v_exp_f32 v41, v41\n"
      "v_mfma_f32_16x16x32_bf16 v[44:47], a[24:27], a[192:195], v[44:47]\n"
      "ds_read_b128 a[8:11], v3 offset:18432\n"
      "v_cvt_pk_bf16_f32 v92, v40, v41\n"
      "v_mfma_f32_16x16x32_bf16 v[44:47], a[28:31], a[196:199], v[44:47]\n"
      "s_waitcnt lgkmcnt(3)\n"
      "v_exp_f32 v42, v42\n"
      "v_mfma_f32_16x16x32_bf16 v[48:51], a[32:35], a[104:107], v[24:27]\n"
      "ds_read_b128 a[12:15], v4 offset:18432\n"
      "v_add_f32 v6, v6, v39\n"
      "v_mfma_f32_16x16x32_bf16 v[48:51], a[36:39], a[108:111], v[48:51]\n"
      "s_add_u32 m0, s23, 102144\n"
      "v_exp_f32 v43, v43\n"
      "v_mfma_f32_16x16x32_bf16 v[48:51], a[40:43], a[112:115], v[48:51]\n"
      "buffer_load_dwordx4 v2, s[40:43], 0 offen offset:256 nt lds\n"
      "v_mfma_f32_16x16x32_bf16 v[48:51], a[44:47], a[116:119], v[48:51]\n"
      "v_exp_f32 v44, v44\n"
      "v_mfma_f32_16x16x32_bf16 v[48:51], a[48:51], a[120:123], v[48:51]\n"
      "ds_read_b128 a[16:19], v3 offset:20480\n"
      "v_cvt_pk_bf16_f32 v93, v42, v43\n"
      "v_mfma_f32_16x16x32_bf16 v[48:51], a[52:55], a[124:127], v[48:51]\n"
      "v_exp_f32 v45, v45\n"
      "v_mfma_f32_16x16x32_bf16 v[48:51], a[56:59], a[128:131], v[48:51]\n"
      "ds_read_b128 a[20:23], v4 offset:20480\n"
      "v_cvt_pk_bf16_f32 v100, v44, v45\n"
      "v_mfma_f32_16x16x32_bf16 v[48:51], a[60:63], a[132:135], v[48:51]\n"
      "v_exp_f32 v46, v46\n"
      "v_mfma_f32_16x16x32_bf16 v[52:55], a[32:35], a[136:139], v[28:31]\n"
      "ds_read_b128 a[24:27], v3 offset:22528\n"
      "v_add_f32 v8, v8, v40\n"
      "v_mfma_f32_16x16x32_bf16 v[52:55], a[36:39], a[140:143], v[52:55]\n"
      "v_exp_f32 v47, v47\n"
      "v_mfma_f32_16x16x32_bf16 v[52:55], a[40:43], a[144:147], v[52:55]\n"
      "ds_read_b128 a[28:31], v4 offset:22528\n"
      "v_cvt_pk_bf16_f32 v101, v46, v47\n"
      "v_mfma_f32_16x16x32_bf16 v[52:55], a[44:47], a[148:151], v[52:55]\n"
      "v_exp_f32 v48, v48\n"
      "v_mfma_f32_16x16x32_bf16 v[52:55], a[48:51], a[152:155], v[52:55]\n"
      "v_add_f32 v8, v8, v41\n"
      "v_add_f32 v8, v8, v42\n"
      "v_mfma_f32_16x16x32_bf16 v[52:55], a[52:55], a[156:159], v[52:55]\n"
      "s_add_u32 m0, s23, 103168\n"
      "v_exp_f32 v49, v49\n"
      "v_mfma_f32_16x16x32_bf16 v[52:55], a[56:59], a[160:163], v[52:55]\n"
      "buffer_load_dwordx4 v2, s[40:43], s52 offen offset:256 nt lds\n"
      "v_mfma_f32_16x16x32_bf16 v[52:55], a[60:63], a[164:167], v[52:55]\n"
      "v_exp_f32 v50, v50\n"
      "v_mfma_f32_16x16x32_bf16 v[56:59], a[32:35], a[168:171], v[32:35]\n"
      "ds_read_b128 a[32:35], v3 offset:24576\n"
      "v_cvt_pk_bf16_f32 v86, v48, v49\n"
      "v_mfma_f32_16x16x32_bf16 v[56:59], a[36:39], a[172:175], v[56:59]\n"
      "v_exp_f32 v51, v51\n"
      "v_mfma_f32_16x16x32_bf16 v[56:59], a[40:43], a[176:179], v[56:59]\n"
      "ds_read_b128 a[36:39], v4 offset:24576\n"
      "v_cvt_pk_bf16_f32 v87, v50, v51\n"
      "v_mfma_f32_16x16x32_bf16 v[56:59], a[44:47], a[180:183], v[56:59]\n"
      "v_exp_f32 v52, v52\n"
      "v_mfma_f32_16x16x32_bf16 v[56:59], a[48:51], a[184:187], v[56:59]\n"
      "ds_read_b128 a[40:43], v3 offset:26624\n"
      "v_add_f32 v8, v8, v43\n"
      "v_mfma_f32_16x16x32_bf16 v[56:59], a[52:55], a[188:191], v[56:59]\n"
      "s_add_u32 m0, s23, 104064\n"
      "v_exp_f32 v53, v53\n"
      "v_mfma_f32_16x16x32_bf16 v[56:59], a[56:59], a[192:195], v[56:59]\n"
      "buffer_load_dwordx4 v2, s[40:43], 0 offen offset:384 nt lds\n"
      "v_mfma_f32_16x16x32_bf16 v[56:59], a[60:63], a[196:199], v[56:59]\n"
      "s_waitcnt lgkmcnt(3)\n"
      "v_exp_f32 v54, v54\n"
      "v_mfma_f32_16x16x32_bf16 v[60:63], a[0:3], a[104:107], v[24:27]\n"
      "ds_read_b128 a[44:47], v4 offset:26624\n"
      "v_cvt_pk_bf16_f32 v94, v52, v53\n"
      "v_mfma_f32_16x16x32_bf16 v[60:63], a[4:7], a[108:111], v[60:63]\n"
      "v_exp_f32 v55, v55\n"
      "v_mfma_f32_16x16x32_bf16 v[60:63], a[8:11], a[112:115], v[60:63]\n"
      "ds_read_b128 a[48:51], v3 offset:28672\n"
      "v_cvt_pk_bf16_f32 v95, v54, v55\n"
      "v_mfma_f32_16x16x32_bf16 v[60:63], a[12:15], a[116:119], v[60:63]\n"
      "v_exp_f32 v56, v56\n"
      "v_mfma_f32_16x16x32_bf16 v[60:63], a[16:19], a[120:123], v[60:63]\n"
      "ds_read_b128 a[52:55], v4 offset:28672\n"
      "v_add_f32 v10, v10, v44\n"
      "v_mfma_f32_16x16x32_bf16 v[60:63], a[20:23], a[124:127], v[60:63]\n"
      "v_exp_f32 v57, v57\n"
      "v_mfma_f32_16x16x32_bf16 v[60:63], a[24:27], a[128:131], v[60:63]\n"
      "ds_read_b128 a[56:59], v3 offset:30720\n"
      "v_cvt_pk_bf16_f32 v102, v56, v57\n"
      "v_mfma_f32_16x16x32_bf16 v[60:63], a[28:31], a[132:135], v[60:63]\n"
      "v_exp_f32 v58, v58\n"
      "v_mfma_f32_16x16x32_bf16 v[64:67], a[0:3], a[136:139], v[28:31]\n"
      "ds_read_b128 a[60:63], v4 offset:30720\n"
      "v_add_f32 v10, v10, v45\n"
      "v_mfma_f32_16x16x32_bf16 v[64:67], a[4:7], a[140:143], v[64:67]\n"
      "s_add_u32 m0, s23, 105088\n"
      "v_exp_f32 v59, v59\n"
      "v_mfma_f32_16x16x32_bf16 v[64:67], a[8:11], a[144:147], v[64:67]\n"
      "buffer_load_dwordx4 v2, s[40:43], s52 offen offset:384 nt lds\n"
      "v_mfma_f32_16x16x32_bf16 v[64:67], a[12:15], a[148:151], v[64:67]\n"
      "v_exp_f32 v60, v60\n"
      "v_mfma_f32_16x16x32_bf16 v[64:67], a[16:19], a[152:155], v[64:67]\n"
      "v_cvt_pk_bf16_f32 v103, v58, v59\n"
      "v_add_u32 v2, 0x8000, v2\n"
      "v_mfma_f32_16x16x32_bf16 v[64:67], a[20:23], a[156:159], v[64:67]\n"
      "v_exp_f32 v61, v61\n"
      "v_mfma_f32_16x16x32_bf16 v[64:67], a[24:27], a[160:163], v[64:67]\n"
      "v_cvt_pk_bf16_f32 v88, v60, v61\n"
      "v_add_f32 v10, v10, v46\n"
      "v_mfma_f32_16x16x32_bf16 v[64:67], a[28:31], a[164:167], v[64:67]\n"
      "v_exp_f32 v62, v62\n"
      "v_mfma_f32_16x16x32_bf16 v[68:71], a[0:3], a[168:171], v[32:35]\n"
      "v_add_f32 v10, v10, v47\n"
      "v_add_f32 v6, v6, v48\n"
      "v_mfma_f32_16x16x32_bf16 v[68:71], a[4:7], a[172:175], v[68:71]\n"
      "v_exp_f32 v63, v63\n"
      "v_mfma_f32_16x16x32_bf16 v[68:71], a[8:11], a[176:179], v[68:71]\n"
      "s_waitcnt vmcnt(17) lgkmcnt(0)\n"
      "v_cvt_pk_bf16_f32 v89, v62, v63\n"
      "v_add_f32 v6, v6, v49\n"
      "v_mfma_f32_16x16x32_bf16 v[68:71], a[12:15], a[180:183], v[68:71]\n"
      "s_barrier\n"
      "v_exp_f32 v64, v64\n"
      "v_mfma_f32_16x16x32_bf16 v[68:71], a[16:19], a[184:187], v[68:71]\n"
      "ds_read_b64_tr_b16 a[64:65], v12 offset:0\n"
      "v_add_f32 v6, v6, v50\n"
      "v_mfma_f32_16x16x32_bf16 v[68:71], a[20:23], a[188:191], v[68:71]\n"
      "v_exp_f32 v65, v65\n"
      "v_mfma_f32_16x16x32_bf16 v[68:71], a[24:27], a[192:195], v[68:71]\n"
      "ds_read_b64_tr_b16 a[66:67], v12 offset:8192\n"
      "v_cvt_pk_bf16_f32 v96, v64, v65\n"
      "v_mfma_f32_16x16x32_bf16 v[68:71], a[28:31], a[196:199], v[68:71]\n"
      "v_exp_f32 v66, v66\n"
      "v_mfma_f32_16x16x32_bf16 v[72:75], a[32:35], a[104:107], v[24:27]\n"
      "ds_read_b64_tr_b16 a[68:69], v12 offset:16384\n"
      "v_add_f32 v6, v6, v51\n"
      "v_mfma_f32_16x16x32_bf16 v[72:75], a[36:39], a[108:111], v[72:75]\n"
      "v_exp_f32 v67, v67\n"
      "v_mfma_f32_16x16x32_bf16 v[72:75], a[40:43], a[112:115], v[72:75]\n"
      "ds_read_b64_tr_b16 a[70:71], v12 offset:24576\n"
      "v_cvt_pk_bf16_f32 v97, v66, v67\n"
      "v_mfma_f32_16x16x32_bf16 v[72:75], a[44:47], a[116:119], v[72:75]\n"
      "v_exp_f32 v68, v68\n"
      "v_mfma_f32_16x16x32_bf16 v[72:75], a[48:51], a[120:123], v[72:75]\n"
      "ds_read_b64_tr_b16 a[72:73], v13 offset:0\n"
      "v_add_f32 v8, v8, v52\n"
      "v_mfma_f32_16x16x32_bf16 v[72:75], a[52:55], a[124:127], v[72:75]\n"
      "v_exp_f32 v69, v69\n"
      "v_mfma_f32_16x16x32_bf16 v[72:75], a[56:59], a[128:131], v[72:75]\n"
      "ds_read_b64_tr_b16 a[74:75], v13 offset:8192\n"
      "v_cvt_pk_bf16_f32 v104, v68, v69\n"
      "v_mfma_f32_16x16x32_bf16 v[72:75], a[60:63], a[132:135], v[72:75]\n"
      "v_exp_f32 v70, v70\n"
      "v_mfma_f32_16x16x32_bf16 v[76:79], a[32:35], a[136:139], v[28:31]\n"
      "ds_read_b64_tr_b16 a[76:77], v13 offset:16384\n"
      "v_add_f32 v8, v8, v53\n"
      "v_mfma_f32_16x16x32_bf16 v[76:79], a[36:39], a[140:143], v[76:79]\n"
      "v_exp_f32 v71, v71\n"
      "v_mfma_f32_16x16x32_bf16 v[76:79], a[40:43], a[144:147], v[76:79]\n"
      "ds_read_b64_tr_b16 a[78:79], v13 offset:24576\n"
      "v_cvt_pk_bf16_f32 v105, v70, v71\n"
      "v_mfma_f32_16x16x32_bf16 v[76:79], a[44:47], a[148:151], v[76:79]\n"
      "v_exp_f32 v72, v72\n"
      "v_mfma_f32_16x16x32_bf16 v[76:79], a[48:51], a[152:155], v[76:79]\n"
      "ds_read_b64_tr_b16 a[80:81], v14 offset:0\n"
      "v_add_f32 v8, v8, v54\n"
      "v_mfma_f32_16x16x32_bf16 v[76:79], a[52:55], a[156:159], v[76:79]\n"
      "v_exp_f32 v73, v73\n"
      "v_mfma_f32_16x16x32_bf16 v[76:79], a[56:59], a[160:163], v[76:79]\n"
      "ds_read_b64_tr_b16 a[82:83], v14 offset:8192\n"
      "v_cvt_pk_bf16_f32 v90, v72, v73\n"
      "v_mfma_f32_16x16x32_bf16 v[76:79], a[60:63], a[164:167], v[76:79]\n"
      "v_exp_f32 v74, v74\n"
      "v_mfma_f32_16x16x32_bf16 v[80:83], a[32:35], a[168:171], v[32:35]\n"
      "ds_read_b64_tr_b16 a[84:85], v14 offset:16384\n"
      "v_add_f32 v8, v8, v55\n"
      "v_mfma_f32_16x16x32_bf16 v[80:83], a[36:39], a[172:175], v[80:83]\n"
      "v_exp_f32 v75, v75\n"
      "v_mfma_f32_16x16x32_bf16 v[80:83], a[40:43], a[176:179], v[80:83]\n"
      "ds_read_b64_tr_b16 a[86:87], v14 offset:24576\n"
      "v_cvt_pk_bf16_f32 v91, v74, v75\n"
      "v_mfma_f32_16x16x32_bf16 v[80:83], a[44:47], a[180:183], v[80:83]\n"
      "v_exp_f32 v76, v76\n"
      "v_mfma_f32_16x16x32_bf16 v[80:83], a[48:51], a[184:187], v[80:83]\n"
      "v_mfma_f32_16x16x32_bf16 v[80:83], a[52:55], a[188:191], v[80:83]\n"
      "v_exp_f32 v77, v77\n"
      "v_mfma_f32_16x16x32_bf16 v[80:83], a[56:59], a[192:195], v[80:83]\n"
      "v_cvt_pk_bf16_f32 v98, v76, v77\n"
      "v_mfma_f32_16x16x32_bf16 v[80:83], a[60:63], a[196:199], v[80:83]\n"
      "s_waitcnt lgkmcnt(8)\n"
      "v_exp_f32 v78, v78\n"
      "L_loopN2_%=:\n"
      "v_mfma_f32_16x16x32_bf16 a[200:203], v[84:87], a[64:67], a[200:203]\n"
      "s_add_u32 m0, s23, 0\n"
      "ds_read_b64_tr_b16 a[88:89], v15 offset:0\n"
      "v_add_f32 v10, v10, v56\n"
      "v_mfma_f32_16x16x32_bf16 a[200:203], v[88:91], a[68:71], a[200:203]\n"
      "s_waitcnt lgkmcnt(5)\n"
      "v_exp_f32 v79, v79\n"
      "v_mfma_f32_16x16x32_bf16 a[212:215], v[84:87], a[72:75], a[212:215]\n"
      "buffer_load_dwordx4 v1, s[36:39], 0 offen offset:0 nt lds\n"
      "v_mfma_f32_16x16x32_bf16 a[212:215], v[88:91], a[76:79], a[212:215]\n"
      "s_waitcnt lgkmcnt(1)\n"
      "v_exp_f32 v80, v80\n"
      "v_mfma_f32_16x16x32_bf16 a[224:227], v[84:87], a[80:83], a[224:227]\n"
      "ds_read_b64_tr_b16 a[90:91], v15 offset:8192\n"
      "v_cvt_pk_bf16_f32 v99, v78, v79\n"
      "v_mfma_f32_16x16x32_bf16 a[224:227], v[88:91], a[84:87], a[224:227]\n"
      "v_exp_f32 v81, v81\n"
      "v_mfma_f32_16x16x32_bf16 a[204:207], v[92:95], a[64:67], a[204:207]\n"
      "ds_read_b64_tr_b16 a[92:93], v15 offset:16384\n"
      "v_cvt_pk_bf16_f32 v106, v80, v81\n"
      "v_mfma_f32_16x16x32_bf16 a[204:207], v[96:99], a[68:71], a[204:207]\n"
      "v_exp_f32 v82, v82\n"
      "v_mfma_f32_16x16x32_bf16 a[216:219], v[92:95], a[72:75], a[216:219]\n"
      "ds_read_b64_tr_b16 a[94:95], v15 offset:24576\n"
      "v_add_f32 v10, v10, v57\n"
      "v_mfma_f32_16x16x32_bf16 a[216:219], v[96:99], a[76:79], a[216:219]\n"
      "s_add_u32 m0, s23, 1024\n"
      "v_exp_f32 v83, v83\n"
      "v_mfma_f32_16x16x32_bf16 a[228:231], v[92:95], a[80:83], a[228:231]\n"
      "buffer_load_dwordx4 v1, s[36:39], s52 offen offset:0 nt lds\n"
      "v_mfma_f32_16x16x32_bf16 a[228:231], v[96:99], a[84:87], a[228:231]\n"
      "ds_read_b64_tr_b16 a[96:97], v12 offset:2048\n"
      "v_cvt_pk_bf16_f32 v107, v82, v83\n"
      "v_mfma_f32_16x16x32_bf16 a[208:211], v[100:103], a[64:67], a[208:211]\n"
      "ds_read_b64_tr_b16 a[98:99], v12 offset:10240\n"
      "v_add_f32 v10, v10, v58\n"
      "v_mfma_f32_16x16x32_bf16 a[208:211], v[104:107], a[68:71], a[208:211]\n"
      "ds_read_b64_tr_b16 a[100:101], v12 offset:18432\n"
      "v_add_f32 v10, v10, v59\n"
      "v_mfma_f32_16x16x32_bf16 a[220:223], v[100:103], a[72:75], a[220:223]\n"
      "ds_read_b64_tr_b16 a[102:103], v12 offset:26624\n"
      "v_add_f32 v6, v6, v60\n"
      "v_mfma_f32_16x16x32_bf16 a[220:223], v[104:107], a[76:79], a[220:223]\n"
      "ds_read_b64_tr_b16 a[64:65], v13 offset:2048\n"
      "v_add_f32 v6, v6, v61\n"
      "v_mfma_f32_16x16x32_bf16 a[232:235], v[100:103], a[80:83], a[232:235]\n"
      "s_add_u32 m0, s23, 1920\n"
      "ds_read_b64_tr_b16 a[66:67], v13 offset:10240\n"
      "v_add_f32 v6, v6, v62\n"
      "v_mfma_f32_16x16x32_bf16 a[232:235], v[104:107], a[84:87], a[232:235]\n"
      "s_waitcnt lgkmcnt(6)\n"
      "ds_read_b64_tr_b16 a[68:69], v13 offset:18432\n"
      "v_add_f32 v6, v6, v63\n"
      "v_mfma_f32_16x16x32_bf16 a[236:239], v[84:87], a[88:91], a[236:239]\n"
      "buffer_load_dwordx4 v1, s[36:39], 0 offen offset:128 nt lds\n"
      "v_mfma_f32_16x16x32_bf16 a[236:239], v[88:91], a[92:95], a[236:239]\n"
      "ds_read_b64_tr_b16 a[70:71], v13 offset:26624\n"
      "v_add_f32 v8, v8, v64\n"
      "v_mfma_f32_16x16x32_bf16 a[240:243], v[92:95], a[88:91], a[240:243]\n"
      "ds_read_b64_tr_b16 a[72:73], v14 offset:2048\n"
      "v_add_f32 v8, v8, v65\n"
      "v_mfma_f32_16x16x32_bf16 a[240:243], v[96:99], a[92:95], a[240:243]\n"
      "ds_read_b64_tr_b16 a[74:75], v14 offset:10240\n"
      "v_add_f32 v8, v8, v66\n"
      "v_mfma_f32_16x16x32_bf16 v[108:111], v[100:103], a[88:91], v[108:111]\n"
      "ds_read_b64_tr_b16 a[76:77], v14 offset:18432\n"
      "v_add_f32 v8, v8, v67\n"
      "v_mfma_f32_16x16x32_bf16 v[108:111], v[104:107], a[92:95], v[108:111]\n"
      "s_waitcnt lgkmcnt(7)\n"
      "ds_read_b64_tr_b16 a[78:79], v14 offset:26624\n"
      "v_add_f32 v10, v10, v68\n"
      "v_mfma_f32_16x16x32_bf16 v[112:115], v[84:87], a[96:99], v[112:115]\n"
      "ds_read_b64_tr_b16 a[80:81], v15 offset:2048\n"
      "v_add_f32 v10, v10, v69\n"
      "v_mfma_f32_16x16x32_bf16 v[112:115], v[88:91], a[100:103], v[112:115]\n"
      "ds_read_b64_tr_b16 a[82:83], v15 offset:10240\n"
      "v_add_f32 v10, v10, v70\n"
      "v_mfma_f32_16x16x32_bf16 v[116:119], v[92:95], a[96:99], v[116:119]\n"
      "ds_read_b64_tr_b16 a[84:85], v15 offset:18432\n"
      "v_add_f32 v10, v10, v71\n"
      "v_mfma_f32_16x16x32_bf16 v[116:119], v[96:99], a[100:103], v[116:119]\n"
      "ds_read_b64_tr_b16 a[86:87], v15 offset:26624\n"
      "v_add_f32 v6, v6, v72\n"
      "v_mfma_f32_16x16x32_bf16 v[120:123], v[100:103], a[96:99], v[120:123]\n"
      "s_add_u32 m0, s23, 2944\n"
      "ds_read_b64_tr_b16 a[88:89], v12 offset:4096\n"
      "v_add_f32 v6, v6, v73\n"
      "v_mfma_f32_16x16x32_bf16 v[120:123], v[104:107], a[100:103], v[120:123]\n"
      "s_waitcnt lgkmcnt(9)\n"
      "ds_read_b64_tr_b16 a[90:91], v12 offset:12288\n"
      "v_add_f32 v6, v6, v74\n"
      "v_mfma_f32_16x16x32_bf16 v[124:127], v[84:87], a[64:67], v[124:127]\n"
      "buffer_load_dwordx4 v1, s[36:39], s52 offen offset:128 nt lds\n"
      "v_mfma_f32_16x16x32_bf16 v[124:127], v[88:91], a[68:71], v[124:127]\n"
      "ds_read_b64_tr_b16 a[92:93], v12 offset:20480\n"
      "v_add_f32 v6, v6, v75\n"
      "v_mfma_f32_16x16x32_bf16 v[128:131], v[92:95], a[64:67], v[128:131]\n"
      "ds_read_b64_tr_b16 a[94:95], v12 offset:28672\n"
      "v_add_f32 v8, v8, v76\n"
      "v_mfma_f32_16x16x32_bf16 v[128:131], v[96:99], a[68:71], v[128:131]\n"
      "ds_read_b64_tr_b16 a[96:97], v13 offset:4096\n"
      "v_add_f32 v8, v8, v77\n"
      "v_mfma_f32_16x16x32_bf16 v[132:135], v[100:103], a[64:67], v[132:135]\n"
      "ds_read_b64_tr_b16 a[98:99], v13 offset:12288\n"
      "v_add_f32 v8, v8, v78\n"
      "v_mfma_f32_16x16x32_bf16 v[132:135], v[104:107], a[68:71], v[132:135]\n"
      "s_waitcnt lgkmcnt(10)\n"
      "ds_read_b64_tr_b16 a[100:101], v13 offset:20480\n"
      "v_add_f32 v8, v8, v79\n"
      "v_mfma_f32_16x16x32_bf16 v[136:139], v[84:87], a[72:75], v[136:139]\n"
      "ds_read_b64_tr_b16 a[102:103], v13 offset:28672\n"
      "v_add_f32 v10, v10, v80\n"
      "v_mfma_f32_16x16x32_bf16 v[136:139], v[88:91], a[76:79], v[136:139]\n"
      "s_add_u32 m0, s23, 3840\n"
      "ds_read_b64_tr_b16 a[64:65], v14 offset:4096\n"
      "v_add_f32 v10, v10, v81\n"
      "v_mfma_f32_16x16x32_bf16 v[140:143], v[92:95], a[72:75], v[140:143]\n"
      "buffer_load_dwordx4 v1, s[36:39], 0 offen offset:256 nt lds\n"
      "v_mfma_f32_16x16x32_bf16 v[140:143], v[96:99], a[76:79], v[140:143]\n"
      "ds_read_b64_tr_b16 a[66:67], v14 offset:12288\n"
      "v_add_f32 v10, v10, v82\n"
      "v_mfma_f32_16x16x32_bf16 v[144:147], v[100:103], a[72:75], v[144:147]\n"
      "ds_read_b64_tr_b16 a[68:69], v14 offset:20480\n"
      "v_add_f32 v10, v10, v83\n"
      "v_mfma_f32_16x16x32_bf16 v[144:147], v[104:107], a[76:79], v[144:147]\n"
      "s_waitcnt lgkmcnt(11)\n"
      "ds_read_b64_tr_b16 a[70:71], v14 offset:28672\n"
      "v_mfma_f32_16x16x32_bf16 v[148:151], v[84:87], a[80:83], v[148:151]\n"
      "ds_read_b64_tr_b16 a[72:73], v15 offset:4096\n"
      "v_mfma_f32_16x16x32_bf16 v[148:151], v[88:91], a[84:87], v[148:151]\n"
      "ds_read_b64_tr_b16 a[74:75], v15 offset:12288\n"
      "v_mfma_f32_16x16x32_bf16 v[152:155], v[92:95], a[80:83], v[152:155]\n"
      "ds_read_b64_tr_b16 a[76:77], v15 offset:20480\n"
      "v_mfma_f32_16x16x32_bf16 v[152:155], v[96:99], a[84:87], v[152:155]\n"
      "s_add_u32 m0, s23, 4864\n"
      "ds_read_b64_tr_b16 a[78:79], v15 offset:28672\n"
      "v_mfma_f32_16x16x32_bf16 v[156:159], v[100:103], a[80:83], v[156:159]\n"
      "buffer_load_dwordx4 v1, s[36:39], s52 offen offset:256 nt lds\n"
      "v_mfma_f32_16x16x32_bf16 v[156:159], v[104:107], a[84:87], v[156:159]\n"
      "s_waitcnt lgkmcnt(12)\n"
      "ds_read_b64_tr_b16 a[80:81], v12 offset:6144\n"
      "v_mfma_f32_16x16x32_bf16 v[160:163], v[84:87], a[88:91], v[160:163]\n"
      "ds_read_b64_tr_b16 a[82:83], v12 offset:14336\n"
      "v_mfma_f32_16x16x32_bf16 v[160:163], v[88:91], a[92:95], v[160:163]\n"
      "ds_read_b64_tr_b16 a[84:85], v12 offset:22528\n"
      "v_mfma_f32_16x16x32_bf16 v[164:167], v[92:95], a[88:91], v[164:167]\n"
      "ds_read_b64_tr_b16 a[86:87], v12 offset:30720\n"
      "v_mfma_f32_16x16x32_bf16 v[164:167], v[96:99], a[92:95], v[164:167]\n"
      "v_mfma_f32_16x16x32_bf16 v[168:171], v[100:103], a[88:91], v[168:171]\n"
      "s_add_u32 m0, s23, 5760\n"
      "v_mfma_f32_16x16x32_bf16 v[168:171], v[104:107], a[92:95], v[168:171]\n"
      "s_waitcnt lgkmcnt(12)\n"
      "ds_read_b64_tr_b16 a[88:89], v13 offset:6144\n"
      "v_mfma_f32_16x16x32_bf16 v[172:175], v[84:87], a[96:99], v[172:175]\n"
      "buffer_load_dwordx4 v1, s[36:39], 0 offen offset:384 nt lds\n"
      "v_mfma_f32_16x16x32_bf16 v[172:175], v[88:91], a[100:103], v[172:175]\n"
      "ds_read_b64_tr_b16 a[90:91], v13 offset:14336\n"
      "v_mfma_f32_16x16x32_bf16 v[176:179], v[92:95], a[96:99], v[176:179]\n"
      "ds_read_b64_tr_b16 a[92:93], v13 offset:22528\n"
      "v_mfma_f32_16x16x32_bf16 v[176:179], v[96:99], a[100:103], v[176:179]\n"
      "ds_read_b64_tr_b16 a[94:95], v13 offset:30720\n"
      "v_mfma_f32_16x16x32_bf16 v[180:183], v[100:103], a[96:99], v[180:183]\n"
      "v_mfma_f32_16x16x32_bf16 v[180:183], v[104:107], a[100:103], v[180:183]\n"
      "s_waitcnt lgkmcnt(12)\n"
      "ds_read_b64_tr_b16 a[96:97], v14 offset:6144\n"
      "v_mfma_f32_16x16x32_bf16 v[184:187], v[84:87], a[64:67], v[184:187]\n"
      "ds_read_b64_tr_b16 a[98:99], v14 offset:14336\n"
      "v_mfma_f32_16x16x32_bf16 v[184:187], v[88:91], a[68:71], v[184:187]\n"
      "ds_read_b64_tr_b16 a[100:101], v14 offset:22528\n"
      "v_mfma_f32_16x16x32_bf16 v[188:191], v[92:95], a[64:67], v[188:191]\n"
      "ds_read_b64_tr_b16 a[102:103], v14 offset:30720\n"
      "v_mfma_f32_16x16x32_bf16 v[188:191], v[96:99], a[68:71], v[188:191]\n"
      "v_mfma_f32_16x16x32_bf16 v[192:195], v[100:103], a[64:67], v[192:195]\n"
      "s_add_u32 m0, s23, 6784\n"
      "v_mfma_f32_16x16x32_bf16 v[192:195], v[104:107], a[68:71], v[192:195]\n"
      "s_waitcnt lgkmcnt(12)\n"
      "ds_read_b64_tr_b16 a[64:65], v15 offset:6144\n"
      "v_mfma_f32_16x16x32_bf16 v[196:199], v[84:87], a[72:75], v[196:199]\n"
      "buffer_load_dwordx4 v1, s[36:39], s52 offen offset:384 nt lds\n"
      "v_mfma_f32_16x16x32_bf16 v[196:199], v[88:91], a[76:79], v[196:199]\n"
      "ds_read_b64_tr_b16 a[66:67], v15 offset:14336\n"
      "v_add_u32 v1, 0x8000, v1\n"
      "v_mfma_f32_16x16x32_bf16 v[200:203], v[92:95], a[72:75], v[200:203]\n"
      "ds_read_b64_tr_b16 a[68:69], v15 offset:22528\n"
      "v_mfma_f32_16x16x32_bf16 v[200:203], v[96:99], a[76:79], v[200:203]\n"
      "ds_read_b64_tr_b16 a[70:71], v15 offset:30720\n"
      "v_mfma_f32_16x16x32_bf16 v[204:207], v[100:103], a[72:75], v[204:207]\n"
      "buffer_load_dword v23, v22, s[60:63], 0 offen\n"
      "v_mfma_f32_16x16x32_bf16 v[204:207], v[104:107], a[76:79], v[204:207]\n"
      "s_waitcnt lgkmcnt(12)\n"
      "v_add_u32 v22, 0x400, v22\n"
      "v_mfma_f32_16x16x32_bf16 v[208:211], v[84:87], a[80:83], v[208:211]\n"
      "v_mfma_f32_16x16x32_bf16 v[208:211], v[88:91], a[84:87], v[208:211]\n"
      "v_mfma_f32_16x16x32_bf16 v[212:215], v[92:95], a[80:83], v[212:215]\n"
      "v_mfma_f32_16x16x32_bf16 v[212:215], v[96:99], a[84:87], v[212:215]\n"
      "L_midN2_%=:\n"
      "v_mfma_f32_16x16x32_bf16 v[216:219], v[100:103], a[80:83], v[216:219]\n"
      "v_mfma_f32_16x16x32_bf16 v[216:219], v[104:107], a[84:87], v[216:219]\n"
      "s_waitcnt lgkmcnt(8)\n"
      "v_mfma_f32_16x16x32_bf16 v[220:223], v[84:87], a[88:91], v[220:223]\n"
      "s_waitcnt vmcnt(17) lgkmcnt(0)\n"
      "v_mfma_f32_16x16x32_bf16 v[220:223], v[88:91], a[92:95], v[220:223]\n"
      "s_barrier\n"
      "ds_read_b128 a[0:3], v3 offset:32768\n"
      "v_mfma_f32_16x16x32_bf16 v[224:227], v[92:95], a[88:91], v[224:227]\n"
      "ds_read_b128 a[4:7], v4 offset:32768\n"
      "v_mfma_f32_16x16x32_bf16 v[224:227], v[96:99], a[92:95], v[224:227]\n"
      "s_add_u32 m0, s23, 65536\n"
      "ds_read_b128 a[8:11], v3 offset:34816\n"
      "v_mfma_f32_16x16x32_bf16 v[228:231], v[100:103], a[88:91], v[228:231]\n"
      "buffer_load_dwordx4 v2, s[40:43], 0 offen offset:0 nt lds\n"
      "v_mfma_f32_16x16x32_bf16 v[228:231], v[104:107], a[92:95], v[228:231]\n"
      "ds_read_b128 a[12:15], v4 offset:34816\n"
      "v_mfma_f32_16x16x32_bf16 v[232:235], v[84:87], a[96:99], v[232:235]\n"
      "ds_read_b128 a[16:19], v3 offset:36864\n"
      "v_mfma_f32_16x16x32_bf16 v[232:235], v[88:91], a[100:103], v[232:235]\n"
      "ds_read_b128 a[20:23], v4 offset:36864\n"
      "v_mfma_f32_16x16x32_bf16 v[236:239], v[92:95], a[96:99], v[236:239]\n"
      "ds_read_b128 a[24:27], v3 offset:38912\n"
      "v_mfma_f32_16x16x32_bf16 v[236:239], v[96:99], a[100:103], v[236:239]\n"
      "ds_read_b128 a[28:31], v4 offset:38912\n"
      "v_mfma_f32_16x16x32_bf16 v[240:243], v[100:103], a[96:99], v[240:243]\n"
      "ds_read_b128 a[32:35], v3 offset:40960\n"
      "v_mfma_f32_16x16x32_bf16 v[240:243], v[104:107], a[100:103], v[240:243]\n"
      "ds_read_b128 a[36:39], v4 offset:40960\n"
      "v_mfma_f32_16x16x32_bf16 v[244:247], v[84:87], a[64:67], v[244:247]\n"
      "ds_read_b128 a[40:43], v3 offset:43008\n"
      "v_mfma_f32_16x16x32_bf16 v[244:247], v[88:91], a[68:71], v[244:247]\n"
      "ds_read_b128 a[44:47], v4 offset:43008\n"
      "v_mfma_f32_16x16x32_bf16 v[248:251], v[92:95], a[64:67], v[248:251]\n"
      "ds_read_b128 a[48:51], v3 offset:45056\n"
      "v_mfma_f32_16x16x32_bf16 v[248:251], v[96:99], a[68:71], v[248:251]\n"
      "s_add_u32 m0, s23, 66560\n"
      "ds_read_b128 a[52:55], v4 offset:45056\n"
      "v_mfma_f32_16x16x32_bf16 v[252:255], v[100:103], a[64:67], v[252:255]\n"
      "buffer_load_dwordx4 v2, s[40:43], s52 offen offset:0 nt lds\n"
      "v_mfma_f32_16x16x32_bf16 v[252:255], v[104:107], a[68:71], v[252:255]\n"
      "s_waitcnt lgkmcnt(6)\n"
      "ds_read_b128 a[56:59], v3 offset:47104\n"
      "v_mfma_f32_16x16x32_bf16 v[36:39], a[0:3], a[104:107], v[24:27]\n"
      "ds_read_b128 a[60:63], v4 offset:47104\n"
      "v_mfma_f32_16x16x32_bf16 v[36:39], a[4:7], a[108:111], v[36:39]\n"
      "v_mfma_f32_16x16x32_bf16 v[36:39], a[8:11], a[112:115], v[36:39]\n"
      "v_mfma_f32_16x16x32_bf16 v[36:39], a[12:15], a[116:119], v[36:39]\n"
      "v_mfma_f32_16x16x32_bf16 v[36:39], a[16:19], a[120:123], v[36:39]\n"
      "v_mfma_f32_16x16x32_bf16 v[36:39], a[20:23], a[124:127], v[36:39]\n"
      "s_add_u32 m0, s23, 67456\n"
      "v_mfma_f32_16x16x32_bf16 v[36:39], a[24:27], a[128:131], v[36:39]\n"
      "buffer_load_dwordx4 v2, s[40:43], 0 offen offset:128 nt lds\n"
      "v_mfma_f32_16x16x32_bf16 v[36:39], a[28:31], a[132:135], v[36:39]\n"
      "v_mfma_f32_16x16x32_bf16 v[40:43], a[0:3], a[136:139], v[28:31]\n"
      "v_mfma_f32_16x16x32_bf16 v[40:43], a[4:7], a[140:143], v[40:43]\n"
      "v_mfma_f32_16x16x32_bf16 v[40:43], a[8:11], a[144:147], v[40:43]\n"
      "v_mfma_f32_16x16x32_bf16 v[40:43], a[12:15], a[148:151], v[40:43]\n"
      "v_exp_f32 v36, v36\n"
      "v_mfma_f32_16x16x32_bf16 v[40:43], a[16:19], a[152:155], v[40:43]\n"
      "v_add_f32 v6, v6, v36\n"
      "v_mfma_f32_16x16x32_bf16 v[40:43], a[20:23], a[156:159], v[40:43]\n"
      "v_exp_f32 v37, v37\n"
      "v_mfma_f32_16x16x32_bf16 v[40:43], a[24:27], a[160:163], v[40:43]\n"
      "v_cvt_pk_bf16_f32 v84, v36, v37\n"
      "v_add_f32 v6, v6, v37\n"
      "v_mfma_f32_16x16x32_bf16 v[40:43], a[28:31], a[164:167], v[40:43]\n"
      "v_exp_f32 v38, v38\n"
      "v_mfma_f32_16x16x32_bf16 v[44:47], a[0:3], a[168:171], v[32:35]\n"
      "ds_read_b128 a[0:3], v3 offset:49152\n"
      "v_add_f32 v6, v6, v38\n"
      "v_mfma_f32_16x16x32_bf16 v[44:47], a[4:7], a[172:175], v[44:47]\n"
      "s_add_u32 m0, s23, 68480\n"
      "v_exp_f32 v39, v39\n"
      "v_mfma_f32_16x16x32_bf16 v[44:47], a[8:11], a[176:179], v[44:47]\n"
      "buffer_load_dwordx4 v2, s[40:43], s52 offen offset:128 nt lds\n"
      "v_mfma_f32_16x16x32_bf16 v[44:47], a[12:15], a[180:183], v[44:47]\n"
      "v_exp_f32 v40, v40\n"
      "v_mfma_f32_16x16x32_bf16 v[44:47], a[16:19], a[184:187], v[44:47]\n"
      "ds_read_b128 a[4:7], v4 offset:49152\n"
      "v_cvt_pk_bf16_f32 v85, v38, v39\n"
      "v_mfma_f32_16x16x32_bf16 v[44:47], a[20:23], a[188:191], v[44:47]\n"
      "v_exp_f32 v41, v41\n"
      "v_mfma_f32_16x16x32_bf16 v[44:47], a[24:27], a[192:195], v[44:47]\n"
      "ds_read_b128 a[8:11], v3 offset:51200\n"
      "v_cvt_pk_bf16_f32 v92, v40, v41\n"
      "v_mfma_f32_16x16x32_bf16 v[44:47], a[28:31], a[196:199], v[44:47]\n"
      "s_waitcnt lgkmcnt(3)\n"
      "v_exp_f32 v42, v42\n"
      "v_mfma_f32_16x16x32_bf16 v[48:51], a[32:35], a[104:107], v[24:27]\n"
      "ds_read_b128 a[12:15], v4 offset:51200\n"
      "v_add_f32 v6, v6, v39\n"
      "v_mfma_f32_16x16x32_bf16 v[48:51], a[36:39], a[108:111], v[48:51]\n"
      "s_add_u32 m0, s23, 69376\n"
      "v_exp_f32 v43, v43\n"
      "v_mfma_f32_16x16x32_bf16 v[48:51], a[40:43], a[112:115], v[48:51]\n"
      "buffer_load_dwordx4 v2, s[40:43], 0 offen offset:256 nt lds\n"
      "v_mfma_f32_16x16x32_bf16 v[48:51], a[44:47], a[116:119], v[48:51]\n"
      "v_exp_f32 v44, v44\n"
      "v_mfma_f32_16x16x32_bf16 v[48:51], a[48:51], a[120:123], v[48:51]\n"
      "ds_read_b128 a[16:19], v3 offset:53248\n"
      "v_cvt_pk_bf16_f32 v93, v42, v43\n"
      "v_mfma_f32_16x16x32_bf16 v[48:51], a[52:55], a[124:127], v[48:51]\n"
      "v_exp_f32 v45, v45\n"
      "v_mfma_f32_16x16x32_bf16 v[48:51], a[56:59], a[128:131], v[48:51]\n"
      "ds_read_b128 a[20:23], v4 offset:53248\n"
      "v_cvt_pk_bf16_f32 v100, v44, v45\n"
      "v_mfma_f32_16x16x32_bf16 v[48:51], a[60:63], a[132:135], v[48:51]\n"
      "v_exp_f32 v46, v46\n"
      "v_mfma_f32_16x16x32_bf16 v[52:55], a[32:35], a[136:139], v[28:31]\n"
      "ds_read_b128 a[24:27], v3 offset:55296\n"
      "v_add_f32 v8, v8, v40\n"
      "v_mfma_f32_16x16x32_bf16 v[52:55], a[36:39], a[140:143], v[52:55]\n"
      "v_exp_f32 v47, v47\n"
      "v_mfma_f32_16x16x32_bf16 v[52:55], a[40:43], a[144:147], v[52:55]\n"
      "ds_read_b128 a[28:31], v4 offset:55296\n"
      "v_cvt_pk_bf16_f32 v101, v46, v47\n"
      "v_mfma_f32_16x16x32_bf16 v[52:55], a[44:47], a[148:151], v[52:55]\n"
      "v_exp_f32 v48, v48\n"
      "v_mfma_f32_16x16x32_bf16 v[52:55], a[48:51], a[152:155], v[52:55]\n"
      "v_add_f32 v8, v8, v41\n"
      "v_add_f32 v8, v8, v42\n"
      "v_mfma_f32_16x16x32_bf16 v[52:55], a[52:55], a[156:159], v[52:55]\n"
      "s_add_u32 m0, s23, 70400\n"
      "v_exp_f32 v49, v49\n"
      "v_mfma_f32_16x16x32_bf16 v[52:55], a[56:59], a[160:163], v[52:55]\n"
      "buffer_load_dwordx4 v2, s[40:43], s52 offen offset:256 nt lds\n"
      "v_mfma_f32_16x16x32_bf16 v[52:55], a[60:63], a[164:167], v[52:55]\n"
      "v_exp_f32 v50, v50\n"
      "v_mfma_f32_16x16x32_bf16 v[56:59], a[32:35], a[168:171], v[32:35]\n"
      "ds_read_b128 a[32:35], v3 offset:57344\n"
      "v_cvt_pk_bf16_f32 v86, v48, v49\n"
      "v_mfma_f32_16x16x32_bf16 v[56:59], a[36:39], a[172:175], v[56:59]\n"
      "v_exp_f32 v51, v51\n"
      "v_mfma_f32_16x16x32_bf16 v[56:59], a[40:43], a[176:179], v[56:59]\n"
      "ds_read_b128 a[36:39], v4 offset:57344\n"
      "v_cvt_pk_bf16_f32 v87, v50, v51\n"
      "v_mfma_f32_16x16x32_bf16 v[56:59], a[44:47], a[180:183], v[56:59]\n"
      "v_exp_f32 v52, v52\n"
      "v_mfma_f32_16x16x32_bf16 v[56:59], a[48:51], a[184:187], v[56:59]\n"
      "ds_read_b128 a[40:43], v3 offset:59392\n"
      "v_add_f32 v8, v8, v43\n"
      "v_mfma_f32_16x16x32_bf16 v[56:59], a[52:55], a[188:191], v[56:59]\n"
      "s_add_u32 m0, s23, 71296\n"
      "v_exp_f32 v53, v53\n"
      "v_mfma_f32_16x16x32_bf16 v[56:59], a[56:59], a[192:195], v[56:59]\n"
      "buffer_load_dwordx4 v2, s[40:43], 0 offen offset:384 nt lds\n"
      "v_mfma_f32_16x16x32_bf16 v[56:59], a[60:63], a[196:199], v[56:59]\n"
      "s_waitcnt lgkmcnt(3)\n"
      "v_exp_f32 v54, v54\n"
      "v_mfma_f32_16x16x32_bf16 v[60:63], a[0:3], a[104:107], v[24:27]\n"
      "ds_read_b128 a[44:47], v4 offset:59392\n"
      "v_cvt_pk_bf16_f32 v94, v52, v53\n"
      "v_mfma_f32_16x16x32_bf16 v[60:63], a[4:7], a[108:111], v[60:63]\n"
      "v_exp_f32 v55, v55\n"
      "v_mfma_f32_16x16x32_bf16 v[60:63], a[8:11], a[112:115], v[60:63]\n"
      "ds_read_b128 a[48:51], v3 offset:61440\n"
      "v_cvt_pk_bf16_f32 v95, v54, v55\n"
      "v_mfma_f32_16x16x32_bf16 v[60:63], a[12:15], a[116:119], v[60:63]\n"
      "v_exp_f32 v56, v56\n"
      "v_mfma_f32_16x16x32_bf16 v[60:63], a[16:19], a[120:123], v[60:63]\n"
      "ds_read_b128 a[52:55], v4 offset:61440\n"
      "v_add_f32 v10, v10, v44\n"
      "v_mfma_f32_16x16x32_bf16 v[60:63], a[20:23], a[124:127], v[60:63]\n"
      "v_exp_f32 v57, v57\n"
      "v_mfma_f32_16x16x32_bf16 v[60:63], a[24:27], a[128:131], v[60:63]\n"
      "ds_read_b128 a[56:59], v3 offset:63488\n"
      "v_cvt_pk_bf16_f32 v102, v56, v57\n"
      "v_mfma_f32_16x16x32_bf16 v[60:63], a[28:31], a[132:135], v[60:63]\n"
      "v_exp_f32 v58, v58\n"
      "v_mfma_f32_16x16x32_bf16 v[64:67], a[0:3], a[136:139], v[28:31]\n"
      "ds_read_b128 a[60:63], v4 offset:63488\n"
      "v_add_f32 v10, v10, v45\n"
      "v_mfma_f32_16x16x32_bf16 v[64:67], a[4:7], a[140:143], v[64:67]\n"
      "s_add_u32 m0, s23, 72320\n"
      "v_exp_f32 v59, v59\n"
      "v_mfma_f32_16x16x32_bf16 v[64:67], a[8:11], a[144:147], v[64:67]\n"
      "buffer_load_dwordx4 v2, s[40:43], s52 offen offset:384 nt lds\n"
      "v_mfma_f32_16x16x32_bf16 v[64:67], a[12:15], a[148:151], v[64:67]\n"
      "v_exp_f32 v60, v60\n"
      "v_mfma_f32_16x16x32_bf16 v[64:67], a[16:19], a[152:155], v[64:67]\n"
      "v_cvt_pk_bf16_f32 v103, v58, v59\n"
      "v_add_u32 v2, 0x8000, v2\n"
      "v_mfma_f32_16x16x32_bf16 v[64:67], a[20:23], a[156:159], v[64:67]\n"
      "v_exp_f32 v61, v61\n"
      "v_mfma_f32_16x16x32_bf16 v[64:67], a[24:27], a[160:163], v[64:67]\n"
      "v_cvt_pk_bf16_f32 v88, v60, v61\n"
      "v_add_f32 v10, v10, v46\n"
      "v_mfma_f32_16x16x32_bf16 v[64:67], a[28:31], a[164:167], v[64:67]\n"
      "v_exp_f32 v62, v62\n"
      "v_mfma_f32_16x16x32_bf16 v[68:71], a[0:3], a[168:171], v[32:35]\n"
      "v_add_f32 v10, v10, v47\n"
      "v_add_f32 v6, v6, v48\n"
      "v_mfma_f32_16x16x32_bf16 v[68:71], a[4:7], a[172:175], v[68:71]\n"
      "v_exp_f32 v63, v63\n"
      "v_mfma_f32_16x16x32_bf16 v[68:71], a[8:11], a[176:179], v[68:71]\n"
      "s_waitcnt vmcnt(17) lgkmcnt(0)\n"
      "v_cvt_pk_bf16_f32 v89, v62, v63\n"
      "v_add_f32 v6, v6, v49\n"
      "v_mfma_f32_16x16x32_bf16 v[68:71], a[12:15], a[180:183], v[68:71]\n"
      "s_barrier\n"
      "v_exp_f32 v64, v64\n"
      "v_mfma_f32_16x16x32_bf16 v[68:71], a[16:19], a[184:187], v[68:71]\n"
      "ds_read_b64_tr_b16 a[64:65], v12 offset:32768\n"
      "v_add_f32 v6, v6, v50\n"
      "v_mfma_f32_16x16x32_bf16 v[68:71], a[20:23], a[188:191], v[68:71]\n"
      "v_exp_f32 v65, v65\n"
      "v_mfma_f32_16x16x32_bf16 v[68:71], a[24:27], a[192:195], v[68:71]\n"
      "ds_read_b64_tr_b16 a[66:67], v12 offset:40960\n"
      "v_cvt_pk_bf16_f32 v96, v64, v65\n"
      "v_mfma_f32_16x16x32_bf16 v[68:71], a[28:31], a[196:199], v[68:71]\n"
      "v_exp_f32 v66, v66\n"
      "v_mfma_f32_16x16x32_bf16 v[72:75], a[32:35], a[104:107], v[24:27]\n"
      "ds_read_b64_tr_b16 a[68:69], v12 offset:49152\n"
      "v_add_f32 v6, v6, v51\n"
      "v_mfma_f32_16x16x32_bf16 v[72:75], a[36:39], a[108:111], v[72:75]\n"
      "v_exp_f32 v67, v67\n"
      "v_mfma_f32_16x16x32_bf16 v[72:75], a[40:43], a[112:115], v[72:75]\n"
      "ds_read_b64_tr_b16 a[70:71], v12 offset:57344\n"
      "v_cvt_pk_bf16_f32 v97, v66, v67\n"
      "v_mfma_f32_16x16x32_bf16 v[72:75], a[44:47], a[116:119], v[72:75]\n"
      "v_exp_f32 v68, v68\n"
      "v_mfma_f32_16x16x32_bf16 v[72:75], a[48:51], a[120:123], v[72:75]\n"
      "ds_read_b64_tr_b16 a[72:73], v13 offset:32768\n"
      "v_add_f32 v8, v8, v52\n"
      "v_mfma_f32_16x16x32_bf16 v[72:75], a[52:55], a[124:127], v[72:75]\n"
      "v_exp_f32 v69, v69\n"
      "v_mfma_f32_16x16x32_bf16 v[72:75], a[56:59], a[128:131], v[72:75]\n"
      "ds_read_b64_tr_b16 a[74:75], v13 offset:40960\n"
      "v_cvt_pk_bf16_f32 v104, v68, v69\n"
      "v_mfma_f32_16x16x32_bf16 v[72:75], a[60:63], a[132:135], v[72:75]\n"
      "v_exp_f32 v70, v70\n"
      "v_mfma_f32_16x16x32_bf16 v[76:79], a[32:35], a[136:139], v[28:31]\n"
      "ds_read_b64_tr_b16 a[76:77], v13 offset:49152\n"
      "v_add_f32 v8, v8, v53\n"
      "v_mfma_f32_16x16x32_bf16 v[76:79], a[36:39], a[140:143], v[76:79]\n"
      "v_exp_f32 v71, v71\n"
      "v_mfma_f32_16x16x32_bf16 v[76:79], a[40:43], a[144:147], v[76:79]\n"
      "ds_read_b64_tr_b16 a[78:79], v13 offset:57344\n"
      "v_cvt_pk_bf16_f32 v105, v70, v71\n"
      "v_mfma_f32_16x16x32_bf16 v[76:79], a[44:47], a[148:151], v[76:79]\n"
      "v_exp_f32 v72, v72\n"
      "v_mfma_f32_16x16x32_bf16 v[76:79], a[48:51], a[152:155], v[76:79]\n"
      "ds_read_b64_tr_b16 a[80:81], v14 offset:32768\n"
      "v_add_f32 v8, v8, v54\n"
      "v_mfma_f32_16x16x32_bf16 v[76:79], a[52:55], a[156:159], v[76:79]\n"
      "v_exp_f32 v73, v73\n"
      "v_mfma_f32_16x16x32_bf16 v[76:79], a[56:59], a[160:163], v[76:79]\n"
      "ds_read_b64_tr_b16 a[82:83], v14 offset:40960\n"
      "v_cvt_pk_bf16_f32 v90, v72, v73\n"
      "v_mfma_f32_16x16x32_bf16 v[76:79], a[60:63], a[164:167], v[76:79]\n"
      "v_exp_f32 v74, v74\n"
      "v_mfma_f32_16x16x32_bf16 v[80:83], a[32:35], a[168:171], v[32:35]\n"
      "ds_read_b64_tr_b16 a[84:85], v14 offset:49152\n"
      "v_add_f32 v8, v8, v55\n"
      "v_mfma_f32_16x16x32_bf16 v[80:83], a[36:39], a[172:175], v[80:83]\n"
      "v_exp_f32 v75, v75\n"
      "v_mfma_f32_16x16x32_bf16 v[80:83], a[40:43], a[176:179], v[80:83]\n"
      "ds_read_b64_tr_b16 a[86:87], v14 offset:57344\n"
      "v_cvt_pk_bf16_f32 v91, v74, v75\n"
      "v_mfma_f32_16x16x32_bf16 v[80:83], a[44:47], a[180:183], v[80:83]\n"
      "v_exp_f32 v76, v76\n"
      "v_mfma_f32_16x16x32_bf16 v[80:83], a[48:51], a[184:187], v[80:83]\n"
      "v_mfma_f32_16x16x32_bf16 v[80:83], a[52:55], a[188:191], v[80:83]\n"
      "v_exp_f32 v77, v77\n"
      "v_mfma_f32_16x16x32_bf16 v[80:83], a[56:59], a[192:195], v[80:83]\n"
      "s_sub_u32 s20, s20, 1\n"
      "v_cvt_pk_bf16_f32 v98, v76, v77\n"
      "v_mfma_f32_16x16x32_bf16 v[80:83], a[60:63], a[196:199], v[80:83]\n"
      "s_waitcnt lgkmcnt(8)\n"
      "v_exp_f32 v78, v78\n"
      "s_cbranch_scc1 L_tail_%=\n"
      "v_mfma_f32_16x16x32_bf16 a[200:203], v[84:87], a[64:67], a[200:203]\n"
      "s_add_u32 m0, s23, 32768\n"
      "ds_read_b64_tr_b16 a[88:89], v15 offset:32768\n"
      "v_add_f32 v10, v10, v56\n"
      "v_mfma_f32_16x16x32_bf16 a[200:203], v[88:91], a[68:71], a[200:203]\n"
      "s_waitcnt lgkmcnt(5)\n"
      "v_exp_f32 v79, v79\n"
      "v_mfma_f32_16x16x32_bf16 a[212:215], v[84:87], a[72:75], a[212:215]\n"
      "buffer_load_dwordx4 v1, s[36:39], 0 offen offset:0 nt lds\n"
      "v_mfma_f32_16x16x32_bf16 a[212:215], v[88:91], a[76:79], a[212:215]\n"
      "s_waitcnt lgkmcnt(1)\n"
      "v_exp_f32 v80, v80\n"
      "v_mfma_f32_16x16x32_bf16 a[224:227], v[84:87], a[80:83], a[224:227]\n"
      "ds_read_b64_tr_b16 a[90:91], v15 offset:40960\n"
      "v_cvt_pk_bf16_f32 v99, v78, v79\n"
      "v_mfma_f32_16x16x32_bf16 a[224:227], v[88:91], a[84:87], a[224:227]\n"
      "v_exp_f32 v81, v81\n"
      "v_mfma_f32_16x16x32_bf16 a[204:207], v[92:95], a[64:67], a[204:207]\n"
      "ds_read_b64_tr_b16 a[92:93], v15 offset:49152\n"
      "v_cvt_pk_bf16_f32 v106, v80, v81\n"
      "v_mfma_f32_16x16x32_bf16 a[204:207], v[96:99], a[68:71], a[204:207]\n"
      "v_exp_f32 v82, v82\n"
      "v_mfma_f32_16x16x32_bf16 a[216:219], v[92:95], a[72:75], a[216:219]\n"
      "ds_read_b64_tr_b16 a[94:95], v15 offset:57344\n"
      "v_add_f32 v10, v10, v57\n"
      "v_mfma_f32_16x16x32_bf16 a[216:219], v[96:99], a[76:79], a[216:219]\n"
      "s_add_u32 m0, s23, 33792\n"
      "v_exp_f32 v83, v83\n"
      "v_mfma_f32_16x16x32_bf16 a[228:231], v[92:95], a[80:83], a[228:231]\n"
      "buffer_load_dwordx4 v1, s[36:39], s52 offen offset:0 nt lds\n"
      "v_mfma_f32_16x16x32_bf16 a[228:231], v[96:99], a[84:87], a[228:231]\n"
      "ds_read_b64_tr_b16 a[96:97], v12 offset:34816\n"
      "v_cvt_pk_bf16_f32 v107, v82, v83\n"
      "v_mfma_f32_16x16x32_bf16 a[208:211], v[100:103], a[64:67], a[208:211]\n"
      "ds_read_b64_tr_b16 a[98:99], v12 offset:43008\n"
      "v_add_f32 v10, v10, v58\n"
      "v_mfma_f32_16x16x32_bf16 a[208:211], v[104:107], a[68:71], a[208:211]\n"
      "ds_read_b64_tr_b16 a[100:101], v12 offset:51200\n"
      "v_add_f32 v10, v10, v59\n"
      "v_mfma_f32_16x16x32_bf16 a[220:223], v[100:103], a[72:75], a[220:223]\n"
      "ds_read_b64_tr_b16 a[102:103], v12 offset:59392\n"
      "v_add_f32 v6, v6, v60\n"
      "v_mfma_f32_16x16x32_bf16 a[220:223], v[104:107], a[76:79], a[220:223]\n"
      "ds_read_b64_tr_b16 a[64:65], v13 offset:34816\n"
      "v_add_f32 v6, v6, v61\n"
      "v_mfma_f32_16x16x32_bf16 a[232:235], v[100:103], a[80:83], a[232:235]\n"
      "s_add_u32 m0, s23, 34688\n"
      "ds_read_b64_tr_b16 a[66:67], v13 offset:43008\n"
      "v_add_f32 v6, v6, v62\n"
      "v_mfma_f32_16x16x32_bf16 a[232:235], v[104:107], a[84:87], a[232:235]\n"
      "s_waitcnt lgkmcnt(6)\n"
      "ds_read_b64_tr_b16 a[68:69], v13 offset:51200\n"
      "v_add_f32 v6, v6, v63\n"
      "v_mfma_f32_16x16x32_bf16 a[236:239], v[84:87], a[88:91], a[236:239]\n"
      "buffer_load_dwordx4 v1, s[36:39], 0 offen offset:128 nt lds\n"
      "v_mfma_f32_16x16x32_bf16 a[236:239], v[88:91], a[92:95], a[236:239]\n"
      "ds_read_b64_tr_b16 a[70:71], v13 offset:59392\n"
      "v_add_f32 v8, v8, v64\n"
      "v_mfma_f32_16x16x32_bf16 a[240:243], v[92:95], a[88:91], a[240:243]\n"
      "ds_read_b64_tr_b16 a[72:73], v14 offset:34816\n"
      "v_add_f32 v8, v8, v65\n"
      "v_mfma_f32_16x16x32_bf16 a[240:243], v[96:99], a[92:95], a[240:243]\n"
      "ds_read_b64_tr_b16 a[74:75], v14 offset:43008\n"
      "v_add_f32 v8, v8, v66\n"
      "v_mfma_f32_16x16x32_bf16 v[108:111], v[100:103], a[88:91], v[108:111]\n"
      "ds_read_b64_tr_b16 a[76:77], v14 offset:51200\n"
      "v_add_f32 v8, v8, v67\n"
      "v_mfma_f32_16x16x32_bf16 v[108:111], v[104:107], a[92:95], v[108:111]\n"
      "s_waitcnt lgkmcnt(7)\n"
      "ds_read_b64_tr_b16 a[78:79], v14 offset:59392\n"
      "v_add_f32 v10, v10, v68\n"
      "v_mfma_f32_16x16x32_bf16 v[112:115], v[84:87], a[96:99], v[112:115]\n"
      "ds_read_b64_tr_b16 a[80:81], v15 offset:34816\n"
      "v_add_f32 v10, v10, v69\n"
      "v_mfma_f32_16x16x32_bf16 v[112:115], v[88:91], a[100:103], v[112:115]\n"
      "ds_read_b64_tr_b16 a[82:83], v15 offset:43008\n"
      "v_add_f32 v10, v10, v70\n"
      "v_mfma_f32_16x16x32_bf16 v[116:119], v[92:95], a[96:99], v[116:119]\n"
      "ds_read_b64_tr_b16 a[84:85], v15 offset:51200\n"
      "v_add_f32 v10, v10, v71\n"
      "v_mfma_f32_16x16x32_bf16 v[116:119], v[96:99], a[100:103], v[116:119]\n"
      "ds_read_b64_tr_b16 a[86:87], v15 offset:59392\n"
      "v_add_f32 v6, v6, v72\n"
      "v_mfma_f32_16x16x32_bf16 v[120:123], v[100:103], a[96:99], v[120:123]\n"
      "s_add_u32 m0, s23, 35712\n"
      "ds_read_b64_tr_b16 a[88:89], v12 offset:36864\n"
      "v_add_f32 v6, v6, v73\n"
      "v_mfma_f32_16x16x32_bf16 v[120:123], v[104:107], a[100:103], v[120:123]\n"
      "s_waitcnt lgkmcnt(9)\n"
      "ds_read_b64_tr_b16 a[90:91], v12 offset:45056\n"
      "v_add_f32 v6, v6, v74\n"
      "v_mfma_f32_16x16x32_bf16 v[124:127], v[84:87], a[64:67], v[124:127]\n"
      "buffer_load_dwordx4 v1, s[36:39], s52 offen offset:128 nt lds\n"
      "v_mfma_f32_16x16x32_bf16 v[124:127], v[88:91], a[68:71], v[124:127]\n"
      "ds_read_b64_tr_b16 a[92:93], v12 offset:53248\n"
      "v_add_f32 v6, v6, v75\n"
      "v_mfma_f32_16x16x32_bf16 v[128:131], v[92:95], a[64:67], v[128:131]\n"
      "ds_read_b64_tr_b16 a[94:95], v12 offset:61440\n"
      "v_add_f32 v8, v8, v76\n"
      "v_mfma_f32_16x16x32_bf16 v[128:131], v[96:99], a[68:71], v[128:131]\n"
      "ds_read_b64_tr_b16 a[96:97], v13 offset:36864\n"
      "v_add_f32 v8, v8, v77\n"
      "v_mfma_f32_16x16x32_bf16 v[132:135], v[100:103], a[64:67], v[132:135]\n"
      "ds_read_b64_tr_b16 a[98:99], v13 offset:45056\n"
      "v_add_f32 v8, v8, v78\n"
      "v_mfma_f32_16x16x32_bf16 v[132:135], v[104:107], a[68:71], v[132:135]\n"
      "s_waitcnt lgkmcnt(10)\n"
      "ds_read_b64_tr_b16 a[100:101], v13 offset:53248\n"
      "v_add_f32 v8, v8, v79\n"
      "v_mfma_f32_16x16x32_bf16 v[136:139], v[84:87], a[72:75], v[136:139]\n"
      "ds_read_b64_tr_b16 a[102:103], v13 offset:61440\n"
      "v_add_f32 v10, v10, v80\n"
      "v_mfma_f32_16x16x32_bf16 v[136:139], v[88:91], a[76:79], v[136:139]\n"
      "s_add_u32 m0, s23, 36608\n"
      "ds_read_b64_tr_b16 a[64:65], v14 offset:36864\n"
      "v_add_f32 v10, v10, v81\n"
      "v_mfma_f32_16x16x32_bf16 v[140:143], v[92:95], a[72:75], v[140:143]\n"
      "buffer_load_dwordx4 v1, s[36:39], 0 offen offset:256 nt lds\n"
      "v_mfma_f32_16x16x32_bf16 v[140:143], v[96:99], a[76:79], v[140:143]\n"
      "ds_read_b64_tr_b16 a[66:67], v14 offset:45056\n"
      "v_add_f32 v10, v10, v82\n"
      "v_mfma_f32_16x16x32_bf16 v[144:147], v[100:103], a[72:75], v[144:147]\n"
      "ds_read_b64_tr_b16 a[68:69], v14 offset:53248\n"
      "v_add_f32 v10, v10, v83\n"
      "v_mfma_f32_16x16x32_bf16 v[144:147], v[104:107], a[76:79], v[144:147]\n"
      "s_waitcnt lgkmcnt(11)\n"
      "ds_read_b64_tr_b16 a[70:71], v14 offset:61440\n"
      "v_mfma_f32_16x16x32_bf16 v[148:151], v[84:87], a[80:83], v[148:151]\n"
      "ds_read_b64_tr_b16 a[72:73], v15 offset:36864\n"
      "v_mfma_f32_16x16x32_bf16 v[148:151], v[88:91], a[84:87], v[148:151]\n"
      "ds_read_b64_tr_b16 a[74:75], v15 offset:45056\n"
      "v_mfma_f32_16x16x32_bf16 v[152:155], v[92:95], a[80:83], v[152:155]\n"
      "ds_read_b64_tr_b16 a[76:77], v15 offset:53248\n"
      "v_mfma_f32_16x16x32_bf16 v[152:155], v[96:99], a[84:87], v[152:155]\n"
      "s_add_u32 m0, s23, 37632\n"
      "ds_read_b64_tr_b16 a[78:79], v15 offset:61440\n"
      "v_mfma_f32_16x16x32_bf16 v[156:159], v[100:103], a[80:83], v[156:159]\n"
      "buffer_load_dwordx4 v1, s[36:39], s52 offen offset:256 nt lds\n"
      "v_mfma_f32_16x16x32_bf16 v[156:159], v[104:107], a[84:87], v[156:159]\n"
      "s_waitcnt lgkmcnt(12)\n"
      "ds_read_b64_tr_b16 a[80:81], v12 offset:38912\n"
      "v_mfma_f32_16x16x32_bf16 v[160:163], v[84:87], a[88:91], v[160:163]\n"
      "ds_read_b64_tr_b16 a[82:83], v12 offset:47104\n"
      "v_mfma_f32_16x16x32_bf16 v[160:163], v[88:91], a[92:95], v[160:163]\n"
      "ds_read_b64_tr_b16 a[84:85], v12 offset:55296\n"
      "v_mfma_f32_16x16x32_bf16 v[164:167], v[92:95], a[88:91], v[164:167]\n"
      "ds_read_b64_tr_b16 a[86:87], v12 offset:63488\n"
      "v_mfma_f32_16x16x32_bf16 v[164:167], v[96:99], a[92:95], v[164:167]\n"
      "v_mfma_f32_16x16x32_bf16 v[168:171], v[100:103], a[88:91], v[168:171]\n"
      "s_add_u32 m0, s23, 38528\n"
      "v_mfma_f32_16x16x32_bf16 v[168:171], v[104:107], a[92:95], v[168:171]\n"
      "s_waitcnt lgkmcnt(12)\n"
      "ds_read_b64_tr_b16 a[88:89], v13 offset:38912\n"
      "v_mfma_f32_16x16x32_bf16 v[172:175], v[84:87], a[96:99], v[172:175]\n"
      "buffer_load_dwordx4 v1, s[36:39], 0 offen offset:384 nt lds\n"
      "v_mfma_f32_16x16x32_bf16 v[172:175], v[88:91], a[100:103], v[172:175]\n"
      "ds_read_b64_tr_b16 a[90:91], v13 offset:47104\n"
      "v_mfma_f32_16x16x32_bf16 v[176:179], v[92:95], a[96:99], v[176:179]\n"
      "ds_read_b64_tr_b16 a[92:93], v13 offset:55296\n"
      "v_mfma_f32_16x16x32_bf16 v[176:179], v[96:99], a[100:103], v[176:179]\n"
      "ds_read_b64_tr_b16 a[94:95], v13 offset:63488\n"
      "v_mfma_f32_16x16x32_bf16 v[180:183], v[100:103], a[96:99], v[180:183]\n"
      "v_mfma_f32_16x16x32_bf16 v[180:183], v[104:107], a[100:103], v[180:183]\n"
      "s_waitcnt lgkmcnt(12)\n"
      "ds_read_b64_tr_b16 a[96:97], v14 offset:38912\n"
      "v_mfma_f32_16x16x32_bf16 v[184:187], v[84:87], a[64:67], v[184:187]\n"
      "ds_read_b64_tr_b16 a[98:99], v14 offset:47104\n"
      "v_mfma_f32_16x16x32_bf16 v[184:187], v[88:91], a[68:71], v[184:187]\n"
      "ds_read_b64_tr_b16 a[100:101], v14 offset:55296\n"
      "v_mfma_f32_16x16x32_bf16 v[188:191], v[92:95], a[64:67], v[188:191]\n"
      "ds_read_b64_tr_b16 a[102:103], v14 offset:63488\n"
      "v_mfma_f32_16x16x32_bf16 v[188:191], v[96:99], a[68:71], v[188:191]\n"
      "v_mfma_f32_16x16x32_bf16 v[192:195], v[100:103], a[64:67], v[192:195]\n"
      "s_add_u32 m0, s23, 39552\n"
      "v_mfma_f32_16x16x32_bf16 v[192:195], v[104:107], a[68:71], v[192:195]\n"
      "s_waitcnt lgkmcnt(12)\n"
      "ds_read_b64_tr_b16 a[64:65], v15 offset:38912\n"
      "v_mfma_f32_16x16x32_bf16 v[196:199], v[84:87], a[72:75], v[196:199]\n"
      "buffer_load_dwordx4 v1, s[36:39], s52 offen offset:384 nt lds\n"
      "v_mfma_f32_16x16x32_bf16 v[196:199], v[88:91], a[76:79], v[196:199]\n"
      "ds_read_b64_tr_b16 a[66:67], v15 offset:47104\n"
      "v_add_u32 v1, 0x8000, v1\n"
      "v_mfma_f32_16x16x32_bf16 v[200:203], v[92:95], a[72:75], v[200:203]\n"
      "ds_read_b64_tr_b16 a[68:69], v15 offset:55296\n"
      "v_mfma_f32_16x16x32_bf16 v[200:203], v[96:99], a[76:79], v[200:203]\n"
      "ds_read_b64_tr_b16 a[70:71], v15 offset:63488\n"
      "v_mfma_f32_16x16x32_bf16 v[204:207], v[100:103], a[72:75], v[204:207]\n"
      "buffer_load_dword v23, v22, s[60:63], 0 offen\n"
      "v_mfma_f32_16x16x32_bf16 v[204:207], v[104:107], a[76:79], v[204:207]\n"
      "s_waitcnt lgkmcnt(12)\n"
      "v_add_u32 v22, 0x400, v22\n"
      "v_mfma_f32_16x16x32_bf16 v[208:211], v[84:87], a[80:83], v[208:211]\n"
      "v_mfma_f32_16x16x32_bf16 v[208:211], v[88:91], a[84:87], v[208:211]\n"
      "v_mfma_f32_16x16x32_bf16 v[212:215], v[92:95], a[80:83], v[212:215]\n"
      "s_cmp_eq_u32 s20, 14\n"
      "v_mfma_f32_16x16x32_bf16 v[212:215], v[96:99], a[84:87], v[212:215]\n"
      "s_cbranch_scc1 L_topD2_%=\n"
      "s_branch L_topN2_%=\n"
      ".p2align 6\n"
      "L_topD3_%=:\n"
      "v_mfma_f32_16x16x32_bf16 v[216:219], v[100:103], a[80:83], v[216:219]\n"
      "v_mfma_f32_16x16x32_bf16 v[216:219], v[104:107], a[84:87], v[216:219]\n"
      "s_waitcnt lgkmcnt(8)\n"
      "v_mfma_f32_16x16x32_bf16 v[220:223], v[84:87], a[88:91], v[220:223]\n"
      "s_waitcnt vmcnt(17) lgkmcnt(0)\n"
      "v_mfma_f32_16x16x32_bf16 v[220:223], v[88:91], a[92:95], v[220:223]\n"
      "s_barrier\n"
      "ds_read_b128 a[0:3], v3 offset:0\n"
      "v_mfma_f32_16x16x32_bf16 v[224:227], v[92:95], a[88:91], v[224:227]\n"
      "ds_read_b128 a[4:7], v4 offset:0\n"
      "v_mfma_f32_16x16x32_bf16 v[224:227], v[96:99], a[92:95], v[224:227]\n"
      "ds_read_b128 a[8:11], v3 offset:2048\n"
      "v_mfma_f32_16x16x32_bf16 v[228:231], v[100:103], a[88:91], v[228:231]\n"
      "s_add_u32 m0, s23, 98304\n"
      "ds_read_b128 a[12:15], v4 offset:2048\n"
      "v_mfma_f32_16x16x32_bf16 v[228:231], v[104:107], a[92:95], v[228:231]\n"
      "buffer_load_dwordx4 v2, s[40:43], 0 offen offset:0 lds\n"
      "v_mfma_f32_16x16x32_bf16 v[232:235], v[84:87], a[96:99], v[232:235]\n"
      "ds_read_b128 a[16:19], v3 offset:4096\n"
      "v_mfma_f32_16x16x32_bf16 v[232:235], v[88:91], a[100:103], v[232:235]\n"
      "ds_read_b128 a[20:23], v4 offset:4096\n"
      "v_mfma_f32_16x16x32_bf16 v[236:239], v[92:95], a[96:99], v[236:239]\n"
      "ds_read_b128 a[24:27], v3 offset:6144\n"
      "v_mfma_f32_16x16x32_bf16 v[236:239], v[96:99], a[100:103], v[236:239]\n"
      "ds_read_b128 a[28:31], v4 offset:6144\n"
      "v_mfma_f32_16x16x32_bf16 v[240:243], v[100:103], a[96:99], v[240:243]\n"
      "ds_read_b128 a[32:35], v3 offset:8192\n"
      "v_mfma_f32_16x16x32_bf16 v[240:243], v[104:107], a[100:103], v[240:243]\n"
      "ds_read_b128 a[36:39], v4 offset:8192\n"
      "v_mfma_f32_16x16x32_bf16 v[244:247], v[84:87], a[64:67], v[244:247]\n"
      "ds_read_b128 a[40:43], v3 offset:10240\n"
      "v_mfma_f32_16x16x32_bf16 v[244:247], v[88:91], a[68:71], v[244:247]\n"
      "ds_read_b128 a[44:47], v4 offset:10240\n"
      "v_mfma_f32_16x16x32_bf16 v[248:251], v[92:95], a[64:67], v[248:251]\n"
      "ds_read_b128 a[48:51], v3 offset:12288\n"
      "v_mfma_f32_16x16x32_bf16 v[248:251], v[96:99], a[68:71], v[248:251]\n"
      "ds_read_b128 a[52:55], v4 offset:12288\n"
      "v_mfma_f32_16x16x32_bf16 v[252:255], v[100:103], a[64:67], v[252:255]\n"
      "s_add_u32 m0, s23, 99328\n"
      "ds_read_b128 a[56:59], v3 offset:14336\n"
      "v_mfma_f32_16x16x32_bf16 v[252:255], v[104:107], a[68:71], v[252:255]\n"
      "s_waitcnt lgkmcnt(7)\n"
      "buffer_load_dwordx4 v2, s[40:43], s52 offen offset:0 lds\n"
      "v_mfma_f32_16x16x32_bf16 v[36:39], a[0:3], a[104:107], v[24:27]\n"
      "ds_read_b128 a[60:63], v4 offset:14336\n"
      "v_mfma_f32_16x16x32_bf16 v[36:39], a[4:7], a[108:111], v[36:39]\n"
      "v_mfma_f32_16x16x32_bf16 v[36:39], a[8:11], a[112:115], v[36:39]\n"
      "v_mfma_f32_16x16x32_bf16 v[36:39], a[12:15], a[116:119], v[36:39]\n"
      "v_mfma_f32_16x16x32_bf16 v[36:39], a[16:19], a[120:123], v[36:39]\n"
      "v_mfma_f32_16x16x32_bf16 v[36:39], a[20:23], a[124:127], v[36:39]\n"
      "v_mfma_f32_16x16x32_bf16 v[36:39], a[24:27], a[128:131], v[36:39]\n"
      "s_add_u32 m0, s23, 100224\n"
      "v_mfma_f32_16x16x32_bf16 v[36:39], a[28:31], a[132:135], v[36:39]\n"
      "buffer_load_dwordx4 v2, s[40:43], 0 offen offset:128 lds\n"
      "v_mfma_f32_16x16x32_bf16 v[40:43], a[0:3], a[136:139], v[28:31]\n"
      "v_mfma_f32_16x16x32_bf16 v[40:43], a[4:7], a[140:143], v[40:43]\n"
      "v_mfma_f32_16x16x32_bf16 v[40:43], a[8:11], a[144:147], v[40:43]\n"
      "v_exp_f32 v36, v36\n"
      "v_mfma_f32_16x16x32_bf16 v[40:43], a[12:15], a[148:151], v[40:43]\n"
      "v_add_f32 v6, v6, v36\n"
      "v_mfma_f32_16x16x32_bf16 v[40:43], a[16:19], a[152:155], v[40:43]\n"
      "v_exp_f32 v37, v37\n"
      "v_mfma_f32_16x16x32_bf16 v[40:43], a[20:23], a[156:159], v[40:43]\n"
      "v_cvt_pk_bf16_f32 v84, v36, v37\n"
      "v_add_f32 v6, v6, v37\n"
      "v_mfma_f32_16x16x32_bf16 v[40:43], a[24:27], a[160:163], v[40:43]\n"
      "v_exp_f32 v38, v38\n"
      "v_mfma_f32_16x16x32_bf16 v[40:43], a[28:31], a[164:167], v[40:43]\n"
      "v_add_f32 v6, v6, v38\n"
      "v_mfma_f32_16x16x32_bf16 v[44:47], a[0:3], a[168:171], v[32:35]\n"
      "v_exp_f32 v39, v39\n"
      "v_mfma_f32_16x16x32_bf16 v[44:47], a[4:7], a[172:175], v[44:47]\n"
      "ds_read_b128 a[0:3], v3 offset:16384\n"
      "v_cvt_pk_bf16_f32 v85, v38, v39\n"
      "v_mfma_f32_16x16x32_bf16 v[44:47], a[8:11], a[176:179], v[44:47]\n"
      "s_add_u32 m0, s23, 101248\n"
      "v_exp_f32 v40, v40\n"
      "v_mfma_f32_16x16x32_bf16 v[44:47], a[12:15], a[180:183], v[44:47]\n"
      "buffer_load_dwordx4 v2, s[40:43], s52 offen offset:128 lds\n"
      "v_mfma_f32_16x16x32_bf16 v[44:47], a[16:19], a[184:187], v[44:47]\n"
      "v_exp_f32 v41, v41\n"
      "v_mfma_f32_16x16x32_bf16 v[44:47], a[20:23], a[188:191], v[44:47]\n"
      "ds_read_b128 a[4:7], v4 offset:16384\n"
      "v_cvt_pk_bf16_f32 v92, v40, v41\n"
      "v_mfma_f32_16x16x32_bf16 v[44:47], a[24:27], a[192:195], v[44:47]\n"
      "v_exp_f32 v42, v42\n"
      "v_mfma_f32_16x16x32_bf16 v[44:47], a[28:31], a[196:199], v[44:47]\n"
      "s_waitcnt lgkmcnt(2)\n"
      "ds_read_b128 a[8:11], v3 offset:18432\n"
      "v_add_f32 v6, v6, v39\n"
      "v_mfma_f32_16x16x32_bf16 v[48:51], a[32:35], a[104:107], v[24:27]\n"
      "v_exp_f32 v43, v43\n"
      "v_mfma_f32_16x16x32_bf16 v[48:51], a[36:39], a[108:111], v[48:51]\n"
      "ds_read_b128 a[12:15], v4 offset:18432\n"
      "v_cvt_pk_bf16_f32 v93, v42, v43\n"
      "v_mfma_f32_16x16x32_bf16 v[48:51], a[40:43], a[112:115], v[48:51]\n"
      "s_add_u32 m0, s23, 102144\n"
      "v_exp_f32 v44, v44\n"
      "v_mfma_f32_16x16x32_bf16 v[48:51], a[44:47], a[116:119], v[48:51]\n"
      "buffer_load_dwordx4 v2, s[40:43], 0 offen offset:256 lds\n"
      "v_mfma_f32_16x16x32_bf16 v[48:51], a[48:51], a[120:123], v[48:51]\n"
      "v_exp_f32 v45, v45\n"
      "v_mfma_f32_16x16x32_bf16 v[48:51], a[52:55], a[124:127], v[48:51]\n"
      "ds_read_b128 a[16:19], v3 offset:20480\n"
      "v_cvt_pk_bf16_f32 v100, v44, v45\n"
      "v_mfma_f32_16x16x32_bf16 v[48:51], a[56:59], a[128:131], v[48:51]\n"
      "v_exp_f32 v46, v46\n"
      "v_mfma_f32_16x16x32_bf16 v[48:51], a[60:63], a[132:135], v[48:51]\n"
      "ds_read_b128 a[20:23], v4 offset:20480\n"
      "v_add_f32 v8, v8, v40\n"
      "v_mfma_f32_16x16x32_bf16 v[52:55], a[32:35], a[136:139], v[28:31]\n"
      "v_exp_f32 v47, v47\n"
      "v_mfma_f32_16x16x32_bf16 v[52:55], a[36:39], a[140:143], v[52:55]\n"
      "ds_read_b128 a[24:27], v3 offset:22528\n"
      "v_cvt_pk_bf16_f32 v101, v46, v47\n"
      "v_mfma_f32_16x16x32_bf16 v[52:55], a[40:43], a[144:147], v[52:55]\n"
      "v_exp_f32 v48, v48\n"
      "v_mfma_f32_16x16x32_bf16 v[52:55], a[44:47], a[148:151], v[52:55]\n"
      "ds_read_b128 a[28:31], v4 offset:22528\n"
      "v_add_f32 v8, v8, v41\n"
      "v_mfma_f32_16x16x32_bf16 v[52:55], a[48:51], a[152:155], v[52:55]\n"
      "v_exp_f32 v49, v49\n"
      "v_mfma_f32_16x16x32_bf16 v[52:55], a[52:55], a[156:159], v[52:55]\n"
      "v_cvt_pk_bf16_f32 v86, v48, v49\n"
      "v_add_f32 v8, v8, v42\n"
      "v_mfma_f32_16x16x32_bf16 v[52:55], a[56:59], a[160:163], v[52:55]\n"
      "s_add_u32 m0, s23, 103168\n"
      "v_exp_f32 v50, v50\n"
      "v_mfma_f32_16x16x32_bf16 v[52:55], a[60:63], a[164:167], v[52:55]\n"
      "buffer_load_dwordx4 v2, s[40:43], s52 offen offset:256 lds\n"
      "v_mfma_f32_16x16x32_bf16 v[56:59], a[32:35], a[168:171], v[32:35]\n"
      "v_exp_f32 v51, v51\n"
      "v_mfma_f32_16x16x32_bf16 v[56:59], a[36:39], a[172:175], v[56:59]\n"
      "ds_read_b128 a[32:35], v3 offset:24576\n"
      "v_cvt_pk_bf16_f32 v87, v50, v51\n"
      "v_mfma_f32_16x16x32_bf16 v[56:59], a[40:43], a[176:179], v[56:59]\n"
      "v_exp_f32 v52, v52\n"
      "v_mfma_f32_16x16x32_bf16 v[56:59], a[44:47], a[180:183], v[56:59]\n"
      "ds_read_b128 a[36:39], v4 offset:24576\n"
      "v_add_f32 v8, v8, v43\n"
      "v_mfma_f32_16x16x32_bf16 v[56:59], a[48:51], a[184:187], v[56:59]\n"
      "v_exp_f32 v53, v53\n"
      "v_mfma_f32_16x16x32_bf16 v[56:59], a[52:55], a[188:191], v[56:59]\n"
      "ds_read_b128 a[40:43], v3 offset:26624\n"
      "v_cvt_pk_bf16_f32 v94, v52, v53\n"
      "v_mfma_f32_16x16x32_bf16 v[56:59], a[56:59], a[192:195], v[56:59]\n"
      "s_add_u32 m0, s23, 104064\n"
      "v_exp_f32 v54, v54\n"
      "v_mfma_f32_16x16x32_bf16 v[56:59], a[60:63], a[196:199], v[56:59]\n"
      "s_waitcnt lgkmcnt(3)\n"
      "buffer_load_dwordx4 v2, s[40:43], 0 offen offset:384 lds\n"
      "v_mfma_f32_16x16x32_bf16 v[60:63], a[0:3], a[104:107], v[24:27]\n"
      "v_exp_f32 v55, v55\n"
      "v_mfma_f32_16x16x32_bf16 v[60:63], a[4:7], a[108:111], v[60:63]\n"
      "ds_read_b128 a[44:47], v4 offset:26624\n"
      "v_cvt_pk_bf16_f32 v95, v54, v55\n"
      "v_mfma_f32_16x16x32_bf16 v[60:63], a[8:11], a[112:115], v[60:63]\n"
      "v_exp_f32 v56, v56\n"
      "v_mfma_f32_16x16x32_bf16 v[60:63], a[12:15], a[116:119], v[60:63]\n"
      "ds_read_b128 a[48:51], v3 offset:28672\n"
      "v_add_f32 v10, v10, v44\n"
      "v_mfma_f32_16x16x32_bf16 v[60:63], a[16:19], a[120:123], v[60:63]\n"
      "v_exp_f32 v57, v57\n"
      "v_mfma_f32_16x16x32_bf16 v[60:63], a[20:23], a[124:127], v[60:63]\n"
      "ds_read_b128 a[52:55], v4 offset:28672\n"
      "v_cvt_pk_bf16_f32 v102, v56, v57\n"
      "v_mfma_f32_16x16x32_bf16 v[60:63], a[24:27], a[128:131], v[60:63]\n"
      "v_exp_f32 v58, v58\n"
      "v_mfma_f32_16x16x32_bf16 v[60:63], a[28:31], a[132:135], v[60:63]\n"
      "ds_read_b128 a[56:59], v3 offset:30720\n"
      "v_add_f32 v10, v10, v45\n"
      "v_mfma_f32_16x16x32_bf16 v[64:67], a[0:3], a[136:139], v[28:31]\n"
      "v_exp_f32 v59, v59\n"
      "v_mfma_f32_16x16x32_bf16 v[64:67], a[4:7], a[140:143], v[64:67]\n"
      "ds_read_b128 a[60:63], v4 offset:30720\n"
      "v_cvt_pk_bf16_f32 v103, v58, v59\n"
      "v_mfma_f32_16x16x32_bf16 v[64:67], a[8:11], a[144:147], v[64:67]\n"
      "s_add_u32 m0, s23, 105088\n"
      "v_exp_f32 v60, v60\n"
      "v_mfma_f32_16x16x32_bf16 v[64:67], a[12:15], a[148:151], v[64:67]\n"
      "buffer_load_dwordx4 v2, s[40:43], s52 offen offset:384 lds\n"
      "v_mfma_f32_16x16x32_bf16 v[64:67], a[16:19], a[152:155], v[64:67]\n"
      "v_exp_f32 v61, v61\n"
      "v_mfma_f32_16x16x32_bf16 v[64:67], a[20:23], a[156:159], v[64:67]\n"
      "v_cvt_pk_bf16_f32 v88, v60, v61\n"
      "v_add_u32 v2, 0x8000, v2\n"
      "v_mfma_f32_16x16x32_bf16 v[64:67], a[24:27], a[160:163], v[64:67]\n"
      "v_exp_f32 v62, v62\n"
      "v_mfma_f32_16x16x32_bf16 v[64:67], a[28:31], a[164:167], v[64:67]\n"
      "v_add_f32 v10, v10, v46\n"
      "v_add_f32 v10, v10, v47\n"
      "v_mfma_f32_16x16x32_bf16 v[68:71], a[0:3], a[168:171], v[32:35]\n"
      "v_exp_f32 v63, v63\n"
      "v_mfma_f32_16x16x32_bf16 v[68:71], a[4:7], a[172:175], v[68:71]\n"
      "v_cvt_pk_bf16_f32 v89, v62, v63\n"
      "v_add_f32 v6, v6, v48\n"
      "v_mfma_f32_16x16x32_bf16 v[68:71], a[8:11], a[176:179], v[68:71]\n"
      "s_waitcnt vmcnt(17) lgkmcnt(0)\n"
      "v_exp_f32 v64, v64\n"
      "v_mfma_f32_16x16x32_bf16 v[68:71], a[12:15], a[180:183], v[68:71]\n"
      "s_barrier\n"
      "ds_read_b64_tr_b16 a[64:65], v12 offset:0\n"
      "v_add_f32 v6, v6, v49\n"
      "v_mfma_f32_16x16x32_bf16 v[68:71], a[16:19], a[184:187], v[68:71]\n"
      "v_exp_f32 v65, v65\n"
      "v_mfma_f32_16x16x32_bf16 v[68:71], a[20:23], a[188:191], v[68:71]\n"
      "ds_read_b64_tr_b16 a[66:67], v12 offset:8192\n"
      "v_cvt_pk_bf16_f32 v96, v64, v65\n"
      "v_mfma_f32_16x16x32_bf16 v[68:71], a[24:27], a[192:195], v[68:71]\n"
      "v_exp_f32 v66, v66\n"
      "v_mfma_f32_16x16x32_bf16 v[68:71], a[28:31], a[196:199], v[68:71]\n"
      "ds_read_b64_tr_b16 a[68:69], v12 offset:16384\n"
      "v_add_f32 v6, v6, v50\n"
      "v_mfma_f32_16x16x32_bf16 v[72:75], a[32:35], a[104:107], v[24:27]\n"
      "v_exp_f32 v67, v67\n"
      "v_mfma_f32_16x16x32_bf16 v[72:75], a[36:39], a[108:111], v[72:75]\n"
      "ds_read_b64_tr_b16 a[70:71], v12 offset:24576\n"
      "v_cvt_pk_bf16_f32 v97, v66, v67\n"
      "v_mfma_f32_16x16x32_bf16 v[72:75], a[40:43], a[112:115], v[72:75]\n"
      "v_exp_f32 v68, v68\n"
      "v_mfma_f32_16x16x32_bf16 v[72:75], a[44:47], a[116:119], v[72:75]\n"
      "ds_read_b64_tr_b16 a[72:73], v13 offset:0\n"
      "v_add_f32 v6, v6, v51\n"
      "v_mfma_f32_16x16x32_bf16 v[72:75], a[48:51], a[120:123], v[72:75]\n"
      "v_exp_f32 v69, v69\n"
      "v_mfma_f32_16x16x32_bf16 v[72:75], a[52:55], a[124:127], v[72:75]\n"
      "ds_read_b64_tr_b16 a[74:75], v13 offset:8192\n"
      "v_cvt_pk_bf16_f32 v104, v68, v69\n"
      "v_mfma_f32_16x16x32_bf16 v[72:75], a[56:59], a[128:131], v[72:75]\n"
      "v_exp_f32 v70, v70\n"
      "v_mfma_f32_16x16x32_bf16 v[72:75], a[60:63], a[132:135], v[72:75]\n"
      "ds_read_b64_tr_b16 a[76:77], v13 offset:16384\n"
      "v_add_f32 v8, v8, v52\n"
      "v_mfma_f32_16x16x32_bf16 v[76:79], a[32:35], a[136:139], v[28:31]\n"
      "v_exp_f32 v71, v71\n"
      "v_mfma_f32_16x16x32_bf16 v[76:79], a[36:39], a[140:143], v[76:79]\n"
      "ds_read_b64_tr_b16 a[78:79], v13 offset:24576\n"
      "v_cvt_pk_bf16_f32 v105, v70, v71\n"
      "v_mfma_f32_16x16x32_bf16 v[76:79], a[40:43], a[144:147], v[76:79]\n"
      "v_exp_f32 v72, v72\n"
      "v_mfma_f32_16x16x32_bf16 v[76:79], a[44:47], a[148:151], v[76:79]\n"
      "ds_read_b64_tr_b16 a[80:81], v14 offset:0\n"
      "v_add_f32 v8, v8, v53\n"
      "v_mfma_f32_16x16x32_bf16 v[76:79], a[48:51], a[152:155], v[76:79]\n"
      "v_exp_f32 v73, v73\n"
      "v_mfma_f32_16x16x32_bf16 v[76:79], a[52:55], a[156:159], v[76:79]\n"
      "ds_read_b64_tr_b16 a[82:83], v14 offset:8192\n"
      "v_cvt_pk_bf16_f32 v90, v72, v73\n"
      "v_mfma_f32_16x16x32_bf16 v[76:79], a[56:59], a[160:163], v[76:79]\n"
      "v_exp_f32 v74, v74\n"
      "v_mfma_f32_16x16x32_bf16 v[76:79], a[60:63], a[164:167], v[76:79]\n"
      "ds_read_b64_tr_b16 a[84:85], v14 offset:16384\n"
      "v_add_f32 v8, v8, v54\n"
      "v_mfma_f32_16x16x32_bf16 v[80:83], a[32:35], a[168:171], v[32:35]\n"
      "v_exp_f32 v75, v75\n"
      "v_mfma_f32_16x16x32_bf16 v[80:83], a[36:39], a[172:175], v[80:83]\n"
      "ds_read_b64_tr_b16 a[86:87], v14 offset:24576\n"
      "v_cvt_pk_bf16_f32 v91, v74, v75\n"
      "v_mfma_f32_16x16x32_bf16 v[80:83], a[40:43], a[176:179], v[80:83]\n"
      "v_exp_f32 v76, v76\n"
      "v_mfma_f32_16x16x32_bf16 v[80:83], a[44:47], a[180:183], v[80:83]\n"
      "v_add_f32 v8, v8, v55\n"
      "v_mfma_f32_16x16x32_bf16 v[80:83], a[48:51], a[184:187], v[80:83]\n"
      "v_exp_f32 v77, v77\n"
      "v_mfma_f32_16x16x32_bf16 v[80:83], a[52:55], a[188:191], v[80:83]\n"
      "v_cvt_pk_bf16_f32 v98, v76, v77\n"
      "v_mfma_f32_16x16x32_bf16 v[80:83], a[56:59], a[192:195], v[80:83]\n"
      "v_exp_f32 v78, v78\n"
      "v_mfma_f32_16x16x32_bf16 v[80:83], a[60:63], a[196:199], v[80:83]\n"
      "s_waitcnt lgkmcnt(8)\n"
      "L_loopD3_%=:\n"
      "v_mfma_f32_16x16x32_bf16 a[200:203], v[84:87], a[64:67], a[200:203]\n"
      "v_exp_f32 v79, v79\n"
      "v_mfma_f32_16x16x32_bf16 a[200:203], v[88:91], a[68:71], a[200:203]\n"
      "s_waitcnt lgkmcnt(4)\n"
      "ds_read_b64_tr_b16 a[88:89], v15 offset:0\n"
      "v_cvt_pk_bf16_f32 v99, v78, v79\n"
      "v_mfma_f32_16x16x32_bf16 a[212:215], v[84:87], a[72:75], a[212:215]\n"
      "s_add_u32 m0, s23, 0\n"
      "v_exp_f32 v80, v80\n"
      "v_mfma_f32_16x16x32_bf16 a[212:215], v[88:91], a[76:79], a[212:215]\n"
      "s_waitcnt lgkmcnt(1)\n"
      "buffer_load_dwordx4 v1, s[36:39], 0 offen offset:0 lds\n"
      "v_mfma_f32_16x16x32_bf16 a[224:227], v[84:87], a[80:83], a[224:227]\n"
      "v_exp_f32 v81, v81\n"
      "v_mfma_f32_16x16x32_bf16 a[224:227], v[88:91], a[84:87], a[224:227]\n"
      "ds_read_b64_tr_b16 a[90:91], v15 offset:8192\n"
      "v_cvt_pk_bf16_f32 v106, v80, v81\n"
      "v_mfma_f32_16x16x32_bf16 a[204:207], v[92:95], a[64:67], a[204:207]\n"
      "v_exp_f32 v82, v82\n"
      "v_mfma_f32_16x16x32_bf16 a[204:207], v[96:99], a[68:71], a[204:207]\n"
      "ds_read_b64_tr_b16 a[92:93], v15 offset:16384\n"
      "v_add_f32 v10, v10, v56\n"
      "v_mfma_f32_16x16x32_bf16 a[216:219], v[92:95], a[72:75], a[216:219]\n"
      "v_exp_f32 v83, v83\n"
      "v_mfma_f32_16x16x32_bf16 a[216:219], v[96:99], a[76:79], a[216:219]\n"
      "ds_read_b64_tr_b16 a[94:95], v15 offset:24576\n"
      "v_cvt_pk_bf16_f32 v107, v82, v83\n"
      "v_mfma_f32_16x16x32_bf16 a[228:231], v[92:95], a[80:83], a[228:231]\n"
      "s_add_u32 m0, s23, 1024\n"
      "ds_read_b64_tr_b16 a[96:97], v12 offset:2048\n"
      "v_add_f32 v10, v10, v57\n"
      "v_mfma_f32_16x16x32_bf16 a[228:231], v[96:99], a[84:87], a[228:231]\n"
      "buffer_load_dwordx4 v1, s[36:39], s52 offen offset:0 lds\n"
      "v_mfma_f32_16x16x32_bf16 a[208:211], v[100:103], a[64:67], a[208:211]\n"
      "ds_read_b64_tr_b16 a[98:99], v12 offset:10240\n"
      "v_add_f32 v10, v10, v58\n"
      "v_mfma_f32_16x16x32_bf16 a[208:211], v[104:107], a[68:71], a[208:211]\n"
      "ds_read_b64_tr_b16 a[100:101], v12 offset:18432\n"
      "v_add_f32 v10, v10, v59\n"
      "v_mfma_f32_16x16x32_bf16 a[220:223], v[100:103], a[72:75], a[220:223]\n"
      "ds_read_b64_tr_b16 a[102:103], v12 offset:26624\n"
      "v_add_f32 v6, v6, v60\n"
      "v_mfma_f32_16x16x32_bf16 a[220:223], v[104:107], a[76:79], a[220:223]\n"
      "ds_read_b64_tr_b16 a[64:65], v13 offset:2048\n"
      "v_add_f32 v6, v6, v61\n"
      "v_mfma_f32_16x16x32_bf16 a[232:235], v[100:103], a[80:83], a[232:235]\n"
      "ds_read_b64_tr_b16 a[66:67], v13 offset:10240\n"
      "v_add_f32 v6, v6, v62\n"
      "v_mfma_f32_16x16x32_bf16 a[232:235], v[104:107], a[84:87], a[232:235]\n"
      "s_waitcnt lgkmcnt(6)\n"
      "ds_read_b64_tr_b16 a[68:69], v13 offset:18432\n"
      "v_add_f32 v6, v6, v63\n"
      "v_mfma_f32_16x16x32_bf16 a[236:239], v[84:87], a[88:91], a[236:239]\n"
      "s_add_u32 m0, s23, 1920\n"
      "ds_read_b64_tr_b16 a[70:71], v13 offset:26624\n"
      "v_add_f32 v8, v8, v64\n"
      "v_mfma_f32_16x16x32_bf16 a[236:239], v[88:91], a[92:95], a[236:239]\n"
      "buffer_load_dwordx4 v1, s[36:39], 0 offen offset:128 lds\n"
      "v_mfma_f32_16x16x32_bf16 a[240:243], v[92:95], a[88:91], a[240:243]\n"
      "ds_read_b64_tr_b16 a[72:73], v14 offset:2048\n"
      "v_add_f32 v8, v8, v65\n"
      "v_mfma_f32_16x16x32_bf16 a[240:243], v[96:99], a[92:95], a[240:243]\n"
      "ds_read_b64_tr_b16 a[74:75], v14 offset:10240\n"
      "v_add_f32 v8, v8, v66\n"
      "v_mfma_f32_16x16x32_bf16 v[108:111], v[100:103], a[88:91], v[108:111]\n"
      "ds_read_b64_tr_b16 a[76:77], v14 offset:18432\n"
      "v_add_f32 v8, v8, v67\n"
      "v_mfma_f32_16x16x32_bf16 v[108:111], v[104:107], a[92:95], v[108:111]\n"
      "s_waitcnt lgkmcnt(7)\n"
      "ds_read_b64_tr_b16 a[78:79], v14 offset:26624\n"
      "v_add_f32 v10, v10, v68\n"
      "v_mfma_f32_16x16x32_bf16 v[112:115], v[84:87], a[96:99], v[112:115]\n"
      "ds_read_b64_tr_b16 a[80:81], v15 offset:2048\n"
      "v_add_f32 v10, v10, v69\n"
      "v_mfma_f32_16x16x32_bf16 v[112:115], v[88:91], a[100:103], v[112:115]\n"
      "ds_read_b64_tr_b16 a[82:83], v15 offset:10240\n"
      "v_add_f32 v10, v10, v70\n"
      "v_mfma_f32_16x16x32_bf16 v[116:119], v[92:95], a[96:99], v[116:119]\n"
      "s_add_u32 m0, s23, 2944\n"
      "ds_read_b64_tr_b16 a[84:85], v15 offset:18432\n"
      "v_add_f32 v10, v10, v71\n"
      "v_mfma_f32_16x16x32_bf16 v[116:119], v[96:99], a[100:103], v[116:119]\n"
      "buffer_load_dwordx4 v1, s[36:39], s52 offen offset:128 lds\n"
      "v_mfma_f32_16x16x32_bf16 v[120:123], v[100:103], a[96:99], v[120:123]\n"
      "ds_read_b64_tr_b16 a[86:87], v15 offset:26624\n"
      "v_add_f32 v6, v6, v72\n"
      "v_mfma_f32_16x16x32_bf16 v[120:123], v[104:107], a[100:103], v[120:123]\n"
      "s_waitcnt lgkmcnt(8)\n"
      "ds_read_b64_tr_b16 a[88:89], v12 offset:4096\n"
      "v_add_f32 v6, v6, v73\n"
      "v_mfma_f32_16x16x32_bf16 v[124:127], v[84:87], a[64:67], v[124:127]\n"
      "ds_read_b64_tr_b16 a[90:91], v12 offset:12288\n"
      "v_add_f32 v6, v6, v74\n"
      "v_mfma_f32_16x16x32_bf16 v[124:127], v[88:91], a[68:71], v[124:127]\n"
      "ds_read_b64_tr_b16 a[92:93], v12 offset:20480\n"
      "v_add_f32 v6, v6, v75\n"
      "v_mfma_f32_16x16x32_bf16 v[128:131], v[92:95], a[64:67], v[128:131]\n"
      "ds_read_b64_tr_b16 a[94:95], v12 offset:28672\n"
      "v_add_f32 v8, v8, v76\n"
      "v_mfma_f32_16x16x32_bf16 v[128:131], v[96:99], a[68:71], v[128:131]\n"
      "ds_read_b64_tr_b16 a[96:97], v13 offset:4096\n"
      "v_add_f32 v8, v8, v77\n"
      "v_mfma_f32_16x16x32_bf16 v[132:135], v[100:103], a[64:67], v[132:135]\n"
      "ds_read_b64_tr_b16 a[98:99], v13 offset:12288\n"
      "v_add_f32 v8, v8, v78\n"
      "v_mfma_f32_16x16x32_bf16 v[132:135], v[104:107], a[68:71], v[132:135]\n"
      "s_waitcnt lgkmcnt(10)\n"
      "ds_read_b64_tr_b16 a[100:101], v13 offset:20480\n"
      "v_add_f32 v8, v8, v79\n"
      "v_mfma_f32_16x16x32_bf16 v[136:139], v[84:87], a[72:75], v[136:139]\n"
      "ds_read_b64_tr_b16 a[102:103], v13 offset:28672\n"
      "v_add_f32 v10, v10, v80\n"
      "v_mfma_f32_16x16x32_bf16 v[136:139], v[88:91], a[76:79], v[136:139]\n"
      "ds_read_b64_tr_b16 a[64:65], v14 offset:4096\n"
      "v_add_f32 v10, v10, v81\n"
      "v_mfma_f32_16x16x32_bf16 v[140:143], v[92:95], a[72:75], v[140:143]\n"
      "s_add_u32 m0, s23, 3840\n"
      "ds_read_b64_tr_b16 a[66:67], v14 offset:12288\n"
      "v_add_f32 v10, v10, v82\n"
      "v_mfma_f32_16x16x32_bf16 v[140:143], v[96:99], a[76:79], v[140:143]\n"
      "buffer_load_dwordx4 v1, s[36:39], 0 offen offset:256 lds\n"
      "v_mfma_f32_16x16x32_bf16 v[144:147], v[100:103], a[72:75], v[144:147]\n"
      "ds_read_b64_tr_b16 a[68:69], v14 offset:20480\n"
      "v_add_f32 v10, v10, v83\n"
      "v_mfma_f32_16x16x32_bf16 v[144:147], v[104:107], a[76:79], v[144:147]\n"
      "s_waitcnt lgkmcnt(11)\n"
      "ds_read_b64_tr_b16 a[70:71], v14 offset:28672\n"
      "v_mfma_f32_16x16x32_bf16 v[148:151], v[84:87], a[80:83], v[148:151]\n"
      "ds_read_b64_tr_b16 a[72:73], v15 offset:4096\n"
      "v_mfma_f32_16x16x32_bf16 v[148:151], v[88:91], a[84:87], v[148:151]\n"
      "ds_read_b64_tr_b16 a[74:75], v15 offset:12288\n"
      "v_mfma_f32_16x16x32_bf16 v[152:155], v[92:95], a[80:83], v[152:155]\n"
      "ds_read_b64_tr_b16 a[76:77], v15 offset:20480\n"
      "v_mfma_f32_16x16x32_bf16 v[152:155], v[96:99], a[84:87], v[152:155]\n"
      "ds_read_b64_tr_b16 a[78:79], v15 offset:28672\n"
      "v_mfma_f32_16x16x32_bf16 v[156:159], v[100:103], a[80:83], v[156:159]\n"
      "s_add_u32 m0, s23, 4864\n"
      "v_mfma_f32_16x16x32_bf16 v[156:159], v[104:107], a[84:87], v[156:159]\n"
      "s_waitcnt lgkmcnt(12)\n"
      "buffer_load_dwordx4 v1, s[36:39], s52 offen offset:256 lds\n"
      "v_mfma_f32_16x16x32_bf16 v[160:163], v[84:87], a[88:91], v[160:163]\n"
      "ds_read_b64_tr_b16 a[80:81], v12 offset:6144\n"
      "v_mfma_f32_16x16x32_bf16 v[160:163], v[88:91], a[92:95], v[160:163]\n"
      "ds_read_b64_tr_b16 a[82:83], v12 offset:14336\n"
      "v_mfma_f32_16x16x32_bf16 v[164:167], v[92:95], a[88:91], v[164:167]\n"
      "ds_read_b64_tr_b16 a[84:85], v12 offset:22528\n"
      "v_mfma_f32_16x16x32_bf16 v[164:167], v[96:99], a[92:95], v[164:167]\n"
      "ds_read_b64_tr_b16 a[86:87], v12 offset:30720\n"
      "v_mfma_f32_16x16x32_bf16 v[168:171], v[100:103], a[88:91], v[168:171]\n"
      "v_mfma_f32_16x16x32_bf16 v[168:171], v[104:107], a[92:95], v[168:171]\n"
      "s_waitcnt lgkmcnt(12)\n"
      "ds_read_b64_tr_b16 a[88:89], v13 offset:6144\n"
      "v_mfma_f32_16x16x32_bf16 v[172:175], v[84:87], a[96:99], v[172:175]\n"
      "s_add_u32 m0, s23, 5760\n"
      "ds_read_b64_tr_b16 a[90:91], v13 offset:14336\n"
      "v_mfma_f32_16x16x32_bf16 v[172:175], v[88:91], a[100:103], v[172:175]\n"
      "buffer_load_dwordx4 v1, s[36:39], 0 offen offset:384 lds\n"
      "v_mfma_f32_16x16x32_bf16 v[176:179], v[92:95], a[96:99], v[176:179]\n"
      "ds_read_b64_tr_b16 a[92:93], v13 offset:22528\n"
      "v_mfma_f32_16x16x32_bf16 v[176:179], v[96:99], a[100:103], v[176:179]\n"
      "ds_read_b64_tr_b16 a[94:95], v13 offset:30720\n"
      "v_mfma_f32_16x16x32_bf16 v[180:183], v[100:103], a[96:99], v[180:183]\n"
      "v_mfma_f32_16x16x32_bf16 v[180:183], v[104:107], a[100:103], v[180:183]\n"
      "s_waitcnt lgkmcnt(12)\n"
      "ds_read_b64_tr_b16 a[96:97], v14 offset:6144\n"
      "v_mfma_f32_16x16x32_bf16 v[184:187], v[84:87], a[64:67], v[184:187]\n"
      "ds_read_b64_tr_b16 a[98:99], v14 offset:14336\n"
      "v_mfma_f32_16x16x32_bf16 v[184:187], v[88:91], a[68:71], v[184:187]\n"
      "ds_read_b64_tr_b16 a[100:101], v14 offset:22528\n"
      "v_mfma_f32_16x16x32_bf16 v[188:191], v[92:95], a[64:67], v[188:191]\n"
      "s_add_u32 m0, s23, 6784\n"
      "ds_read_b64_tr_b16 a[102:103], v14 offset:30720\n"
      "v_mfma_f32_16x16x32_bf16 v[188:191], v[96:99], a[68:71], v[188:191]\n"
      "buffer_load_dwordx4 v1, s[36:39], s52 offen offset:384 lds\n"
      "v_mfma_f32_16x16x32_bf16 v[192:195], v[100:103], a[64:67], v[192:195]\n"
      "v_add_u32 v1, 0x8000, v1\n"
      "v_mfma_f32_16x16x32_bf16 v[192:195], v[104:107], a[68:71], v[192:195]\n"
      "s_waitcnt lgkmcnt(12)\n"
      "ds_read_b64_tr_b16 a[64:65], v15 offset:6144\n"
      "v_mfma_f32_16x16x32_bf16 v[196:199], v[84:87], a[72:75], v[196:199]\n"
      "ds_read_b64_tr_b16 a[66:67], v15 offset:14336\n"
      "v_mfma_f32_16x16x32_bf16 v[196:199], v[88:91], a[76:79], v[196:199]\n"
      "buffer_load_dword v23, v22, s[60:63], 0 offen\n"
      "v_mfma_f32_16x16x32_bf16 v[200:203], v[92:95], a[72:75], v[200:203]\n"
      "ds_read_b64_tr_b16 a[68:69], v15 offset:22528\n"
      "v_add_u32 v22, 0x400, v22\n"
      "v_mfma_f32_16x16x32_bf16 v[200:203], v[96:99], a[76:79], v[200:203]\n"
      "ds_read_b64_tr_b16 a[70:71], v15 offset:30720\n"
      "v_mfma_f32_16x16x32_bf16 v[204:207], v[100:103], a[72:75], v[204:207]\n"
      "v_mfma_f32_16x16x32_bf16 v[204:207], v[104:107], a[76:79], v[204:207]\n"
      "s_waitcnt lgkmcnt(12)\n"
      "v_mfma_f32_16x16x32_bf16 v[208:211], v[84:87], a[80:83], v[208:211]\n"
      "v_mfma_f32_16x16x32_bf16 v[208:211], v[88:91], a[84:87], v[208:211]\n"
      "s_getreg_b32 s29, hwreg(HW_REG_IB_STS, 0, 4)\n"
      "v_mfma_f32_16x16x32_bf16 v[212:215], v[92:95], a[80:83], v[212:215]\n"
      "s_cmp_ge_u32 s29, 10\n"
      "v_mfma_f32_16x16x32_bf16 v[212:215], v[96:99], a[84:87], v[212:215]\n"
      "s_cbranch_scc1 L_midN3_%=\n"
      "v_mfma_f32_16x16x32_bf16 v[216:219], v[100:103], a[80:83], v[216:219]\n"
      "v_mfma_f32_16x16x32_bf16 v[216:219], v[104:107], a[84:87], v[216:219]\n"
      "s_waitcnt lgkmcnt(8)\n"
      "v_mfma_f32_16x16x32_bf16 v[220:223], v[84:87], a[88:91], v[220:223]\n"
      "s_waitcnt vmcnt(17) lgkmcnt(0)\n"
      "v_mfma_f32_16x16x32_bf16 v[220:223], v[88:91], a[92:95], v[220:223]\n"
      "s_barrier\n"
      "ds_read_b128 a[0:3], v3 offset:32768\n"
      "v_mfma_f32_16x16x32_bf16 v[224:227], v[92:95], a[88:91], v[224:227]\n"
      "ds_read_b128 a[4:7], v4 offset:32768\n"
      "v_mfma_f32_16x16x32_bf16 v[224:227], v[96:99], a[92:95], v[224:227]\n"
      "ds_read_b128 a[8:11], v3 offset:34816\n"
      "v_mfma_f32_16x16x32_bf16 v[228:231], v[100:103], a[88:91], v[228:231]\n"
      "s_add_u32 m0, s23, 65536\n"
      "ds_read_b128 a[12:15], v4 offset:34816\n"
      "v_mfma_f32_16x16x32_bf16 v[228:231], v[104:107], a[92:95], v[228:231]\n"
      "buffer_load_dwordx4 v2, s[40:43], 0 offen offset:0 lds\n"
      "v_mfma_f32_16x16x32_bf16 v[232:235], v[84:87], a[96:99], v[232:235]\n"
      "ds_read_b128 a[16:19], v3 offset:36864\n"
      "v_mfma_f32_16x16x32_bf16 v[232:235], v[88:91], a[100:103], v[232:235]\n"
      "ds_read_b128 a[20:23], v4 offset:36864\n"
      "v_mfma_f32_16x16x32_bf16 v[236:239], v[92:95], a[96:99], v[236:239]\n"
      "ds_read_b128 a[24:27], v3 offset:38912\n"
      "v_mfma_f32_16x16x32_bf16 v[236:239], v[96:99], a[100:103], v[236:239]\n"
      "ds_read_b128 a[28:31], v4 offset:38912\n"
      "v_mfma_f32_16x16x32_bf16 v[240:243], v[100:103], a[96:99], v[240:243]\n"
      "ds_read_b128 a[32:35], v3 offset:40960\n"
      "v_mfma_f32_16x16x32_bf16 v[240:243], v[104:107], a[100:103], v[240:243]\n"
      "ds_read_b128 a[36:39], v4 offset:40960\n"
      "v_mfma_f32_16x16x32_bf16 v[244:247], v[84:87], a[64:67], v[244:247]\n"
      "ds_read_b128 a[40:43], v3 offset:43008\n"
      "v_mfma_f32_16x16x32_bf16 v[244:247], v[88:91], a[68:71], v[244:247]\n"
      "ds_read_b128 a[44:47], v4 offset:43008\n"
      "v_mfma_f32_16x16x32_bf16 v[248:251], v[92:95], a[64:67], v[248:251]\n"
      "ds_read_b128 a[48:51], v3 offset:45056\n"
      "v_mfma_f32_16x16x32_bf16 v[248:251], v[96:99], a[68:71], v[248:251]\n"
      "ds_read_b128 a[52:55], v4 offset:45056\n"
      "v_mfma_f32_16x16x32_bf16 v[252:255], v[100:103], a[64:67], v[252:255]\n"
      "s_add_u32 m0, s23, 66560\n"
      "ds_read_b128 a[56:59], v3 offset:47104\n"
      "v_mfma_f32_16x16x32_bf16 v[252:255], v[104:107], a[68:71], v[252:255]\n"
      "s_waitcnt lgkmcnt(7)\n"
      "buffer_load_dwordx4 v2, s[40:43], s52 offen offset:0 lds\n"
      "v_mfma_f32_16x16x32_bf16 v[36:39], a[0:3], a[104:107], v[24:27]\n"
      "ds_read_b128 a[60:63], v4 offset:47104\n"
      "v_mfma_f32_16x16x32_bf16 v[36:39], a[4:7], a[108:111], v[36:39]\n"
      "v_mfma_f32_16x16x32_bf16 v[36:39], a[8:11], a[112:115], v[36:39]\n"
      "v_mfma_f32_16x16x32_bf16 v[36:39], a[12:15], a[116:119], v[36:39]\n"
      "v_mfma_f32_16x16x32_bf16 v[36:39], a[16:19], a[120:123], v[36:39]\n"
      "v_mfma_f32_16x16x32_bf16 v[36:39], a[20:23], a[124:127], v[36:39]\n"
      "v_mfma_f32_16x16x32_bf16 v[36:39], a[24:27], a[128:131], v[36:39]\n"
      "s_add_u32 m0, s23, 67456\n"
      "v_mfma_f32_16x16x32_bf16 v[36:39], a[28:31], a[132:135], v[36:39]\n"
      "buffer_load_dwordx4 v2, s[40:43], 0 offen offset:128 lds\n"
      "v_mfma_f32_16x16x32_bf16 v[40:43], a[0:3], a[136:139], v[28:31]\n"
      "v_mfma_f32_16x16x32_bf16 v[40:43], a[4:7], a[140:143], v[40:43]\n"
      "v_mfma_f32_16x16x32_bf16 v[40:43], a[8:11], a[144:147], v[40:43]\n"
      "v_exp_f32 v36, v36\n"
      "v_mfma_f32_16x16x32_bf16 v[40:43], a[12:15], a[148:151], v[40:43]\n"
      "v_add_f32 v6, v6, v36\n"
      "v_mfma_f32_16x16x32_bf16 v[40:43], a[16:19], a[152:155], v[40:43]\n"
      "v_exp_f32 v37, v37\n"
      "v_mfma_f32_16x16x32_bf16 v[40:43], a[20:23], a[156:159], v[40:43]\n"
      "v_cvt_pk_bf16_f32 v84, v36, v37\n"
      "v_add_f32 v6, v6, v37\n"
      "v_mfma_f32_16x16x32_bf16 v[40:43], a[24:27], a[160:163], v[40:43]\n"
      "v_exp_f32 v38, v38\n"
      "v_mfma_f32_16x16x32_bf16 v[40:43], a[28:31], a[164:167], v[40:43]\n"
      "v_add_f32 v6, v6, v38\n"
      "v_mfma_f32_16x16x32_bf16 v[44:47], a[0:3], a[168:171], v[32:35]\n"
      "v_exp_f32 v39, v39\n"
      "v_mfma_f32_16x16x32_bf16 v[44:47], a[4:7], a[172:175], v[44:47]\n"
      "ds_read_b128 a[0:3], v3 offset:49152\n"
      "v_cvt_pk_bf16_f32 v85, v38, v39\n"
      "v_mfma_f32_16x16x32_bf16 v[44:47], a[8:11], a[176:179], v[44:47]\n"
      "s_add_u32 m0, s23, 68480\n"
      "v_exp_f32 v40, v40\n"
      "v_mfma_f32_16x16x32_bf16 v[44:47], a[12:15], a[180:183], v[44:47]\n"
      "buffer_load_dwordx4 v2, s[40:43], s52 offen offset:128 lds\n"
      "v_mfma_f32_16x16x32_bf16 v[44:47], a[16:19], a[184:187], v[44:47]\n"
      "v_exp_f32 v41, v41\n"
      "v_mfma_f32_16x16x32_bf16 v[44:47], a[20:23], a[188:191], v[44:47]\n"
      "ds_read_b128 a[4:7], v4 offset:49152\n"
      "v_cvt_pk_bf16_f32 v92, v40, v41\n"
      "v_mfma_f32_16x16x32_bf16 v[44:47], a[24:27], a[192:195], v[44:47]\n"
      "v_exp_f32 v42, v42\n"
      "v_mfma_f32_16x16x32_bf16 v[44:47], a[28:31], a[196:199], v[44:47]\n"
      "s_waitcnt lgkmcnt(2)\n"
      "ds_read_b128 a[8:11], v3 offset:51200\n"
      "v_add_f32 v6, v6, v39\n"
      "v_mfma_f32_16x16x32_bf16 v[48:51], a[32:35], a[104:107], v[24:27]\n"
      "v_exp_f32 v43, v43\n"
      "v_mfma_f32_16x16x32_bf16 v[48:51], a[36:39], a[108:111], v[48:51]\n"
      "ds_read_b128 a[12:15], v4 offset:51200\n"
      "v_cvt_pk_bf16_f32 v93, v42, v43\n"
      "v_mfma_f32_16x16x32_bf16 v[48:51], a[40:43], a[112:115], v[48:51]\n"
      "s_add_u32 m0, s23, 69376\n"
      "v_exp_f32 v44, v44\n"
      "v_mfma_f32_16x16x32_bf16 v[48:51], a[44:47], a[116:119], v[48:51]\n"
      "buffer_load_dwordx4 v2, s[40:43], 0 offen offset:256 lds\n"
      "v_mfma_f32_16x16x32_bf16 v[48:51], a[48:51], a[120:123], v[48:51]\n"
      "v_exp_f32 v45, v45\n"
      "v_mfma_f32_16x16x32_bf16 v[48:51], a[52:55], a[124:127], v[48:51]\n"
      "ds_read_b128 a[16:19], v3 offset:53248\n"
      "v_cvt_pk_bf16_f32 v100, v44, v45\n"
      "v_mfma_f32_16x16x32_bf16 v[48:51], a[56:59], a[128:131], v[48:51]\n"
      "v_exp_f32 v46, v46\n"
      "v_mfma_f32_16x16x32_bf16 v[48:51], a[60:63], a[132:135], v[48:51]\n"
      "ds_read_b128 a[20:23], v4 offset:53248\n"
      "v_add_f32 v8, v8, v40\n"
      "v_mfma_f32_16x16x32_bf16 v[52:55], a[32:35], a[136:139], v[28:31]\n"
      "v_exp_f32 v47, v47\n"
      "v_mfma_f32_16x16x32_bf16 v[52:55], a[36:39], a[140:143], v[52:55]\n"
      "ds_read_b128 a[24:27], v3 offset:55296\n"
      "v_cvt_pk_bf16_f32 v101, v46, v47\n"
      "v_mfma_f32_16x16x32_bf16 v[52:55], a[40:43], a[144:147], v[52:55]\n"
      "v_exp_f32 v48, v48\n"
      "v_mfma_f32_16x16x32_bf16 v[52:55], a[44:47], a[148:151], v[52:55]\n"
      "ds_read_b128 a[28:31], v4 offset:55296\n"
      "v_add_f32 v8, v8, v41\n"
      "v_mfma_f32_16x16x32_bf16 v[52:55], a[48:51], a[152:155], v[52:55]\n"
      "v_exp_f32 v49, v49\n"
      "v_mfma_f32_16x16x32_bf16 v[52:55], a[52:55], a[156:159], v[52:55]\n"
      "v_cvt_pk_bf16_f32 v86, v48, v49\n"
      "v_add_f32 v8, v8, v42\n"
      "v_mfma_f32_16x16x32_bf16 v[52:55], a[56:59], a[160:163], v[52:55]\n"
      "s_add_u32 m0, s23, 70400\n"
      "v_exp_f32 v50, v50\n"
      "v_mfma_f32_16x16x32_bf16 v[52:55], a[60:63], a[164:167], v[52:55]\n"
      "buffer_load_dwordx4 v2, s[40:43], s52 offen offset:256 lds\n"
      "v_mfma_f32_16x16x32_bf16 v[56:59], a[32:35], a[168:171], v[32:35]\n"
      "v_exp_f32 v51, v51\n"
      "v_mfma_f32_16x16x32_bf16 v[56:59], a[36:39], a[172:175], v[56:59]\n"
      "ds_read_b128 a[32:35], v3 offset:57344\n"
      "v_cvt_pk_bf16_f32 v87, v50, v51\n"
      "v_mfma_f32_16x16x32_bf16 v[56:59], a[40:43], a[176:179], v[56:59]\n"
      "v_exp_f32 v52, v52\n"
      "v_mfma_f32_16x16x32_bf16 v[56:59], a[44:47], a[180:183], v[56:59]\n"
      "ds_read_b128 a[36:39], v4 offset:57344\n"
      "v_add_f32 v8, v8, v43\n"
      "v_mfma_f32_16x16x32_bf16 v[56:59], a[48:51], a[184:187], v[56:59]\n"
      "v_exp_f32 v53, v53\n"
      "v_mfma_f32_16x16x32_bf16 v[56:59], a[52:55], a[188:191], v[56:59]\n"
      "ds_read_b128 a[40:43], v3 offset:59392\n"
      "v_cvt_pk_bf16_f32 v94, v52, v53\n"
      "v_mfma_f32_16x16x32_bf16 v[56:59], a[56:59], a[192:195], v[56:59]\n"
      "s_add_u32 m0, s23, 71296\n"
      "v_exp_f32 v54, v54\n"
      "v_mfma_f32_16x16x32_bf16 v[56:59], a[60:63], a[196:199], v[56:59]\n"
      "s_waitcnt lgkmcnt(3)\n"
      "buffer_load_dwordx4 v2, s[40:43], 0 offen offset:384 lds\n"
      "v_mfma_f32_16x16x32_bf16 v[60:63], a[0:3], a[104:107], v[24:27]\n"
      "v_exp_f32 v55, v55\n"
      "v_mfma_f32_16x16x32_bf16 v[60:63], a[4:7], a[108:111], v[60:63]\n"
      "ds_read_b128 a[44:47], v4 offset:59392\n"
      "v_cvt_pk_bf16_f32 v95, v54, v55\n"
      "v_mfma_f32_16x16x32_bf16 v[60:63], a[8:11], a[112:115], v[60:63]\n"
      "v_exp_f32 v56, v56\n"
      "v_mfma_f32_16x16x32_bf16 v[60:63], a[12:15], a[116:119], v[60:63]\n"
      "ds_read_b128 a[48:51], v3 offset:61440\n"
      "v_add_f32 v10, v10, v44\n"
      "v_mfma_f32_16x16x32_bf16 v[60:63], a[16:19], a[120:123], v[60:63]\n"
      "v_exp_f32 v57, v57\n"
      "v_mfma_f32_16x16x32_bf16 v[60:63], a[20:23], a[124:127], v[60:63]\n"
      "ds_read_b128 a[52:55], v4 offset:61440\n"
      "v_cvt_pk_bf16_f32 v102, v56, v57\n"
      "v_mfma_f32_16x16x32_bf16 v[60:63], a[24:27], a[128:131], v[60:63]\n"
      "v_exp_f32 v58, v58\n"
      "v_mfma_f32_16x16x32_bf16 v[60:63], a[28:31], a[132:135], v[60:63]\n"
      "ds_read_b128 a[56:59], v3 offset:63488\n"
      "v_add_f32 v10, v10, v45\n"
      "v_mfma_f32_16x16x32_bf16 v[64:67], a[0:3], a[136:139], v[28:31]\n"
      "v_exp_f32 v59, v59\n"
      "v_mfma_f32_16x16x32_bf16 v[64:67], a[4:7], a[140:143], v[64:67]\n"
      "ds_read_b128 a[60:63], v4 offset:63488\n"
      "v_cvt_pk_bf16_f32 v103, v58, v59\n"
      "v_mfma_f32_16x16x32_bf16 v[64:67], a[8:11], a[144:147], v[64:67]\n"
      "s_add_u32 m0, s23, 72320\n"
      "v_exp_f32 v60, v60\n"
      "v_mfma_f32_16x16x32_bf16 v[64:67], a[12:15], a[148:151], v[64:67]\n"
      "buffer_load_dwordx4 v2, s[40:43], s52 offen offset:384 lds\n"
      "v_mfma_f32_16x16x32_bf16 v[64:67], a[16:19], a[152:155], v[64:67]\n"
      "v_exp_f32 v61, v61\n"
      "v_mfma_f32_16x16x32_bf16 v[64:67], a[20:23], a[156:159], v[64:67]\n"
      "v_cvt_pk_bf16_f32 v88, v60, v61\n"
      "v_add_u32 v2, 0x8000, v2\n"
      "v_mfma_f32_16x16x32_bf16 v[64:67], a[24:27], a[160:163], v[64:67]\n"
      "v_exp_f32 v62, v62\n"
      "v_mfma_f32_16x16x32_bf16 v[64:67], a[28:31], a[164:167], v[64:67]\n"
      "v_add_f32 v10, v10, v46\n"
      "v_add_f32 v10, v10, v47\n"
      "v_mfma_f32_16x16x32_bf16 v[68:71], a[0:3], a[168:171], v[32:35]\n"
      "v_exp_f32 v63, v63\n"
      "v_mfma_f32_16x16x32_bf16 v[68:71], a[4:7], a[172:175], v[68:71]\n"
      "v_cvt_pk_bf16_f32 v89, v62, v63\n"
      "v_add_f32 v6, v6, v48\n"
      "v_mfma_f32_16x16x32_bf16 v[68:71], a[8:11], a[176:179], v[68:71]\n"
      "s_waitcnt vmcnt(17) lgkmcnt(0)\n"
      "v_exp_f32 v64, v64\n"
      "v_mfma_f32_16x16x32_bf16 v[68:71], a[12:15], a[180:183], v[68:71]\n"
      "s_barrier\n"
      "ds_read_b64_tr_b16 a[64:65], v12 offset:32768\n"
      "v_add_f32 v6, v6, v49\n"
      "v_mfma_f32_16x16x32_bf16 v[68:71], a[16:19], a[184:187], v[68:71]\n"
      "v_exp_f32 v65, v65\n"
      "v_mfma_f32_16x16x32_bf16 v[68:71], a[20:23], a[188:191], v[68:71]\n"
      "ds_read_b64_tr_b16 a[66:67], v12 offset:40960\n"
      "v_cvt_pk_bf16_f32 v96, v64, v65\n"
      "v_mfma_f32_16x16x32_bf16 v[68:71], a[24:27], a[192:195], v[68:71]\n"
      "v_exp_f32 v66, v66\n"
      "v_mfma_f32_16x16x32_bf16 v[68:71], a[28:31], a[196:199], v[68:71]\n"
      "ds_read_b64_tr_b16 a[68:69], v12 offset:49152\n"
      "v_add_f32 v6, v6, v50\n"
      "v_mfma_f32_16x16x32_bf16 v[72:75], a[32:35], a[104:107], v[24:27]\n"
      "v_exp_f32 v67, v67\n"
      "v_mfma_f32_16x16x32_bf16 v[72:75], a[36:39], a[108:111], v[72:75]\n"
      "ds_read_b64_tr_b16 a[70:71], v12 offset:57344\n"
      "v_cvt_pk_bf16_f32 v97, v66, v67\n"
      "v_mfma_f32_16x16x32_bf16 v[72:75], a[40:43], a[112:115], v[72:75]\n"
      "v_exp_f32 v68, v68\n"
      "v_mfma_f32_16x16x32_bf16 v[72:75], a[44:47], a[116:119], v[72:75]\n"
      "ds_read_b64_tr_b16 a[72:73], v13 offset:32768\n"
      "v_add_f32 v6, v6, v51\n"
      "v_mfma_f32_16x16x32_bf16 v[72:75], a[48:51], a[120:123], v[72:75]\n"
      "v_exp_f32 v69, v69\n"
      "v_mfma_f32_16x16x32_bf16 v[72:75], a[52:55], a[124:127], v[72:75]\n"
      "ds_read_b64_tr_b16 a[74:75], v13 offset:40960\n"
      "v_cvt_pk_bf16_f32 v104, v68, v69\n"
      "v_mfma_f32_16x16x32_bf16 v[72:75], a[56:59], a[128:131], v[72:75]\n"
      "v_exp_f32 v70, v70\n"
      "v_mfma_f32_16x16x32_bf16 v[72:75], a[60:63], a[132:135], v[72:75]\n"
      "ds_read_b64_tr_b16 a[76:77], v13 offset:49152\n"
      "v_add_f32 v8, v8, v52\n"
      "v_mfma_f32_16x16x32_bf16 v[76:79], a[32:35], a[136:139], v[28:31]\n"
      "v_exp_f32 v71, v71\n"
      "v_mfma_f32_16x16x32_bf16 v[76:79], a[36:39], a[140:143], v[76:79]\n"
      "ds_read_b64_tr_b16 a[78:79], v13 offset:57344\n"
      "v_cvt_pk_bf16_f32 v105, v70, v71\n"
      "v_mfma_f32_16x16x32_bf16 v[76:79], a[40:43], a[144:147], v[76:79]\n"
      "v_exp_f32 v72, v72\n"
      "v_mfma_f32_16x16x32_bf16 v[76:79], a[44:47], a[148:151], v[76:79]\n"
      "ds_read_b64_tr_b16 a[80:81], v14 offset:32768\n"
      "v_add_f32 v8, v8, v53\n"
      "v_mfma_f32_16x16x32_bf16 v[76:79], a[48:51], a[152:155], v[76:79]\n"
      "v_exp_f32 v73, v73\n"
      "v_mfma_f32_16x16x32_bf16 v[76:79], a[52:55], a[156:159], v[76:79]\n"
      "ds_read_b64_tr_b16 a[82:83], v14 offset:40960\n"
      "v_cvt_pk_bf16_f32 v90, v72, v73\n"
      "v_mfma_f32_16x16x32_bf16 v[76:79], a[56:59], a[160:163], v[76:79]\n"
      "v_exp_f32 v74, v74\n"
      "v_mfma_f32_16x16x32_bf16 v[76:79], a[60:63], a[164:167], v[76:79]\n"
      "ds_read_b64_tr_b16 a[84:85], v14 offset:49152\n"
      "v_add_f32 v8, v8, v54\n"
      "v_mfma_f32_16x16x32_bf16 v[80:83], a[32:35], a[168:171], v[32:35]\n"
      "v_exp_f32 v75, v75\n"
      "v_mfma_f32_16x16x32_bf16 v[80:83], a[36:39], a[172:175], v[80:83]\n"
      "ds_read_b64_tr_b16 a[86:87], v14 offset:57344\n"
      "v_cvt_pk_bf16_f32 v91, v74, v75\n"
      "v_mfma_f32_16x16x32_bf16 v[80:83], a[40:43], a[176:179], v[80:83]\n"
      "v_exp_f32 v76, v76\n"
      "v_mfma_f32_16x16x32_bf16 v[80:83], a[44:47], a[180:183], v[80:83]\n"
      "v_add_f32 v8, v8, v55\n"
      "v_mfma_f32_16x16x32_bf16 v[80:83], a[48:51], a[184:187], v[80:83]\n"
      "v_exp_f32 v77, v77\n"
      "v_mfma_f32_16x16x32_bf16 v[80:83], a[52:55], a[188:191], v[80:83]\n"
      "v_cvt_pk_bf16_f32 v98, v76, v77\n"
      "v_mfma_f32_16x16x32_bf16 v[80:83], a[56:59], a[192:195], v[80:83]\n"
      "s_sub_u32 s20, s20, 1\n"
      "v_exp_f32 v78, v78\n"
      "v_mfma_f32_16x16x32_bf16 v[80:83], a[60:63], a[196:199], v[80:83]\n"
      "s_waitcnt lgkmcnt(8)\n"
      "s_cbranch_scc1 L_tail_%=\n"
      "v_mfma_f32_16x16x32_bf16 a[200:203], v[84:87], a[64:67], a[200:203]\n"
      "v_exp_f32 v79, v79\n"
      "v_mfma_f32_16x16x32_bf16 a[200:203], v[88:91], a[68:71], a[200:203]\n"
      "s_waitcnt lgkmcnt(4)\n"
      "ds_read_b64_tr_b16 a[88:89], v15 offset:32768\n"
      "v_cvt_pk_bf16_f32 v99, v78, v79\n"
      "v_mfma_f32_16x16x32_bf16 a[212:215], v[84:87], a[72:75], a[212:215]\n"
      "s_add_u32 m0, s23, 32768\n"
      "v_exp_f32 v80, v80\n"
      "v_mfma_f32_16x16x32_bf16 a[212:215], v[88:91], a[76:79], a[212:215]\n"
      "s_waitcnt lgkmcnt(1)\n"
      "buffer_load_dwordx4 v1, s[36:39], 0 offen offset:0 lds\n"
      "v_mfma_f32_16x16x32_bf16 a[224:227], v[84:87], a[80:83], a[224:227]\n"
      "v_exp_f32 v81, v81\n"
      "v_mfma_f32_16x16x32_bf16 a[224:227], v[88:91], a[84:87], a[224:227]\n"
      "ds_read_b64_tr_b16 a[90:91], v15 offset:40960\n"
      "v_cvt_pk_bf16_f32 v106, v80, v81\n"
      "v_mfma_f32_16x16x32_bf16 a[204:207], v[92:95], a[64:67], a[204:207]\n"
      "v_exp_f32 v82, v82\n"
      "v_mfma_f32_16x16x32_bf16 a[204:207], v[96:99], a[68:71], a[204:207]\n"
      "ds_read_b64_tr_b16 a[92:93], v15 offset:49152\n"
      "v_add_f32 v10, v10, v56\n"
      "v_mfma_f32_16x16x32_bf16 a[216:219], v[92:95], a[72:75], a[216:219]\n"
      "v_exp_f32 v83, v83\n"
      "v_mfma_f32_16x16x32_bf16 a[216:219], v[96:99], a[76:79], a[216:219]\n"
      "ds_read_b64_tr_b16 a[94:95], v15 offset:57344\n"
      "v_cvt_pk_bf16_f32 v107, v82, v83\n"
      "v_mfma_f32_16x16x32_bf16 a[228:231], v[92:95], a[80:83], a[228:231]\n"
      "s_add_u32 m0, s23, 33792\n"
      "ds_read_b64_tr_b16 a[96:97], v12 offset:34816\n"
      "v_add_f32 v10, v10, v57\n"
      "v_mfma_f32_16x16x32_bf16 a[228:231], v[96:99], a[84:87], a[228:231]\n"
      "buffer_load_dwordx4 v1, s[36:39], s52 offen offset:0 lds\n"
      "v_mfma_f32_16x16x32_bf16 a[208:211], v[100:103], a[64:67], a[208:211]\n"
      "ds_read_b64_tr_b16 a[98:99], v12 offset:43008\n"
      "v_add_f32 v10, v10, v58\n"
      "v_mfma_f32_16x16x32_bf16 a[208:211], v[104:107], a[68:71], a[208:211]\n"
      "ds_read_b64_tr_b16 a[100:101], v12 offset:51200\n"
      "v_add_f32 v10, v10, v59\n"
      "v_mfma_f32_16x16x32_bf16 a[220:223], v[100:103], a[72:75], a[220:223]\n"
      "ds_read_b64_tr_b16 a[102:103], v12 offset:59392\n"
      "v_add_f32 v6, v6, v60\n"
      "v_mfma_f32_16x16x32_bf16 a[220:223], v[104:107], a[76:79], a[220:223]\n"
      "ds_read_b64_tr_b16 a[64:65], v13 offset:34816\n"
      "v_add_f32 v6, v6, v61\n"
      "v_mfma_f32_16x16x32_bf16 a[232:235], v[100:103], a[80:83], a[232:235]\n"
      "ds_read_b64_tr_b16 a[66:67], v13 offset:43008\n"
      "v_add_f32 v6, v6, v62\n"
      "v_mfma_f32_16x16x32_bf16 a[232:235], v[104:107], a[84:87], a[232:235]\n"
      "s_waitcnt lgkmcnt(6)\n"
      "ds_read_b64_tr_b16 a[68:69], v13 offset:51200\n"
      "v_add_f32 v6, v6, v63\n"
      "v_mfma_f32_16x16x32_bf16 a[236:239], v[84:87], a[88:91], a[236:239]\n"
      "s_add_u32 m0, s23, 34688\n"
      "ds_read_b64_tr_b16 a[70:71], v13 offset:59392\n"
      "v_add_f32 v8, v8, v64\n"
      "v_mfma_f32_16x16x32_bf16 a[236:239], v[88:91], a[92:95], a[236:239]\n"
      "buffer_load_dwordx4 v1, s[36:39], 0 offen offset:128 lds\n"
      "v_mfma_f32_16x16x32_bf16 a[240:243], v[92:95], a[88:91], a[240:243]\n"
      "ds_read_b64_tr_b16 a[72:73], v14 offset:34816\n"
      "v_add_f32 v8, v8, v65\n"
      "v_mfma_f32_16x16x32_bf16 a[240:243], v[96:99], a[92:95], a[240:243]\n"
      "ds_read_b64_tr_b16 a[74:75], v14 offset:43008\n"
      "v_add_f32 v8, v8, v66\n"
      "v_mfma_f32_16x16x32_bf16 v[108:111], v[100:103], a[88:91], v[108:111]\n"
      "ds_read_b64_tr_b16 a[76:77], v14 offset:51200\n"
      "v_add_f32 v8, v8, v67\n"
      "v_mfma_f32_16x16x32_bf16 v[108:111], v[104:107], a[92:95], v[108:111]\n"
      "s_waitcnt lgkmcnt(7)\n"
      "ds_read_b64_tr_b16 a[78:79], v14 offset:59392\n"
      "v_add_f32 v10, v10, v68\n"
      "v_mfma_f32_16x16x32_bf16 v[112:115], v[84:87], a[96:99], v[112:115]\n"
      "ds_read_b64_tr_b16 a[80:81], v15 offset:34816\n"
      "v_add_f32 v10, v10, v69\n"
      "v_mfma_f32_16x16x32_bf16 v[112:115], v[88:91], a[100:103], v[112:115]\n"
      "ds_read_b64_tr_b16 a[82:83], v15 offset:43008\n"
      "v_add_f32 v10, v10, v70\n"
      "v_mfma_f32_16x16x32_bf16 v[116:119], v[92:95], a[96:99], v[116:119]\n"
      "s_add_u32 m0, s23, 35712\n"
      "ds_read_b64_tr_b16 a[84:85], v15 offset:51200\n"
      "v_add_f32 v10, v10, v71\n"
      "v_mfma_f32_16x16x32_bf16 v[116:119], v[96:99], a[100:103], v[116:119]\n"
      "buffer_load_dwordx4 v1, s[36:39], s52 offen offset:128 lds\n"
      "v_mfma_f32_16x16x32_bf16 v[120:123], v[100:103], a[96:99], v[120:123]\n"
      "ds_read_b64_tr_b16 a[86:87], v15 offset:59392\n"
      "v_add_f32 v6, v6, v72\n"
      "v_mfma_f32_16x16x32_bf16 v[120:123], v[104:107], a[100:103], v[120:123]\n"
      "s_waitcnt lgkmcnt(8)\n"
      "ds_read_b64_tr_b16 a[88:89], v12 offset:36864\n"
      "v_add_f32 v6, v6, v73\n"
      "v_mfma_f32_16x16x32_bf16 v[124:127], v[84:87], a[64:67], v[124:127]\n"
      "ds_read_b64_tr_b16 a[90:91], v12 offset:45056\n"
      "v_add_f32 v6, v6, v74\n"
      "v_mfma_f32_16x16x32_bf16 v[124:127], v[88:91], a[68:71], v[124:127]\n"
      "ds_read_b64_tr_b16 a[92:93], v12 offset:53248\n"
      "v_add_f32 v6, v6, v75\n"
      "v_mfma_f32_16x16x32_bf16 v[128:131], v[92:95], a[64:67], v[128:131]\n"
      "ds_read_b64_tr_b16 a[94:95], v12 offset:61440\n"
      "v_add_f32 v8, v8, v76\n"
      "v_mfma_f32_16x16x32_bf16 v[128:131], v[96:99], a[68:71], v[128:131]\n"
      "ds_read_b64_tr_b16 a[96:97], v13 offset:36864\n"
      "v_add_f32 v8, v8, v77\n"
      "v_mfma_f32_16x16x32_bf16 v[132:135], v[100:103], a[64:67], v[132:135]\n"
      "ds_read_b64_tr_b16 a[98:99], v13 offset:45056\n"
      "v_add_f32 v8, v8, v78\n"
      "v_mfma_f32_16x16x32_bf16 v[132:135], v[104:107], a[68:71], v[132:135]\n"
      "s_waitcnt lgkmcnt(10)\n"
      "ds_read_b64_tr_b16 a[100:101], v13 offset:53248\n"
      "v_add_f32 v8, v8, v79\n"
      "v_mfma_f32_16x16x32_bf16 v[136:139], v[84:87], a[72:75], v[136:139]\n"
      "ds_read_b64_tr_b16 a[102:103], v13 offset:61440\n"
      "v_add_f32 v10, v10, v80\n"
      "v_mfma_f32_16x16x32_bf16 v[136:139], v[88:91], a[76:79], v[136:139]\n"
      "ds_read_b64_tr_b16 a[64:65], v14 offset:36864\n"
      "v_add_f32 v10, v10, v81\n"
      "v_mfma_f32_16x16x32_bf16 v[140:143], v[92:95], a[72:75], v[140:143]\n"
      "s_add_u32 m0, s23, 36608\n"
      "ds_read_b64_tr_b16 a[66:67], v14 offset:45056\n"
      "v_add_f32 v10, v10, v82\n"
      "v_mfma_f32_16x16x32_bf16 v[140:143], v[96:99], a[76:79], v[140:143]\n"
      "buffer_load_dwordx4 v1, s[36:39], 0 offen offset:256 lds\n"
      "v_mfma_f32_16x16x32_bf16 v[144:147], v[100:103], a[72:75], v[144:147]\n"
      "ds_read_b64_tr_b16 a[68:69], v14 offset:53248\n"
      "v_add_f32 v10, v10, v83\n"
      "v_mfma_f32_16x16x32_bf16 v[144:147], v[104:107], a[76:79], v[144:147]\n"
      "s_waitcnt lgkmcnt(11)\n"
      "ds_read_b64_tr_b16 a[70:71], v14 offset:61440\n"
      "v_mfma_f32_16x16x32_bf16 v[148:151], v[84:87], a[80:83], v[148:151]\n"
      "ds_read_b64_tr_b16 a[72:73], v15 offset:36864\n"
      "v_mfma_f32_16x16x32_bf16 v[148:151], v[88:91], a[84:87], v[148:151]\n"
      "ds_read_b64_tr_b16 a[74:75], v15 offset:45056\n"
      "v_mfma_f32_16x16x32_bf16 v[152:155], v[92:95], a[80:83], v[152:155]\n"
      "ds_read_b64_tr_b16 a[76:77], v15 offset:53248\n"
      "v_mfma_f32_16x16x32_bf16 v[152:155], v[96:99], a[84:87], v[152:155]\n"
      "ds_read_b64_tr_b16 a[78:79], v15 offset:61440\n"
      "v_mfma_f32_16x16x32_bf16 v[156:159], v[100:103], a[80:83], v[156:159]\n"
      "s_add_u32 m0, s23, 37632\n"
      "v_mfma_f32_16x16x32_bf16 v[156:159], v[104:107], a[84:87], v[156:159]\n"
      "s_waitcnt lgkmcnt(12)\n"
      "buffer_load_dwordx4 v1, s[36:39], s52 offen offset:256 lds\n"
      "v_mfma_f32_16x16x32_bf16 v[160:163], v[84:87], a[88:91], v[160:163]\n"
      "ds_read_b64_tr_b16 a[80:81], v12 offset:38912\n"
      "v_mfma_f32_16x16x32_bf16 v[160:163], v[88:91], a[92:95], v[160:163]\n"
      "ds_read_b64_tr_b16 a[82:83], v12 offset:47104\n"
      "v_mfma_f32_16x16x32_bf16 v[164:167], v[92:95], a[88:91], v[164:167]\n"
      "ds_read_b64_tr_b16 a[84:85], v12 offset:55296\n"
      "v_mfma_f32_16x16x32_bf16 v[164:167], v[96:99], a[92:95], v[164:167]\n"
      "ds_read_b64_tr_b16 a[86:87], v12 offset:63488\n"
      "v_mfma_f32_16x16x32_bf16 v[168:171], v[100:103], a[88:91], v[168:171]\n"
      "v_mfma_f32_16x16x32_bf16 v[168:171], v[104:107], a[92:95], v[168:171]\n"
      "s_waitcnt lgkmcnt(12)\n"
      "ds_read_b64_tr_b16 a[88:89], v13 offset:38912\n"
      "v_mfma_f32_16x16x32_bf16 v[172:175], v[84:87], a[96:99], v[172:175]\n"
      "s_add_u32 m0, s23, 38528\n"
      "ds_read_b64_tr_b16 a[90:91], v13 offset:47104\n"
      "v_mfma_f32_16x16x32_bf16 v[172:175], v[88:91], a[100:103], v[172:175]\n"
      "buffer_load_dwordx4 v1, s[36:39], 0 offen offset:384 lds\n"
      "v_mfma_f32_16x16x32_bf16 v[176:179], v[92:95], a[96:99], v[176:179]\n"
      "ds_read_b64_tr_b16 a[92:93], v13 offset:55296\n"
      "v_mfma_f32_16x16x32_bf16 v[176:179], v[96:99], a[100:103], v[176:179]\n"
      "ds_read_b64_tr_b16 a[94:95], v13 offset:63488\n"
      "v_mfma_f32_16x16x32_bf16 v[180:183], v[100:103], a[96:99], v[180:183]\n"
      "v_mfma_f32_16x16x32_bf16 v[180:183], v[104:107], a[100:103], v[180:183]\n"
      "s_waitcnt lgkmcnt(12)\n"
      "ds_read_b64_tr_b16 a[96:97], v14 offset:38912\n"
      "v_mfma_f32_16x16x32_bf16 v[184:187], v[84:87], a[64:67], v[184:187]\n"
      "ds_read_b64_tr_b16 a[98:99], v14 offset:47104\n"
      "v_mfma_f32_16x16x32_bf16 v[184:187], v[88:91], a[68:71], v[184:187]\n"
      "ds_read_b64_tr_b16 a[100:101], v14 offset:55296\n"
      "v_mfma_f32_16x16x32_bf16 v[188:191], v[92:95], a[64:67], v[188:191]\n"
      "s_add_u32 m0, s23, 39552\n"
      "ds_read_b64_tr_b16 a[102:103], v14 offset:63488\n"
      "v_mfma_f32_16x16x32_bf16 v[188:191], v[96:99], a[68:71], v[188:191]\n"
      "buffer_load_dwordx4 v1, s[36:39], s52 offen offset:384 lds\n"
      "v_mfma_f32_16x16x32_bf16 v[192:195], v[100:103], a[64:67], v[192:195]\n"
      "v_add_u32 v1, 0x8000, v1\n"
      "v_mfma_f32_16x16x32_bf16 v[192:195], v[104:107], a[68:71], v[192:195]\n"
      "s_waitcnt lgkmcnt(12)\n"
      "ds_read_b64_tr_b16 a[64:65], v15 offset:38912\n"
      "v_mfma_f32_16x16x32_bf16 v[196:199], v[84:87], a[72:75], v[196:199]\n"
      "ds_read_b64_tr_b16 a[66:67], v15 offset:47104\n"
      "v_mfma_f32_16x16x32_bf16 v[196:199], v[88:91], a[76:79], v[196:199]\n"
      "buffer_load_dword v23, v22, s[60:63], 0 offen\n"
      "v_mfma_f32_16x16x32_bf16 v[200:203], v[92:95], a[72:75], v[200:203]\n"
      "ds_read_b64_tr_b16 a[68:69], v15 offset:55296\n"
      "v_add_u32 v22, 0x400, v22\n"
      "v_mfma_f32_16x16x32_bf16 v[200:203], v[96:99], a[76:79], v[200:203]\n"
      "ds_read_b64_tr_b16 a[70:71], v15 offset:63488\n"
      "v_mfma_f32_16x16x32_bf16 v[204:207], v[100:103], a[72:75], v[204:207]\n"
      "v_mfma_f32_16x16x32_bf16 v[204:207], v[104:107], a[76:79], v[204:207]\n"
      "s_waitcnt lgkmcnt(12)\n"
      "v_mfma_f32_16x16x32_bf16 v[208:211], v[84:87], a[80:83], v[208:211]\n"
      "v_mfma_f32_16x16x32_bf16 v[208:211], v[88:91], a[84:87], v[208:211]\n"
      "s_getreg_b32 s29, hwreg(HW_REG_IB_STS, 0, 4)\n"
      "v_mfma_f32_16x16x32_bf16 v[212:215], v[92:95], a[80:83], v[212:215]\n"
      "s_cmp_ge_u32 s29, 10\n"
      "v_mfma_f32_16x16x32_bf16 v[212:215], v[96:99], a[84:87], v[212:215]\n"
      "s_cbranch_scc1 L_topN3_%=\n"
      "s_branch L_topD3_%=\n"
      ".p2align 6\n"
      "L_topN3_%=:\n"
      "v_mfma_f32_16x16x32_bf16 v[216:219], v[100:103], a[80:83], v[216:219]\n"
      "v_mfma_f32_16x16x32_bf16 v[216:219], v[104:107], a[84:87], v[216:219]\n"
      "s_waitcnt lgkmcnt(8)\n"
      "v_mfma_f32_16x16x32_bf16 v[220:223], v[84:87], a[88:91], v[220:223]\n"
      "s_waitcnt vmcnt(17) lgkmcnt(0)\n"
      "v_mfma_f32_16x16x32_bf16 v[220:223], v[88:91], a[92:95], v[220:223]\n"
      "s_barrier\n"
      "ds_read_b128 a[0:3], v3 offset:0\n"
      "v_mfma_f32_16x16x32_bf16 v[224:227], v[92:95], a[88:91], v[224:227]\n"
      "ds_read_b128 a[4:7], v4 offset:0\n"
      "v_mfma_f32_16x16x32_bf16 v[224:227], v[96:99], a[92:95], v[224:227]\n"
      "ds_read_b128 a[8:11], v3 offset:2048\n"
      "v_mfma_f32_16x16x32_bf16 v[228:231], v[100:103], a[88:91], v[228:231]\n"
      "s_add_u32 m0, s23, 98304\n"
      "ds_read_b128 a[12:15], v4 offset:2048\n"
      "v_mfma_f32_16x16x32_bf16 v[228:231], v[104:107], a[92:95], v[228:231]\n"
      "buffer_load_dwordx4 v2, s[40:43], 0 offen offset:0 nt lds\n"
      "v_mfma_f32_16x16x32_bf16 v[232:235], v[84:87], a[96:99], v[232:235]\n"
      "ds_read_b128 a[16:19], v3 offset:4096\n"
      "v_mfma_f32_16x16x32_bf16 v[232:235], v[88:91], a[100:103], v[232:235]\n"
      "ds_read_b128 a[20:23], v4 offset:4096\n"
      "v_mfma_f32_16x16x32_bf16 v[236:239], v[92:95], a[96:99], v[236:239]\n"
      "ds_read_b128 a[24:27], v3 offset:6144\n"
      "v_mfma_f32_16x16x32_bf16 v[236:239], v[96:99], a[100:103], v[236:239]\n"
      "ds_read_b128 a[28:31], v4 offset:6144\n"
      "v_mfma_f32_16x16x32_bf16 v[240:243], v[100:103], a[96:99], v[240:243]\n"
      "ds_read_b128 a[32:35], v3 offset:8192\n"
      "v_mfma_f32_16x16x32_bf16 v[240:243], v[104:107], a[100:103], v[240:243]\n"
      "ds_read_b128 a[36:39], v4 offset:8192\n"
      "v_mfma_f32_16x16x32_bf16 v[244:247], v[84:87], a[64:67], v[244:247]\n"
      "ds_read_b128 a[40:43], v3 offset:10240\n"
      "v_mfma_f32_16x16x32_bf16 v[244:247], v[88:91], a[68:71], v[244:247]\n"
      "ds_read_b128 a[44:47], v4 offset:10240\n"
      "v_mfma_f32_16x16x32_bf16 v[248:251], v[92:95], a[64:67], v[248:251]\n"
      "ds_read_b128 a[48:51], v3 offset:12288\n"
      "v_mfma_f32_16x16x32_bf16 v[248:251], v[96:99], a[68:71], v[248:251]\n"
      "ds_read_b128 a[52:55], v4 offset:12288\n"
      "v_mfma_f32_16x16x32_bf16 v[252:255], v[100:103], a[64:67], v[252:255]\n"
      "s_add_u32 m0, s23, 99328\n"
      "ds_read_b128 a[56:59], v3 offset:14336\n"
      "v_mfma_f32_16x16x32_bf16 v[252:255], v[104:107], a[68:71], v[252:255]\n"
      "s_waitcnt lgkmcnt(7)\n"
      "buffer_load_dwordx4 v2, s[40:43], s52 offen offset:0 nt lds\n"
      "v_mfma_f32_16x16x32_bf16 v[36:39], a[0:3], a[104:107], v[24:27]\n"
      "ds_read_b128 a[60:63], v4 offset:14336\n"
      "v_mfma_f32_16x16x32_bf16 v[36:39], a[4:7], a[108:111], v[36:39]\n"
      "v_mfma_f32_16x16x32_bf16 v[36:39], a[8:11], a[112:115], v[36:39]\n"
      "v_mfma_f32_16x16x32_bf16 v[36:39], a[12:15], a[116:119], v[36:39]\n"
      "v_mfma_f32_16x16x32_bf16 v[36:39], a[16:19], a[120:123], v[36:39]\n"
      "v_mfma_f32_16x16x32_bf16 v[36:39], a[20:23], a[124:127], v[36:39]\n"
      "v_mfma_f32_16x16x32_bf16 v[36:39], a[24:27], a[128:131], v[36:39]\n"
      "s_add_u32 m0, s23, 100224\n"
      "v_mfma_f32_16x16x32_bf16 v[36:39], a[28:31], a[132:135], v[36:39]\n"
      "buffer_load_dwordx4 v2, s[40:43], 0 offen offset:128 nt lds\n"
      "v_mfma_f32_16x16x32_bf16 v[40:43], a[0:3], a[136:139], v[28:31]\n"
      "v_mfma_f32_16x16x32_bf16 v[40:43], a[4:7], a[140:143], v[40:43]\n"
      "v_mfma_f32_16x16x32_bf16 v[40:43], a[8:11], a[144:147], v[40:43]\n"
      "v_exp_f32 v36, v36\n"
      "v_mfma_f32_16x16x32_bf16 v[40:43], a[12:15], a[148:151], v[40:43]\n"
      "v_add_f32 v6, v6, v36\n"
      "v_mfma_f32_16x16x32_bf16 v[40:43], a[16:19], a[152:155], v[40:43]\n"
      "v_exp_f32 v37, v37\n"
      "v_mfma_f32_16x16x32_bf16 v[40:43], a[20:23], a[156:159], v[40:43]\n"
      "v_cvt_pk_bf16_f32 v84, v36, v37\n"
      "v_add_f32 v6, v6, v37\n"
      "v_mfma_f32_16x16x32_bf16 v[40:43], a[24:27], a[160:163], v[40:43]\n"
      "v_exp_f32 v38, v38\n"
      "v_mfma_f32_16x16x32_bf16 v[40:43], a[28:31], a[164:167], v[40:43]\n"
      "v_add_f32 v6, v6, v38\n"
      "v_mfma_f32_16x16x32_bf16 v[44:47], a[0:3], a[168:171], v[32:35]\n"
      "v_exp_f32 v39, v39\n"
      "v_mfma_f32_16x16x32_bf16 v[44:47], a[4:7], a[172:175], v[44:47]\n"
      "ds_read_b128 a[0:3], v3 offset:16384\n"
      "v_cvt_pk_bf16_f32 v85, v38, v39\n"
      "v_mfma_f32_16x16x32_bf16 v[44:47], a[8:11], a[176:179], v[44:47]\n"
      "s_add_u32 m0, s23, 101248\n"
      "v_exp_f32 v40, v40\n"
      "v_mfma_f32_16x16x32_bf16 v[44:47], a[12:15], a[180:183], v[44:47]\n"
      "buffer_load_dwordx4 v2, s[40:43], s52 offen offset:128 nt lds\n"
      "v_mfma_f32_16x16x32_bf16 v[44:47], a[16:19], a[184:187], v[44:47]\n"
      "v_exp_f32 v41, v41\n"
      "v_mfma_f32_16x16x32_bf16 v[44:47], a[20:23], a[188:191], v[44:47]\n"
      "ds_read_b128 a[4:7], v4 offset:16384\n"
      "v_cvt_pk_bf16_f32 v92, v40, v41\n"
      "v_mfma_f32_16x16x32_bf16 v[44:47], a[24:27], a[192:195], v[44:47]\n"
      "v_exp_f32 v42, v42\n"
      "v_mfma_f32_16x16x32_bf16 v[44:47], a[28:31], a[196:199], v[44:47]\n"
      "s_waitcnt lgkmcnt(2)\n"
      "ds_read_b128 a[8:11], v3 offset:18432\n"
      "v_add_f32 v6, v6, v39\n"
      "v_mfma_f32_16x16x32_bf16 v[48:51], a[32:35], a[104:107], v[24:27]\n"
      "v_exp_f32 v43, v43\n"
      "v_mfma_f32_16x16x32_bf16 v[48:51], a[36:39], a[108:111], v[48:51]\n"
      "ds_read_b128 a[12:15], v4 offset:18432\n"
      "v_cvt_pk_bf16_f32 v93, v42, v43\n"
      "v_mfma_f32_16x16x32_bf16 v[48:51], a[40:43], a[112:115], v[48:51]\n"
      "s_add_u32 m0, s23, 102144\n"
      "v_exp_f32 v44, v44\n"
      "v_mfma_f32_16x16x32_bf16 v[48:51], a[44:47], a[116:119], v[48:51]\n"
      "buffer_load_dwordx4 v2, s[40:43], 0 offen offset:256 nt lds\n"
      "v_mfma_f32_16x16x32_bf16 v[48:51], a[48:51], a[120:123], v[48:51]\n"
      "v_exp_f32 v45, v45\n"
      "v_mfma_f32_16x16x32_bf16 v[48:51], a[52:55], a[124:127], v[48:51]\n"
      "ds_read_b128 a[16:19], v3 offset:20480\n"
      "v_cvt_pk_bf16_f32 v100, v44, v45\n"
      "v_mfma_f32_16x16x32_bf16 v[48:51], a[56:59], a[128:131], v[48:51]\n"
      "v_exp_f32 v46, v46\n"
      "v_mfma_f32_16x16x32_bf16 v[48:51], a[60:63], a[132:135], v[48:51]\n"
      "ds_read_b128 a[20:23], v4 offset:20480\n"
      "v_add_f32 v8, v8, v40\n"
      "v_mfma_f32_16x16x32_bf16 v[52:55], a[32:35], a[136:139], v[28:31]\n"
      "v_exp_f32 v47, v47\n"
      "v_mfma_f32_16x16x32_bf16 v[52:55], a[36:39], a[140:143], v[52:55]\n"
      "ds_read_b128 a[24:27], v3 offset:22528\n"
      "v_cvt_pk_bf16_f32 v101, v46, v47\n"
      "v_mfma_f32_16x16x32_bf16 v[52:55], a[40:43], a[144:147], v[52:55]\n"
      "v_exp_f32 v48, v48\n"
      "v_mfma_f32_16x16x32_bf16 v[52:55], a[44:47], a[148:151], v[52:55]\n"
      "ds_read_b128 a[28:31], v4 offset:22528\n"
      "v_add_f32 v8, v8, v41\n"
      "v_mfma_f32_16x16x32_bf16 v[52:55], a[48:51], a[152:155], v[52:55]\n"
      "v_exp_f32 v49, v49\n"
      "v_mfma_f32_16x16x32_bf16 v[52:55], a[52:55], a[156:159], v[52:55]\n"
      "v_cvt_pk_bf16_f32 v86, v48, v49\n"
      "v_add_f32 v8, v8, v42\n"
      "v_mfma_f32_16x16x32_bf16 v[52:55], a[56:59], a[160:163], v[52:55]\n"
      "s_add_u32 m0, s23, 103168\n"
      "v_exp_f32 v50, v50\n"
      "v_mfma_f32_16x16x32_bf16 v[52:55], a[60:63], a[164:167], v[52:55]\n"
      "buffer_load_dwordx4 v2, s[40:43], s52 offen offset:256 nt lds\n"
      "v_mfma_f32_16x16x32_bf16 v[56:59], a[32:35], a[168:171], v[32:35]\n"
      "v_exp_f32 v51, v51\n"
      "v_mfma_f32_16x16x32_bf16 v[56:59], a[36:39], a[172:175], v[56:59]\n"
      "ds_read_b128 a[32:35], v3 offset:24576\n"
      "v_cvt_pk_bf16_f32 v87, v50, v51\n"
      "v_mfma_f32_16x16x32_bf16 v[56:59], a[40:43], a[176:179], v[56:59]\n"
      "v_exp_f32 v52, v52\n"
      "v_mfma_f32_16x16x32_bf16 v[56:59], a[44:47], a[180:183], v[56:59]\n"
      "ds_read_b128 a[36:39], v4 offset:24576\n"
      "v_add_f32 v8, v8, v43\n"
      "v_mfma_f32_16x16x32_bf16 v[56:59], a[48:51], a[184:187], v[56:59]\n"
      "v_exp_f32 v53, v53\n"
      "v_mfma_f32_16x16x32_bf16 v[56:59], a[52:55], a[188:191], v[56:59]\n"
      "ds_read_b128 a[40:43], v3 offset:26624\n"
      "v_cvt_pk_bf16_f32 v94, v52, v53\n"
      "v_mfma_f32_16x16x32_bf16 v[56:59], a[56:59], a[192:195], v[56:59]\n"
      "s_add_u32 m0, s23, 104064\n"
      "v_exp_f32 v54, v54\n"
      "v_mfma_f32_16x16x32_bf16 v[56:59], a[60:63], a[196:199], v[56:59]\n"
      "s_waitcnt lgkmcnt(3)\n"
      "buffer_load_dwordx4 v2, s[40:43], 0 offen offset:384 nt lds\n"
      "v_mfma_f32_16x16x32_bf16 v[60:63], a[0:3], a[104:107], v[24:27]\n"
      "v_exp_f32 v55, v55\n"
      "v_mfma_f32_16x16x32_bf16 v[60:63], a[4:7], a[108:111], v[60:63]\n"
      "ds_read_b128 a[44:47], v4 offset:26624\n"
      "v_cvt_pk_bf16_f32 v95, v54, v55\n"
      "v_mfma_f32_16x16x32_bf16 v[60:63], a[8:11], a[112:115], v[60:63]\n"
      "v_exp_f32 v56, v56\n"
      "v_mfma_f32_16x16x32_bf16 v[60:63], a[12:15], a[116:119], v[60:63]\n"
      "ds_read_b128 a[48:51], v3 offset:28672\n"
      "v_add_f32 v10, v10, v44\n"
      "v_mfma_f32_16x16x32_bf16 v[60:63], a[16:19], a[120:123], v[60:63]\n"
      "v_exp_f32 v57, v57\n"
      "v_mfma_f32_16x16x32_bf16 v[60:63], a[20:23], a[124:127], v[60:63]\n"
      "ds_read_b128 a[52:55], v4 offset:28672\n"
      "v_cvt_pk_bf16_f32 v102, v56, v57\n"
      "v_mfma_f32_16x16x32_bf16 v[60:63], a[24:27], a[128:131], v[60:63]\n"
      "v_exp_f32 v58, v58\n"
      "v_mfma_f32_16x16x32_bf16 v[60:63], a[28:31], a[132:135], v[60:63]\n"
      "ds_read_b128 a[56:59], v3 offset:30720\n"
      "v_add_f32 v10, v10, v45\n"
      "v_mfma_f32_16x16x32_bf16 v[64:67], a[0:3], a[136:139], v[28:31]\n"
      "v_exp_f32 v59, v59\n"
      "v_mfma_f32_16x16x32_bf16 v[64:67], a[4:7], a[140:143], v[64:67]\n"
      "ds_read_b128 a[60:63], v4 offset:30720\n"
      "v_cvt_pk_bf16_f32 v103, v58, v59\n"
      "v_mfma_f32_16x16x32_bf16 v[64:67], a[8:11], a[144:147], v[64:67]\n"
      "s_add_u32 m0, s23, 105088\n"
      "v_exp_f32 v60, v60\n"
      "v_mfma_f32_16x16x32_bf16 v[64:67], a[12:15], a[148:151], v[64:67]\n"
      "buffer_load_dwordx4 v2, s[40:43], s52 offen offset:384 nt lds\n"
      "v_mfma_f32_16x16x32_bf16 v[64:67], a[16:19], a[152:155], v[64:67]\n"
      "v_exp_f32 v61, v61\n"
      "v_mfma_f32_16x16x32_bf16 v[64:67], a[20:23], a[156:159], v[64:67]\n"
      "v_cvt_pk_bf16_f32 v88, v60, v61\n"
      "v_add_u32 v2, 0x8000, v2\n"
      "v_mfma_f32_16x16x32_bf16 v[64:67], a[24:27], a[160:163], v[64:67]\n"
      "v_exp_f32 v62, v62\n"
      "v_mfma_f32_16x16x32_bf16 v[64:67], a[28:31], a[164:167], v[64:67]\n"
      "v_add_f32 v10, v10, v46\n"
      "v_add_f32 v10, v10, v47\n"
      "v_mfma_f32_16x16x32_bf16 v[68:71], a[0:3], a[168:171], v[32:35]\n"
      "v_exp_f32 v63, v63\n"
      "v_mfma_f32_16x16x32_bf16 v[68:71], a[4:7], a[172:175], v[68:71]\n"
      "v_cvt_pk_bf16_f32 v89, v62, v63\n"
      "v_add_f32 v6, v6, v48\n"
      "v_mfma_f32_16x16x32_bf16 v[68:71], a[8:11], a[176:179], v[68:71]\n"
      "s_waitcnt vmcnt(17) lgkmcnt(0)\n"
      "v_exp_f32 v64, v64\n"
      "v_mfma_f32_16x16x32_bf16 v[68:71], a[12:15], a[180:183], v[68:71]\n"
      "s_barrier\n"
      "ds_read_b64_tr_b16 a[64:65], v12 offset:0\n"
      "v_add_f32 v6, v6, v49\n"
      "v_mfma_f32_16x16x32_bf16 v[68:71], a[16:19], a[184:187], v[68:71]\n"
      "v_exp_f32 v65, v65\n"
      "v_mfma_f32_16x16x32_bf16 v[68:71], a[20:23], a[188:191], v[68:71]\n"
      "ds_read_b64_tr_b16 a[66:67], v12 offset:8192\n"
      "v_cvt_pk_bf16_f32 v96, v64, v65\n"
      "v_mfma_f32_16x16x32_bf16 v[68:71], a[24:27], a[192:195], v[68:71]\n"
      "v_exp_f32 v66, v66\n"
      "v_mfma_f32_16x16x32_bf16 v[68:71], a[28:31], a[196:199], v[68:71]\n"
      "ds_read_b64_tr_b16 a[68:69], v12 offset:16384\n"
      "v_add_f32 v6, v6, v50\n"
      "v_mfma_f32_16x16x32_bf16 v[72:75], a[32:35], a[104:107], v[24:27]\n"
      "v_exp_f32 v67, v67\n"
      "v_mfma_f32_16x16x32_bf16 v[72:75], a[36:39], a[108:111], v[72:75]\n"
      "ds_read_b64_tr_b16 a[70:71], v12 offset:24576\n"
      "v_cvt_pk_bf16_f32 v97, v66, v67\n"
      "v_mfma_f32_16x16x32_bf16 v[72:75], a[40:43], a[112:115], v[72:75]\n"
      "v_exp_f32 v68, v68\n"
      "v_mfma_f32_16x16x32_bf16 v[72:75], a[44:47], a[116:119], v[72:75]\n"
      "ds_read_b64_tr_b16 a[72:73], v13 offset:0\n"
      "v_add_f32 v6, v6, v51\n"
      "v_mfma_f32_16x16x32_bf16 v[72:75], a[48:51], a[120:123], v[72:75]\n"
      "v_exp_f32 v69, v69\n"
      "v_mfma_f32_16x16x32_bf16 v[72:75], a[52:55], a[124:127], v[72:75]\n"
      "ds_read_b64_tr_b16 a[74:75], v13 offset:8192\n"
      "v_cvt_pk_bf16_f32 v104, v68, v69\n"
      "v_mfma_f32_16x16x32_bf16 v[72:75], a[56:59], a[128:131], v[72:75]\n"
      "v_exp_f32 v70, v70\n"
      "v_mfma_f32_16x16x32_bf16 v[72:75], a[60:63], a[132:135], v[72:75]\n"
      "ds_read_b64_tr_b16 a[76:77], v13 offset:16384\n"
      "v_add_f32 v8, v8, v52\n"
      "v_mfma_f32_16x16x32_bf16 v[76:79], a[32:35], a[136:139], v[28:31]\n"
      "v_exp_f32 v71, v71\n"
      "v_mfma_f32_16x16x32_bf16 v[76:79], a[36:39], a[140:143], v[76:79]\n"
      "ds_read_b64_tr_b16 a[78:79], v13 offset:24576\n"
      "v_cvt_pk_bf16_f32 v105, v70, v71\n"
      "v_mfma_f32_16x16x32_bf16 v[76:79], a[40:43], a[144:147], v[76:79]\n"
      "v_exp_f32 v72, v72\n"
      "v_mfma_f32_16x16x32_bf16 v[76:79], a[44:47], a[148:151], v[76:79]\n"
      "ds_read_b64_tr_b16 a[80:81], v14 offset:0\n"
      "v_add_f32 v8, v8, v53\n"
      "v_mfma_f32_16x16x32_bf16 v[76:79], a[48:51], a[152:155], v[76:79]\n"
      "v_exp_f32 v73, v73\n"
      "v_mfma_f32_16x16x32_bf16 v[76:79], a[52:55], a[156:159], v[76:79]\n"
      "ds_read_b64_tr_b16 a[82:83], v14 offset:8192\n"
      "v_cvt_pk_bf16_f32 v90, v72, v73\n"
      "v_mfma_f32_16x16x32_bf16 v[76:79], a[56:59], a[160:163], v[76:79]\n"
      "v_exp_f32 v74, v74\n"
      "v_mfma_f32_16x16x32_bf16 v[76:79], a[60:63], a[164:167], v[76:79]\n"
      "ds_read_b64_tr_b16 a[84:85], v14 offset:16384\n"
      "v_add_f32 v8, v8, v54\n"
      "v_mfma_f32_16x16x32_bf16 v[80:83], a[32:35], a[168:171], v[32:35]\n"
      "v_exp_f32 v75, v75\n"
      "v_mfma_f32_16x16x32_bf16 v[80:83], a[36:39], a[172:175], v[80:83]\n"
      "ds_read_b64_tr_b16 a[86:87], v14 offset:24576\n"
      "v_cvt_pk_bf16_f32 v91, v74, v75\n"
      "v_mfma_f32_16x16x32_bf16 v[80:83], a[40:43], a[176:179], v[80:83]\n"
      "v_exp_f32 v76, v76\n"
      "v_mfma_f32_16x16x32_bf16 v[80:83], a[44:47], a[180:183], v[80:83]\n"
      "v_add_f32 v8, v8, v55\n"
      "v_mfma_f32_16x16x32_bf16 v[80:83], a[48:51], a[184:187], v[80:83]\n"
      "v_exp_f32 v77, v77\n"
      "v_mfma_f32_16x16x32_bf16 v[80:83], a[52:55], a[188:191], v[80:83]\n"
      "v_cvt_pk_bf16_f32 v98, v76, v77\n"
      "v_mfma_f32_16x16x32_bf16 v[80:83], a[56:59], a[192:195], v[80:83]\n"
      "v_exp_f32 v78, v78\n"
      "v_mfma_f32_16x16x32_bf16 v[80:83], a[60:63], a[196:199], v[80:83]\n"
      "s_waitcnt lgkmcnt(8)\n"
      "L_loopN3_%=:\n"
      "v_mfma_f32_16x16x32_bf16 a[200:203], v[84:87], a[64:67], a[200:203]\n"
      "v_exp_f32 v79, v79\n"
      "v_mfma_f32_16x16x32_bf16 a[200:203], v[88:91], a[68:71], a[200:203]\n"
      "s_waitcnt lgkmcnt(4)\n"
      "ds_read_b64_tr_b16 a[88:89], v15 offset:0\n"
      "v_cvt_pk_bf16_f32 v99, v78, v79\n"
      "v_mfma_f32_16x16x32_bf16 a[212:215], v[84:87], a[72:75], a[212:215]\n"
      "s_add_u32 m0, s23, 0\n"
      "v_exp_f32 v80, v80\n"
      "v_mfma_f32_16x16x32_bf16 a[212:215], v[88:91], a[76:79], a[212:215]\n"
      "s_waitcnt lgkmcnt(1)\n"
      "buffer_load_dwordx4 v1, s[36:39], 0 offen offset:0 nt lds\n"
      "v_mfma_f32_16x16x32_bf16 a[224:227], v[84:87], a[80:83], a[224:227]\n"
      "v_exp_f32 v81, v81\n"
      "v_mfma_f32_16x16x32_bf16 a[224:227], v[88:91], a[84:87], a[224:227]\n"
      "ds_read_b64_tr_b16 a[90:91], v15 offset:8192\n"
      "v_cvt_pk_bf16_f32 v106, v80, v81\n"
      "v_mfma_f32_16x16x32_bf16 a[204:207], v[92:95], a[64:67], a[204:207]\n"
      "v_exp_f32 v82, v82\n"
      "v_mfma_f32_16x16x32_bf16 a[204:207], v[96:99], a[68:71], a[204:207]\n"
      "ds_read_b64_tr_b16 a[92:93], v15 offset:16384\n"
      "v_add_f32 v10, v10, v56\n"
      "v_mfma_f32_16x16x32_bf16 a[216:219], v[92:95], a[72:75], a[216:219]\n"
      "v_exp_f32 v83, v83\n"
      "v_mfma_f32_16x16x32_bf16 a[216:219], v[96:99], a[76:79], a[216:219]\n"
      "ds_read_b64_tr_b16 a[94:95], v15 offset:24576\n"
      "v_cvt_pk_bf16_f32 v107, v82, v83\n"
      "v_mfma_f32_16x16x32_bf16 a[228:231], v[92:95], a[80:83], a[228:231]\n"
      "s_add_u32 m0, s23, 1024\n"
      "ds_read_b64_tr_b16 a[96:97], v12 offset:2048\n"
      "v_add_f32 v10, v10, v57\n"
      "v_mfma_f32_16x16x32_bf16 a[228:231], v[96:99], a[84:87], a[228:231]\n"
      "buffer_load_dwordx4 v1, s[36:39], s52 offen offset:0 nt lds\n"
      "v_mfma_f32_16x16x32_bf16 a[208:211], v[100:103], a[64:67], a[208:211]\n"
      "ds_read_b64_tr_b16 a[98:99], v12 offset:10240\n"
      "v_add_f32 v10, v10, v58\n"
      "v_mfma_f32_16x16x32_bf16 a[208:211], v[104:107], a[68:71], a[208:211]\n"
      "ds_read_b64_tr_b16 a[100:101], v12 offset:18432\n"
      "v_add_f32 v10, v10, v59\n"
      "v_mfma_f32_16x16x32_bf16 a[220:223], v[100:103], a[72:75], a[220:223]\n"
      "ds_read_b64_tr_b16 a[102:103], v12 offset:26624\n"
      "v_add_f32 v6, v6, v60\n"
      "v_mfma_f32_16x16x32_bf16 a[220:223], v[104:107], a[76:79], a[220:223]\n"
      "ds_read_b64_tr_b16 a[64:65], v13 offset:2048\n"
      "v_add_f32 v6, v6, v61\n"
      "v_mfma_f32_16x16x32_bf16 a[232:235], v[100:103], a[80:83], a[232:235]\n"
      "ds_read_b64_tr_b16 a[66:67], v13 offset:10240\n"
      "v_add_f32 v6, v6, v62\n"
      "v_mfma_f32_16x16x32_bf16 a[232:235], v[104:107], a[84:87], a[232:235]\n"
      "s_waitcnt lgkmcnt(6)\n"
      "ds_read_b64_tr_b16 a[68:69], v13 offset:18432\n"
      "v_add_f32 v6, v6, v63\n"
      "v_mfma_f32_16x16x32_bf16 a[236:239], v[84:87], a[88:91], a[236:239]\n"
      "s_add_u32 m0, s23, 1920\n"
      "ds_read_b64_tr_b16 a[70:71], v13 offset:26624\n"
      "v_add_f32 v8, v8, v64\n"
      "v_mfma_f32_16x16x32_bf16 a[236:239], v[88:91], a[92:95], a[236:239]\n"
      "buffer_load_dwordx4 v1, s[36:39], 0 offen offset:128 nt lds\n"
      "v_mfma_f32_16x16x32_bf16 a[240:243], v[92:95], a[88:91], a[240:243]\n"
      "ds_read_b64_tr_b16 a[72:73], v14 offset:2048\n"
      "v_add_f32 v8, v8, v65\n"
      "v_mfma_f32_16x16x32_bf16 a[240:243], v[96:99], a[92:95], a[240:243]\n"
      "ds_read_b64_tr_b16 a[74:75], v14 offset:10240\n"
      "v_add_f32 v8, v8, v66\n"
      "v_mfma_f32_16x16x32_bf16 v[108:111], v[100:103], a[88:91], v[108:111]\n"
      "ds_read_b64_tr_b16 a[76:77], v14 offset:18432\n"
      "v_add_f32 v8, v8, v67\n"
      "v_mfma_f32_16x16x32_bf16 v[108:111], v[104:107], a[92:95], v[108:111]\n"
      "s_waitcnt lgkmcnt(7)\n"
      "ds_read_b64_tr_b16 a[78:79], v14 offset:26624\n"
      "v_add_f32 v10, v10, v68\n"
      "v_mfma_f32_16x16x32_bf16 v[112:115], v[84:87], a[96:99], v[112:115]\n"
      "ds_read_b64_tr_b16 a[80:81], v15 offset:2048\n"
      "v_add_f32 v10, v10, v69\n"
      "v_mfma_f32_16x16x32_bf16 v[112:115], v[88:91], a[100:103], v[112:115]\n"
      "ds_read_b64_tr_b16 a[82:83], v15 offset:10240\n"
      "v_add_f32 v10, v10, v70\n"
      "v_mfma_f32_16x16x32_bf16 v[116:119], v[92:95], a[96:99], v[116:119]\n"
      "s_add_u32 m0, s23, 2944\n"
      "ds_read_b64_tr_b16 a[84:85], v15 offset:18432\n"
      "v_add_f32 v10, v10, v71\n"
      "v_mfma_f32_16x16x32_bf16 v[116:119], v[96:99], a[100:103], v[116:119]\n"
      "buffer_load_dwordx4 v1, s[36:39], s52 offen offset:128 nt lds\n"
      "v_mfma_f32_16x16x32_bf16 v[120:123], v[100:103], a[96:99], v[120:123]\n"
      "ds_read_b64_tr_b16 a[86:87], v15 offset:26624\n"
      "v_add_f32 v6, v6, v72\n"
      "v_mfma_f32_16x16x32_bf16 v[120:123], v[104:107], a[100:103], v[120:123]\n"
      "s_waitcnt lgkmcnt(8)\n"
      "ds_read_b64_tr_b16 a[88:89], v12 offset:4096\n"
      "v_add_f32 v6, v6, v73\n"
      "v_mfma_f32_16x16x32_bf16 v[124:127], v[84:87], a[64:67], v[124:127]\n"
      "ds_read_b64_tr_b16 a[90:91], v12 offset:12288\n"
      "v_add_f32 v6, v6, v74\n"
      "v_mfma_f32_16x16x32_bf16 v[124:127], v[88:91], a[68:71], v[124:127]\n"
      "ds_read_b64_tr_b16 a[92:93], v12 offset:20480\n"
      "v_add_f32 v6, v6, v75\n"
      "v_mfma_f32_16x16x32_bf16 v[128:131], v[92:95], a[64:67], v[128:131]\n"
      "ds_read_b64_tr_b16 a[94:95], v12 offset:28672\n"
      "v_add_f32 v8, v8, v76\n"
      "v_mfma_f32_16x16x32_bf16 v[128:131], v[96:99], a[68:71], v[128:131]\n"
      "ds_read_b64_tr_b16 a[96:97], v13 offset:4096\n"
      "v_add_f32 v8, v8, v77\n"
      "v_mfma_f32_16x16x32_bf16 v[132:135], v[100:103], a[64:67], v[132:135]\n"
      "ds_read_b64_tr_b16 a[98:99], v13 offset:12288\n"
      "v_add_f32 v8, v8, v78\n"
      "v_mfma_f32_16x16x32_bf16 v[132:135], v[104:107], a[68:71], v[132:135]\n"
      "s_waitcnt lgkmcnt(10)\n"
      "ds_read_b64_tr_b16 a[100:101], v13 offset:20480\n"
      "v_add_f32 v8, v8, v79\n"
      "v_mfma_f32_16x16x32_bf16 v[136:139], v[84:87], a[72:75], v[136:139]\n"
      "ds_read_b64_tr_b16 a[102:103], v13 offset:28672\n"
      "v_add_f32 v10, v10, v80\n"
      "v_mfma_f32_16x16x32_bf16 v[136:139], v[88:91], a[76:79], v[136:139]\n"
      "ds_read_b64_tr_b16 a[64:65], v14 offset:4096\n"
      "v_add_f32 v10, v10, v81\n"
      "v_mfma_f32_16x16x32_bf16 v[140:143], v[92:95], a[72:75], v[140:143]\n"
      "s_add_u32 m0, s23, 3840\n"
      "ds_read_b64_tr_b16 a[66:67], v14 offset:12288\n"
      "v_add_f32 v10, v10, v82\n"
      "v_mfma_f32_16x16x32_bf16 v[140:143], v[96:99], a[76:79], v[140:143]\n"
      "buffer_load_dwordx4 v1, s[36:39], 0 offen offset:256 nt lds\n"
      "v_mfma_f32_16x16x32_bf16 v[144:147], v[100:103], a[72:75], v[144:147]\n"
      "ds_read_b64_tr_b16 a[68:69], v14 offset:20480\n"
      "v_add_f32 v10, v10, v83\n"
      "v_mfma_f32_16x16x32_bf16 v[144:147], v[104:107], a[76:79], v[144:147]\n"
      "s_waitcnt lgkmcnt(11)\n"
      "ds_read_b64_tr_b16 a[70:71], v14 offset:28672\n"
      "v_mfma_f32_16x16x32_bf16 v[148:151], v[84:87], a[80:83], v[148:151]\n"
      "ds_read_b64_tr_b16 a[72:73], v15 offset:4096\n"
      "v_mfma_f32_16x16x32_bf16 v[148:151], v[88:91], a[84:87], v[148:151]\n"
      "ds_read_b64_tr_b16 a[74:75], v15 offset:12288\n"
      "v_mfma_f32_16x16x32_bf16 v[152:155], v[92:95], a[80:83], v[152:155]\n"
      "ds_read_b64_tr_b16 a[76:77], v15 offset:20480\n"
      "v_mfma_f32_16x16x32_bf16 v[152:155], v[96:99], a[84:87], v[152:155]\n"
      "ds_read_b64_tr_b16 a[78:79], v15 offset:28672\n"
      "v_mfma_f32_16x16x32_bf16 v[156:159], v[100:103], a[80:83], v[156:159]\n"
      "s_add_u32 m0, s23, 4864\n"
      "v_mfma_f32_16x16x32_bf16 v[156:159], v[104:107], a[84:87], v[156:159]\n"
      "s_waitcnt lgkmcnt(12)\n"
      "buffer_load_dwordx4 v1, s[36:39], s52 offen offset:256 nt lds\n"
      "v_mfma_f32_16x16x32_bf16 v[160:163], v[84:87], a[88:91], v[160:163]\n"
      "ds_read_b64_tr_b16 a[80:81], v12 offset:6144\n"
      "v_mfma_f32_16x16x32_bf16 v[160:163], v[88:91], a[92:95], v[160:163]\n"
      "ds_read_b64_tr_b16 a[82:83], v12 offset:14336\n"
      "v_mfma_f32_16x16x32_bf16 v[164:167], v[92:95], a[88:91], v[164:167]\n"
      "ds_read_b64_tr_b16 a[84:85], v12 offset:22528\n"
      "v_mfma_f32_16x16x32_bf16 v[164:167], v[96:99], a[92:95], v[164:167]\n"
      "ds_read_b64_tr_b16 a[86:87], v12 offset:30720\n"
      "v_mfma_f32_16x16x32_bf16 v[168:171], v[100:103], a[88:91], v[168:171]\n"
      "v_mfma_f32_16x16x32_bf16 v[168:171], v[104:107], a[92:95], v[168:171]\n"
      "s_waitcnt lgkmcnt(12)\n"
      "ds_read_b64_tr_b16 a[88:89], v13 offset:6144\n"
      "v_mfma_f32_16x16x32_bf16 v[172:175], v[84:87], a[96:99], v[172:175]\n"
      "s_add_u32 m0, s23, 5760\n"
      "ds_read_b64_tr_b16 a[90:91], v13 offset:14336\n"
      "v_mfma_f32_16x16x32_bf16 v[172:175], v[88:91], a[100:103], v[172:175]\n"
      "buffer_load_dwordx4 v1, s[36:39], 0 offen offset:384 nt lds\n"
      "v_mfma_f32_16x16x32_bf16 v[176:179], v[92:95], a[96:99], v[176:179]\n"
      "ds_read_b64_tr_b16 a[92:93], v13 offset:22528\n"
      "v_mfma_f32_16x16x32_bf16 v[176:179], v[96:99], a[100:103], v[176:179]\n"
      "ds_read_b64_tr_b16 a[94:95], v13 offset:30720\n"
      "v_mfma_f32_16x16x32_bf16 v[180:183], v[100:103], a[96:99], v[180:183]\n"
      "v_mfma_f32_16x16x32_bf16 v[180:183], v[104:107], a[100:103], v[180:183]\n"
      "s_waitcnt lgkmcnt(12)\n"
      "ds_read_b64_tr_b16 a[96:97], v14 offset:6144\n"
      "v_mfma_f32_16x16x32_bf16 v[184:187], v[84:87], a[64:67], v[184:187]\n"
      "ds_read_b64_tr_b16 a[98:99], v14 offset:14336\n"
      "v_mfma_f32_16x16x32_bf16 v[184:187], v[88:91], a[68:71], v[184:187]\n"
      "ds_read_b64_tr_b16 a[100:101], v14 offset:22528\n"
      "v_mfma_f32_16x16x32_bf16 v[188:191], v[92:95], a[64:67], v[188:191]\n"
      "s_add_u32 m0, s23, 6784\n"
      "ds_read_b64_tr_b16 a[102:103], v14 offset:30720\n"
      "v_mfma_f32_16x16x32_bf16 v[188:191], v[96:99], a[68:71], v[188:191]\n"
      "buffer_load_dwordx4 v1, s[36:39], s52 offen offset:384 nt lds\n"
      "v_mfma_f32_16x16x32_bf16 v[192:195], v[100:103], a[64:67], v[192:195]\n"
      "v_add_u32 v1, 0x8000, v1\n"
      "v_mfma_f32_16x16x32_bf16 v[192:195], v[104:107], a[68:71], v[192:195]\n"
      "s_waitcnt lgkmcnt(12)\n"
      "ds_read_b64_tr_b16 a[64:65], v15 offset:6144\n"
      "v_mfma_f32_16x16x32_bf16 v[196:199], v[84:87], a[72:75], v[196:199]\n"
      "ds_read_b64_tr_b16 a[66:67], v15 offset:14336\n"
      "v_mfma_f32_16x16x32_bf16 v[196:199], v[88:91], a[76:79], v[196:199]\n"
      "buffer_load_dword v23, v22, s[60:63], 0 offen\n"
      "v_mfma_f32_16x16x32_bf16 v[200:203], v[92:95], a[72:75], v[200:203]\n"
      "ds_read_b64_tr_b16 a[68:69], v15 offset:22528\n"
      "v_add_u32 v22, 0x400, v22\n"
      "v_mfma_f32_16x16x32_bf16 v[200:203], v[96:99], a[76:79], v[200:203]\n"
      "ds_read_b64_tr_b16 a[70:71], v15 offset:30720\n"
      "v_mfma_f32_16x16x32_bf16 v[204:207], v[100:103], a[72:75], v[204:207]\n"
      "v_mfma_f32_16x16x32_bf16 v[204:207], v[104:107], a[76:79], v[204:207]\n"
      "s_waitcnt lgkmcnt(12)\n"
      "v_mfma_f32_16x16x32_bf16 v[208:211], v[84:87], a[80:83], v[208:211]\n"
      "v_mfma_f32_16x16x32_bf16 v[208:211], v[88:91], a[84:87], v[208:211]\n"
      "v_mfma_f32_16x16x32_bf16 v[212:215], v[92:95], a[80:83], v[212:215]\n"
      "v_mfma_f32_16x16x32_bf16 v[212:215], v[96:99], a[84:87], v[212:215]\n"
      "L_midN3_%=:\n"
      "v_mfma_f32_16x16x32_bf16 v[216:219], v[100:103], a[80:83], v[216:219]\n"
      "v_mfma_f32_16x16x32_bf16 v[216:219], v[104:107], a[84:87], v[216:219]\n"
      "s_waitcnt lgkmcnt(8)\n"
      "v_mfma_f32_16x16x32_bf16 v[220:223], v[84:87], a[88:91], v[220:223]\n"
      "s_waitcnt vmcnt(17) lgkmcnt(0)\n"
      "v_mfma_f32_16x16x32_bf16 v[220:223], v[88:91], a[92:95], v[220:223]\n"
      "s_barrier\n"
      "ds_read_b128 a[0:3], v3 offset:32768\n"
      "v_mfma_f32_16x16x32_bf16 v[224:227], v[92:95], a[88:91], v[224:227]\n"
      "ds_read_b128 a[4:7], v4 offset:32768\n"
      "v_mfma_f32_16x16x32_bf16 v[224:227], v[96:99], a[92:95], v[224:227]\n"
      "ds_read_b128 a[8:11], v3 offset:34816\n"
      "v_mfma_f32_16x16x32_bf16 v[228:231], v[100:103], a[88:91], v[228:231]\n"
      "s_add_u32 m0, s23, 65536\n"
      "ds_read_b128 a[12:15], v4 offset:34816\n"
      "v_mfma_f32_16x16x32_bf16 v[228:231], v[104:107], a[92:95], v[228:231]\n"
      "buffer_load_dwordx4 v2, s[40:43], 0 offen offset:0 nt lds\n"
      "v_mfma_f32_16x16x32_bf16 v[232:235], v[84:87], a[96:99], v[232:235]\n"
      "ds_read_b128 a[16:19], v3 offset:36864\n"
      "v_mfma_f32_16x16x32_bf16 v[232:235], v[88:91], a[100:103], v[232:235]\n"
      "ds_read_b128 a[20:23], v4 offset:36864\n"
      "v_mfma_f32_16x16x32_bf16 v[236:239], v[92:95], a[96:99], v[236:239]\n"
      "ds_read_b128 a[24:27], v3 offset:38912\n"
      "v_mfma_f32_16x16x32_bf16 v[236:239], v[96:99], a[100:103], v[236:239]\n"
      "ds_read_b128 a[28:31], v4 offset:38912\n"
      "v_mfma_f32_16x16x32_bf16 v[240:243], v[100:103], a[96:99], v[240:243]\n"
      "ds_read_b128 a[32:35], v3 offset:40960\n"
      "v_mfma_f32_16x16x32_bf16 v[240:243], v[104:107], a[100:103], v[240:243]\n"
      "ds_read_b128 a[36:39], v4 offset:40960\n"
      "v_mfma_f32_16x16x32_bf16 v[244:247], v[84:87], a[64:67], v[244:247]\n"
      "ds_read_b128 a[40:43], v3 offset:43008\n"
      "v_mfma_f32_16x16x32_bf16 v[244:247], v[88:91], a[68:71], v[244:247]\n"
      "ds_read_b128 a[44:47], v4 offset:43008\n"
      "v_mfma_f32_16x16x32_bf16 v[248:251], v[92:95], a[64:67], v[248:251]\n"
      "ds_read_b128 a[48:51], v3 offset:45056\n"
      "v_mfma_f32_16x16x32_bf16 v[248:251], v[96:99], a[68:71], v[248:251]\n"
      "ds_read_b128 a[52:55], v4 offset:45056\n"
      "v_mfma_f32_16x16x32_bf16 v[252:255], v[100:103], a[64:67], v[252:255]\n"
      "s_add_u32 m0, s23, 66560\n"
      "ds_read_b128 a[56:59], v3 offset:47104\n"
      "v_mfma_f32_16x16x32_bf16 v[252:255], v[104:107], a[68:71], v[252:255]\n"
      "s_waitcnt lgkmcnt(7)\n"
      "buffer_load_dwordx4 v2, s[40:43], s52 offen offset:0 nt lds\n"
      "v_mfma_f32_16x16x32_bf16 v[36:39], a[0:3], a[104:107], v[24:27]\n"
      "ds_read_b128 a[60:63], v4 offset:47104\n"
      "v_mfma_f32_16x16x32_bf16 v[36:39], a[4:7], a[108:111], v[36:39]\n"
      "v_mfma_f32_16x16x32_bf16 v[36:39], a[8:11], a[112:115], v[36:39]\n"
      "v_mfma_f32_16x16x32_bf16 v[36:39], a[12:15], a[116:119], v[36:39]\n"
      "v_mfma_f32_16x16x32_bf16 v[36:39], a[16:19], a[120:123], v[36:39]\n"
      "v_mfma_f32_16x16x32_bf16 v[36:39], a[20:23], a[124:127], v[36:39]\n"
      "v_mfma_f32_16x16x32_bf16 v[36:39], a[24:27], a[128:131], v[36:39]\n"
      "s_add_u32 m0, s23, 67456\n"
      "v_mfma_f32_16x16x32_bf16 v[36:39], a[28:31], a[132:135], v[36:39]\n"
      "buffer_load_dwordx4 v2, s[40:43], 0 offen offset:128 nt lds\n"
      "v_mfma_f32_16x16x32_bf16 v[40:43], a[0:3], a[136:139], v[28:31]\n"
      "v_mfma_f32_16x16x32_bf16 v[40:43], a[4:7], a[140:143], v[40:43]\n"
      "v_mfma_f32_16x16x32_bf16 v[40:43], a[8:11], a[144:147], v[40:43]\n"
      "v_exp_f32 v36, v36\n"
      "v_mfma_f32_16x16x32_bf16 v[40:43], a[12:15], a[148:151], v[40:43]\n"
      "v_add_f32 v6, v6, v36\n"
      "v_mfma_f32_16x16x32_bf16 v[40:43], a[16:19], a[152:155], v[40:43]\n"
      "v_exp_f32 v37, v37\n"
      "v_mfma_f32_16x16x32_bf16 v[40:43], a[20:23], a[156:159], v[40:43]\n"
      "v_cvt_pk_bf16_f32 v84, v36, v37\n"
      "v_add_f32 v6, v6, v37\n"
      "v_mfma_f32_16x16x32_bf16 v[40:43], a[24:27], a[160:163], v[40:43]\n"
      "v_exp_f32 v38, v38\n"
      "v_mfma_f32_16x16x32_bf16 v[40:43], a[28:31], a[164:167], v[40:43]\n"
      "v_add_f32 v6, v6, v38\n"
      "v_mfma_f32_16x16x32_bf16 v[44:47], a[0:3], a[168:171], v[32:35]\n"
      "v_exp_f32 v39, v39\n"
      "v_mfma_f32_16x16x32_bf16 v[44:47], a[4:7], a[172:175], v[44:47]\n"
      "ds_read_b128 a[0:3], v3 offset:49152\n"
      "v_cvt_pk_bf16_f32 v85, v38, v39\n"
      "v_mfma_f32_16x16x32_bf16 v[44:47], a[8:11], a[176:179], v[44:47]\n"
      "s_add_u32 m0, s23, 68480\n"
      "v_exp_f32 v40, v40\n"
      "v_mfma_f32_16x16x32_bf16 v[44:47], a[12:15], a[180:183], v[44:47]\n"
      "buffer_load_dwordx4 v2, s[40:43], s52 offen offset:128 nt lds\n"
      "v_mfma_f32_16x16x32_bf16 v[44:47], a[16:19], a[184:187], v[44:47]\n"
      "v_exp_f32 v41, v41\n"
      "v_mfma_f32_16x16x32_bf16 v[44:47], a[20:23], a[188:191], v[44:47]\n"
      "ds_read_b128 a[4:7], v4 offset:49152\n"
      "v_cvt_pk_bf16_f32 v92, v40, v41\n"
      "v_mfma_f32_16x16x32_bf16 v[44:47], a[24:27], a[192:195], v[44:47]\n"
      "v_exp_f32 v42, v42\n"
      "v_mfma_f32_16x16x32_bf16 v[44:47], a[28:31], a[196:199], v[44:47]\n"
      "s_waitcnt lgkmcnt(2)\n"
      "ds_read_b128 a[8:11], v3 offset:51200\n"
      "v_add_f32 v6, v6, v39\n"
      "v_mfma_f32_16x16x32_bf16 v[48:51], a[32:35], a[104:107], v[24:27]\n"
      "v_exp_f32 v43, v43\n"
      "v_mfma_f32_16x16x32_bf16 v[48:51], a[36:39], a[108:111], v[48:51]\n"
      "ds_read_b128 a[12:15], v4 offset:51200\n"
      "v_cvt_pk_bf16_f32 v93, v42, v43\n"
      "v_mfma_f32_16x16x32_bf16 v[48:51], a[40:43], a[112:115], v[48:51]\n"
      "s_add_u32 m0, s23, 69376\n"
      "v_exp_f32 v44, v44\n"
      "v_mfma_f32_16x16x32_bf16 v[48:51], a[44:47], a[116:119], v[48:51]\n"
      "buffer_load_dwordx4 v2, s[40:43], 0 offen offset:256 nt lds\n"
      "v_mfma_f32_16x16x32_bf16 v[48:51], a[48:51], a[120:123], v[48:51]\n"
      "v_exp_f32 v45, v45\n"
      "v_mfma_f32_16x16x32_bf16 v[48:51], a[52:55], a[124:127], v[48:51]\n"
      "ds_read_b128 a[16:19], v3 offset:53248\n"
      "v_cvt_pk_bf16_f32 v100, v44, v45\n"
      "v_mfma_f32_16x16x32_bf16 v[48:51], a[56:59], a[128:131], v[48:51]\n"
      "v_exp_f32 v46, v46\n"
      "v_mfma_f32_16x16x32_bf16 v[48:51], a[60:63], a[132:135], v[48:51]\n"
      "ds_read_b128 a[20:23], v4 offset:53248\n"
      "v_add_f32 v8, v8, v40\n"
      "v_mfma_f32_16x16x32_bf16 v[52:55], a[32:35], a[136:139], v[28:31]\n"
      "v_exp_f32 v47, v47\n"
      "v_mfma_f32_16x16x32_bf16 v[52:55], a[36:39], a[140:143], v[52:55]\n"
      "ds_read_b128 a[24:27], v3 offset:55296\n"
      "v_cvt_pk_bf16_f32 v101, v46, v47\n"
      "v_mfma_f32_16x16x32_bf16 v[52:55], a[40:43], a[144:147], v[52:55]\n"
      "v_exp_f32 v48, v48\n"
      "v_mfma_f32_16x16x32_bf16 v[52:55], a[44:47], a[148:151], v[52:55]\n"
      "ds_read_b128 a[28:31], v4 offset:55296\n"
      "v_add_f32 v8, v8, v41\n"
      "v_mfma_f32_16x16x32_bf16 v[52:55], a[48:51], a[152:155], v[52:55]\n"
      "v_exp_f32 v49, v49\n"
      "v_mfma_f32_16x16x32_bf16 v[52:55], a[52:55], a[156:159], v[52:55]\n"
      "v_cvt_pk_bf16_f32 v86, v48, v49\n"
      "v_add_f32 v8, v8, v42\n"
      "v_mfma_f32_16x16x32_bf16 v[52:55], a[56:59], a[160:163], v[52:55]\n"
      "s_add_u32 m0, s23, 70400\n"
      "v_exp_f32 v50, v50\n"
      "v_mfma_f32_16x16x32_bf16 v[52:55], a[60:63], a[164:167], v[52:55]\n"
      "buffer_load_dwordx4 v2, s[40:43], s52 offen offset:256 nt lds\n"
      "v_mfma_f32_16x16x32_bf16 v[56:59], a[32:35], a[168:171], v[32:35]\n"
      "v_exp_f32 v51, v51\n"
      "v_mfma_f32_16x16x32_bf16 v[56:59], a[36:39], a[172:175], v[56:59]\n"
      "ds_read_b128 a[32:35], v3 offset:57344\n"
      "v_cvt_pk_bf16_f32 v87, v50, v51\n"
      "v_mfma_f32_16x16x32_bf16 v[56:59], a[40:43], a[176:179], v[56:59]\n"
      "v_exp_f32 v52, v52\n"
      "v_mfma_f32_16x16x32_bf16 v[56:59], a[44:47], a[180:183], v[56:59]\n"
      "ds_read_b128 a[36:39], v4 offset:57344\n"
      "v_add_f32 v8, v8, v43\n"
      "v_mfma_f32_16x16x32_bf16 v[56:59], a[48:51], a[184:187], v[56:59]\n"
      "v_exp_f32 v53, v53\n"
      "v_mfma_f32_16x16x32_bf16 v[56:59], a[52:55], a[188:191], v[56:59]\n"
      "ds_read_b128 a[40:43], v3 offset:59392\n"
      "v_cvt_pk_bf16_f32 v94, v52, v53\n"
      "v_mfma_f32_16x16x32_bf16 v[56:59], a[56:59], a[192:195], v[56:59]\n"
      "s_add_u32 m0, s23, 71296\n"
      "v_exp_f32 v54, v54\n"
      "v_mfma_f32_16x16x32_bf16 v[56:59], a[60:63], a[196:199], v[56:59]\n"
      "s_waitcnt lgkmcnt(3)\n"
      "buffer_load_dwordx4 v2, s[40:43], 0 offen offset:384 nt lds\n"
      "v_mfma_f32_16x16x32_bf16 v[60:63], a[0:3], a[104:107], v[24:27]\n"
      "v_exp_f32 v55, v55\n"
      "v_mfma_f32_16x16x32_bf16 v[60:63], a[4:7], a[108:111], v[60:63]\n"
      "ds_read_b128 a[44:47], v4 offset:59392\n"
      "v_cvt_pk_bf16_f32 v95, v54, v55\n"
      "v_mfma_f32_16x16x32_bf16 v[60:63], a[8:11], a[112:115], v[60:63]\n"
      "v_exp_f32 v56, v56\n"
      "v_mfma_f32_16x16x32_bf16 v[60:63], a[12:15], a[116:119], v[60:63]\n"
      "ds_read_b128 a[48:51], v3 offset:61440\n"
      "v_add_f32 v10, v10, v44\n"
      "v_mfma_f32_16x16x32_bf16 v[60:63], a[16:19], a[120:123], v[60:63]\n"
      "v_exp_f32 v57, v57\n"
      "v_mfma_f32_16x16x32_bf16 v[60:63], a[20:23], a[124:127], v[60:63]\n"
      "ds_read_b128 a[52:55], v4 offset:61440\n"
      "v_cvt_pk_bf16_f32 v102, v56, v57\n"
      "v_mfma_f32_16x16x32_bf16 v[60:63], a[24:27], a[128:131], v[60:63]\n"
      "v_exp_f32 v58, v58\n"
      "v_mfma_f32_16x16x32_bf16 v[60:63], a[28:31], a[132:135], v[60:63]\n"
      "ds_read_b128 a[56:59], v3 offset:63488\n"
      "v_add_f32 v10, v10, v45\n"
      "v_mfma_f32_16x16x32_bf16 v[64:67], a[0:3], a[136:139], v[28:31]\n"
      "v_exp_f32 v59, v59\n"
      "v_mfma_f32_16x16x32_bf16 v[64:67], a[4:7], a[140:143], v[64:67]\n"
      "ds_read_b128 a[60:63], v4 offset:63488\n"
      "v_cvt_pk_bf16_f32 v103, v58, v59\n"
      "v_mfma_f32_16x16x32_bf16 v[64:67], a[8:11], a[144:147], v[64:67]\n"
      "s_add_u32 m0, s23, 72320\n"
      "v_exp_f32 v60, v60\n"
      "v_mfma_f32_16x16x32_bf16 v[64:67], a[12:15], a[148:151], v[64:67]\n"
      "buffer_load_dwordx4 v2, s[40:43], s52 offen offset:384 nt lds\n"
      "v_mfma_f32_16x16x32_bf16 v[64:67], a[16:19], a[152:155], v[64:67]\n"
      "v_exp_f32 v61, v61\n"
      "v_mfma_f32_16x16x32_bf16 v[64:67], a[20:23], a[156:159], v[64:67]\n"
      "v_cvt_pk_bf16_f32 v88, v60, v61\n"
      "v_add_u32 v2, 0x8000, v2\n"
      "v_mfma_f32_16x16x32_bf16 v[64:67], a[24:27], a[160:163], v[64:67]\n"
      "v_exp_f32 v62, v62\n"
      "v_mfma_f32_16x16x32_bf16 v[64:67], a[28:31], a[164:167], v[64:67]\n"
      "v_add_f32 v10, v10, v46\n"
      "v_add_f32 v10, v10, v47\n"
      "v_mfma_f32_16x16x32_bf16 v[68:71], a[0:3], a[168:171], v[32:35]\n"
      "v_exp_f32 v63, v63\n"
      "v_mfma_f32_16x16x32_bf16 v[68:71], a[4:7], a[172:175], v[68:71]\n"
      "v_cvt_pk_bf16_f32 v89, v62, v63\n"
      "v_add_f32 v6, v6, v48\n"
      "v_mfma_f32_16x16x32_bf16 v[68:71], a[8:11], a[176:179], v[68:71]\n"
      "s_waitcnt vmcnt(17) lgkmcnt(0)\n"
      "v_exp_f32 v64, v64\n"
      "v_mfma_f32_16x16x32_bf16 v[68:71], a[12:15], a[180:183], v[68:71]\n"
      "s_barrier\n"
      "ds_read_b64_tr_b16 a[64:65], v12 offset:32768\n"
      "v_add_f32 v6, v6, v49\n"
      "v_mfma_f32_16x16x32_bf16 v[68:71], a[16:19], a[184:187], v[68:71]\n"
      "v_exp_f32 v65, v65\n"
      "v_mfma_f32_16x16x32_bf16 v[68:71], a[20:23], a[188:191], v[68:71]\n"
      "ds_read_b64_tr_b16 a[66:67], v12 offset:40960\n"
      "v_cvt_pk_bf16_f32 v96, v64, v65\n"
      "v_mfma_f32_16x16x32_bf16 v[68:71], a[24:27], a[192:195], v[68:71]\n"
      "v_exp_f32 v66, v66\n"
      "v_mfma_f32_16x16x32_bf16 v[68:71], a[28:31], a[196:199], v[68:71]\n"
      "ds_read_b64_tr_b16 a[68:69], v12 offset:49152\n"
      "v_add_f32 v6, v6, v50\n"
      "v_mfma_f32_16x16x32_bf16 v[72:75], a[32:35], a[104:107], v[24:27]\n"
      "v_exp_f32 v67, v67\n"
      "v_mfma_f32_16x16x32_bf16 v[72:75], a[36:39], a[108:111], v[72:75]\n"
      "ds_read_b64_tr_b16 a[70:71], v12 offset:57344\n"
      "v_cvt_pk_bf16_f32 v97, v66, v67\n"
      "v_mfma_f32_16x16x32_bf16 v[72:75], a[40:43], a[112:115], v[72:75]\n"
      "v_exp_f32 v68, v68\n"
      "v_mfma_f32_16x16x32_bf16 v[72:75], a[44:47], a[116:119], v[72:75]\n"
      "ds_read_b64_tr_b16 a[72:73], v13 offset:32768\n"
      "v_add_f32 v6, v6, v51\n"
      "v_mfma_f32_16x16x32_bf16 v[72:75], a[48:51], a[120:123], v[72:75]\n"
      "v_exp_f32 v69, v69\n"
      "v_mfma_f32_16x16x32_bf16 v[72:75], a[52:55], a[124:127], v[72:75]\n"
      "ds_read_b64_tr_b16 a[74:75], v13 offset:40960\n"
      "v_cvt_pk_bf16_f32 v104, v68, v69\n"
      "v_mfma_f32_16x16x32_bf16 v[72:75], a[56:59], a[128:131], v[72:75]\n"
      "v_exp_f32 v70, v70\n"
      "v_mfma_f32_16x16x32_bf16 v[72:75], a[60:63], a[132:135], v[72:75]\n"
      "ds_read_b64_tr_b16 a[76:77], v13 offset:49152\n"
      "v_add_f32 v8, v8, v52\n"
      "v_mfma_f32_16x16x32_bf16 v[76:79], a[32:35], a[136:139], v[28:31]\n"
      "v_exp_f32 v71, v71\n"
      "v_mfma_f32_16x16x32_bf16 v[76:79], a[36:39], a[140:143], v[76:79]\n"
      "ds_read_b64_tr_b16 a[78:79], v13 offset:57344\n"
      "v_cvt_pk_bf16_f32 v105, v70, v71\n"
      "v_mfma_f32_16x16x32_bf16 v[76:79], a[40:43], a[144:147], v[76:79]\n"
      "v_exp_f32 v72, v72\n"
      "v_mfma_f32_16x16x32_bf16 v[76:79], a[44:47], a[148:151], v[76:79]\n"
      "ds_read_b64_tr_b16 a[80:81], v14 offset:32768\n"
      "v_add_f32 v8, v8, v53\n"
      "v_mfma_f32_16x16x32_bf16 v[76:79], a[48:51], a[152:155], v[76:79]\n"
      "v_exp_f32 v73, v73\n"
      "v_mfma_f32_16x16x32_bf16 v[76:79], a[52:55], a[156:159], v[76:79]\n"
      "ds_read_b64_tr_b16 a[82:83], v14 offset:40960\n"
      "v_cvt_pk_bf16_f32 v90, v72, v73\n"
      "v_mfma_f32_16x16x32_bf16 v[76:79], a[56:59], a[160:163], v[76:79]\n"
      "v_exp_f32 v74, v74\n"
      "v_mfma_f32_16x16x32_bf16 v[76:79], a[60:63], a[164:167], v[76:79]\n"
      "ds_read_b64_tr_b16 a[84:85], v14 offset:49152\n"
      "v_add_f32 v8, v8, v54\n"
      "v_mfma_f32_16x16x32_bf16 v[80:83], a[32:35], a[168:171], v[32:35]\n"
      "v_exp_f32 v75, v75\n"
      "v_mfma_f32_16x16x32_bf16 v[80:83], a[36:39], a[172:175], v[80:83]\n"
      "ds_read_b64_tr_b16 a[86:87], v14 offset:57344\n"
      "v_cvt_pk_bf16_f32 v91, v74, v75\n"
      "v_mfma_f32_16x16x32_bf16 v[80:83], a[40:43], a[176:179], v[80:83]\n"
      "v_exp_f32 v76, v76\n"
      "v_mfma_f32_16x16x32_bf16 v[80:83], a[44:47], a[180:183], v[80:83]\n"
      "v_add_f32 v8, v8, v55\n"
      "v_mfma_f32_16x16x32_bf16 v[80:83], a[48:51], a[184:187], v[80:83]\n"
      "v_exp_f32 v77, v77\n"
      "v_mfma_f32_16x16x32_bf16 v[80:83], a[52:55], a[188:191], v[80:83]\n"
      "v_cvt_pk_bf16_f32 v98, v76, v77\n"
      "v_mfma_f32_16x16x32_bf16 v[80:83], a[56:59], a[192:195], v[80:83]\n"
      "s_sub_u32 s20, s20, 1\n"
      "v_exp_f32 v78, v78\n"
      "v_mfma_f32_16x16x32_bf16 v[80:83], a[60:63], a[196:199], v[80:83]\n"
      "s_waitcnt lgkmcnt(8)\n"
      "s_cbranch_scc1 L_tail_%=\n"
      "v_mfma_f32_16x16x32_bf16 a[200:203], v[84:87], a[64:67], a[200:203]\n"
      "v_exp_f32 v79, v79\n"
      "v_mfma_f32_16x16x32_bf16 a[200:203], v[88:91], a[68:71], a[200:203]\n"
      "s_waitcnt lgkmcnt(4)\n"
      "ds_read_b64_tr_b16 a[88:89], v15 offset:32768\n"
      "v_cvt_pk_bf16_f32 v99, v78, v79\n"
      "v_mfma_f32_16x16x32_bf16 a[212:215], v[84:87], a[72:75], a[212:215]\n"
      "s_add_u32 m0, s23, 32768\n"
      "v_exp_f32 v80, v80\n"
      "v_mfma_f32_16x16x32_bf16 a[212:215], v[88:91], a[76:79], a[212:215]\n"
      "s_waitcnt lgkmcnt(1)\n"
      "buffer_load_dwordx4 v1, s[36:39], 0 offen offset:0 nt lds\n"
      "v_mfma_f32_16x16x32_bf16 a[224:227], v[84:87], a[80:83], a[224:227]\n"
      "v_exp_f32 v81, v81\n"
      "v_mfma_f32_16x16x32_bf16 a[224:227], v[88:91], a[84:87], a[224:227]\n"
      "ds_read_b64_tr_b16 a[90:91], v15 offset:40960\n"
      "v_cvt_pk_bf16_f32 v106, v80, v81\n"
      "v_mfma_f32_16x16x32_bf16 a[204:207], v[92:95], a[64:67], a[204:207]\n"
      "v_exp_f32 v82, v82\n"
      "v_mfma_f32_16x16x32_bf16 a[204:207], v[96:99], a[68:71], a[204:207]\n"
      "ds_read_b64_tr_b16 a[92:93], v15 offset:49152\n"
      "v_add_f32 v10, v10, v56\n"
      "v_mfma_f32_16x16x32_bf16 a[216:219], v[92:95], a[72:75], a[216:219]\n"
      "v_exp_f32 v83, v83\n"
      "v_mfma_f32_16x16x32_bf16 a[216:219], v[96:99], a[76:79], a[216:219]\n"
      "ds_read_b64_tr_b16 a[94:95], v15 offset:57344\n"
      "v_cvt_pk_bf16_f32 v107, v82, v83\n"
      "v_mfma_f32_16x16x32_bf16 a[228:231], v[92:95], a[80:83], a[228:231]\n"
      "s_add_u32 m0, s23, 33792\n"
      "ds_read_b64_tr_b16 a[96:97], v12 offset:34816\n"
      "v_add_f32 v10, v10, v57\n"
      "v_mfma_f32_16x16x32_bf16 a[228:231], v[96:99], a[84:87], a[228:231]\n"
      "buffer_load_dwordx4 v1, s[36:39], s52 offen offset:0 nt lds\n"
      "v_mfma_f32_16x16x32_bf16 a[208:211], v[100:103], a[64:67], a[208:211]\n"
      "ds_read_b64_tr_b16 a[98:99], v12 offset:43008\n"
      "v_add_f32 v10, v10, v58\n"
      "v_mfma_f32_16x16x32_bf16 a[208:211], v[104:107], a[68:71], a[208:211]\n"
      "ds_read_b64_tr_b16 a[100:101], v12 offset:51200\n"
      "v_add_f32 v10, v10, v59\n"
      "v_mfma_f32_16x16x32_bf16 a[220:223], v[100:103], a[72:75], a[220:223]\n"
      "ds_read_b64_tr_b16 a[102:103], v12 offset:59392\n"
      "v_add_f32 v6, v6, v60\n"
      "v_mfma_f32_16x16x32_bf16 a[220:223], v[104:107], a[76:79], a[220:223]\n"
      "ds_read_b64_tr_b16 a[64:65], v13 offset:34816\n"
      "v_add_f32 v6, v6, v61\n"
      "v_mfma_f32_16x16x32_bf16 a[232:235], v[100:103], a[80:83], a[232:235]\n"
      "ds_read_b64_tr_b16 a[66:67], v13 offset:43008\n"
      "v_add_f32 v6, v6, v62\n"
      "v_mfma_f32_16x16x32_bf16 a[232:235], v[104:107], a[84:87], a[232:235]\n"
      "s_waitcnt lgkmcnt(6)\n"
      "ds_read_b64_tr_b16 a[68:69], v13 offset:51200\n"
      "v_add_f32 v6, v6, v63\n"
      "v_mfma_f32_16x16x32_bf16 a[236:239], v[84:87], a[88:91], a[236:239]\n"
      "s_add_u32 m0, s23, 34688\n"
      "ds_read_b64_tr_b16 a[70:71], v13 offset:59392\n"
      "v_add_f32 v8, v8, v64\n"
      "v_mfma_f32_16x16x32_bf16 a[236:239], v[88:91], a[92:95], a[236:239]\n"
      "buffer_load_dwordx4 v1, s[36:39], 0 offen offset:128 nt lds\n"
      "v_mfma_f32_16x16x32_bf16 a[240:243], v[92:95], a[88:91], a[240:243]\n"
      "ds_read_b64_tr_b16 a[72:73], v14 offset:34816\n"
      "v_add_f32 v8, v8, v65\n"
      "v_mfma_f32_16x16x32_bf16 a[240:243], v[96:99], a[92:95], a[240:243]\n"
      "ds_read_b64_tr_b16 a[74:75], v14 offset:43008\n"
      "v_add_f32 v8, v8, v66\n"
      "v_mfma_f32_16x16x32_bf16 v[108:111], v[100:103], a[88:91], v[108:111]\n"
      "ds_read_b64_tr_b16 a[76:77], v14 offset:51200\n"
      "v_add_f32 v8, v8, v67\n"
      "v_mfma_f32_16x16x32_bf16 v[108:111], v[104:107], a[92:95], v[108:111]\n"
      "s_waitcnt lgkmcnt(7)\n"
      "ds_read_b64_tr_b16 a[78:79], v14 offset:59392\n"
      "v_add_f32 v10, v10, v68\n"
      "v_mfma_f32_16x16x32_bf16 v[112:115], v[84:87], a[96:99], v[112:115]\n"
      "ds_read_b64_tr_b16 a[80:81], v15 offset:34816\n"
      "v_add_f32 v10, v10, v69\n"
      "v_mfma_f32_16x16x32_bf16 v[112:115], v[88:91], a[100:103], v[112:115]\n"
      "ds_read_b64_tr_b16 a[82:83], v15 offset:43008\n"
      "v_add_f32 v10, v10, v70\n"
      "v_mfma_f32_16x16x32_bf16 v[116:119], v[92:95], a[96:99], v[116:119]\n"
      "s_add_u32 m0, s23, 35712\n"
      "ds_read_b64_tr_b16 a[84:85], v15 offset:51200\n"
      "v_add_f32 v10, v10, v71\n"
      "v_mfma_f32_16x16x32_bf16 v[116:119], v[96:99], a[100:103], v[116:119]\n"
      "buffer_load_dwordx4 v1, s[36:39], s52 offen offset:128 nt lds\n"
      "v_mfma_f32_16x16x32_bf16 v[120:123], v[100:103], a[96:99], v[120:123]\n"
      "ds_read_b64_tr_b16 a[86:87], v15 offset:59392\n"
      "v_add_f32 v6, v6, v72\n"
      "v_mfma_f32_16x16x32_bf16 v[120:123], v[104:107], a[100:103], v[120:123]\n"
      "s_waitcnt lgkmcnt(8)\n"
      "ds_read_b64_tr_b16 a[88:89], v12 offset:36864\n"
      "v_add_f32 v6, v6, v73\n"
      "v_mfma_f32_16x16x32_bf16 v[124:127], v[84:87], a[64:67], v[124:127]\n"
      "ds_read_b64_tr_b16 a[90:91], v12 offset:45056\n"
      "v_add_f32 v6, v6, v74\n"
      "v_mfma_f32_16x16x32_bf16 v[124:127], v[88:91], a[68:71], v[124:127]\n"
      "ds_read_b64_tr_b16 a[92:93], v12 offset:53248\n"
      "v_add_f32 v6, v6, v75\n"
      "v_mfma_f32_16x16x32_bf16 v[128:131], v[92:95], a[64:67], v[128:131]\n"
      "ds_read_b64_tr_b16 a[94:95], v12 offset:61440\n"
      "v_add_f32 v8, v8, v76\n"
      "v_mfma_f32_16x16x32_bf16 v[128:131], v[96:99], a[68:71], v[128:131]\n"
      "ds_read_b64_tr_b16 a[96:97], v13 offset:36864\n"
      "v_add_f32 v8, v8, v77\n"
      "v_mfma_f32_16x16x32_bf16 v[132:135], v[100:103], a[64:67], v[132:135]\n"
      "ds_read_b64_tr_b16 a[98:99], v13 offset:45056\n"
      "v_add_f32 v8, v8, v78\n"
      "v_mfma_f32_16x16x32_bf16 v[132:135], v[104:107], a[68:71], v[132:135]\n"
      "s_waitcnt lgkmcnt(10)\n"
      "ds_read_b64_tr_b16 a[100:101], v13 offset:53248\n"
      "v_add_f32 v8, v8, v79\n"
      "v_mfma_f32_16x16x32_bf16 v[136:139], v[84:87], a[72:75], v[136:139]\n"
      "ds_read_b64_tr_b16 a[102:103], v13 offset:61440\n"
      "v_add_f32 v10, v10, v80\n"
      "v_mfma_f32_16x16x32_bf16 v[136:139], v[88:91], a[76:79], v[136:139]\n"
      "ds_read_b64_tr_b16 a[64:65], v14 offset:36864\n"
      "v_add_f32 v10, v10, v81\n"
      "v_mfma_f32_16x16x32_bf16 v[140:143], v[92:95], a[72:75], v[140:143]\n"
      "s_add_u32 m0, s23, 36608\n"
      "ds_read_b64_tr_b16 a[66:67], v14 offset:45056\n"
      "v_add_f32 v10, v10, v82\n"
      "v_mfma_f32_16x16x32_bf16 v[140:143], v[96:99], a[76:79], v[140:143]\n"
      "buffer_load_dwordx4 v1, s[36:39], 0 offen offset:256 nt lds\n"
      "v_mfma_f32_16x16x32_bf16 v[144:147], v[100:103], a[72:75], v[144:147]\n"
      "ds_read_b64_tr_b16 a[68:69], v14 offset:53248\n"
      "v_add_f32 v10, v10, v83\n"
      "v_mfma_f32_16x16x32_bf16 v[144:147], v[104:107], a[76:79], v[144:147]\n"
      "s_waitcnt lgkmcnt(11)\n"
      "ds_read_b64_tr_b16 a[70:71], v14 offset:61440\n"
      "v_mfma_f32_16x16x32_bf16 v[148:151], v[84:87], a[80:83], v[148:151]\n"
      "ds_read_b64_tr_b16 a[72:73], v15 offset:36864\n"
      "v_mfma_f32_16x16x32_bf16 v[148:151], v[88:91], a[84:87], v[148:151]\n"
      "ds_read_b64_tr_b16 a[74:75], v15 offset:45056\n"
      "v_mfma_f32_16x16x32_bf16 v[152:155], v[92:95], a[80:83], v[152:155]\n"
      "ds_read_b64_tr_b16 a[76:77], v15 offset:53248\n"
      "v_mfma_f32_16x16x32_bf16 v[152:155], v[96:99], a[84:87], v[152:155]\n"
      "ds_read_b64_tr_b16 a[78:79], v15 offset:61440\n"
      "v_mfma_f32_16x16x32_bf16 v[156:159], v[100:103], a[80:83], v[156:159]\n"
      "s_add_u32 m0, s23, 37632\n"
      "v_mfma_f32_16x16x32_bf16 v[156:159], v[104:107], a[84:87], v[156:159]\n"
      "s_waitcnt lgkmcnt(12)\n"
      "buffer_load_dwordx4 v1, s[36:39], s52 offen offset:256 nt lds\n"
      "v_mfma_f32_16x16x32_bf16 v[160:163], v[84:87], a[88:91], v[160:163]\n"
      "ds_read_b64_tr_b16 a[80:81], v12 offset:38912\n"
      "v_mfma_f32_16x16x32_bf16 v[160:163], v[88:91], a[92:95], v[160:163]\n"
      "ds_read_b64_tr_b16 a[82:83], v12 offset:47104\n"
      "v_mfma_f32_16x16x32_bf16 v[164:167], v[92:95], a[88:91], v[164:167]\n"
      "ds_read_b64_tr_b16 a[84:85], v12 offset:55296\n"
      "v_mfma_f32_16x16x32_bf16 v[164:167], v[96:99], a[92:95], v[164:167]\n"
      "ds_read_b64_tr_b16 a[86:87], v12 offset:63488\n"
      "v_mfma_f32_16x16x32_bf16 v[168:171], v[100:103], a[88:91], v[168:171]\n"
      "v_mfma_f32_16x16x32_bf16 v[168:171], v[104:107], a[92:95], v[168:171]\n"
      "s_waitcnt lgkmcnt(12)\n"
      "ds_read_b64_tr_b16 a[88:89], v13 offset:38912\n"
      "v_mfma_f32_16x16x32_bf16 v[172:175], v[84:87], a[96:99], v[172:175]\n"
      "s_add_u32 m0, s23, 38528\n"
      "ds_read_b64_tr_b16 a[90:91], v13 offset:47104\n"
      "v_mfma_f32_16x16x32_bf16 v[172:175], v[88:91], a[100:103], v[172:175]\n"
      "buffer_load_dwordx4 v1, s[36:39], 0 offen offset:384 nt lds\n"
      "v_mfma_f32_16x16x32_bf16 v[176:179], v[92:95], a[96:99], v[176:179]\n"
      "ds_read_b64_tr_b16 a[92:93], v13 offset:55296\n"
      "v_mfma_f32_16x16x32_bf16 v[176:179], v[96:99], a[100:103], v[176:179]\n"
      "ds_read_b64_tr_b16 a[94:95], v13 offset:63488\n"
      "v_mfma_f32_16x16x32_bf16 v[180:183], v[100:103], a[96:99], v[180:183]\n"
      "v_mfma_f32_16x16x32_bf16 v[180:183], v[104:107], a[100:103], v[180:183]\n"
      "s_waitcnt lgkmcnt(12)\n"
      "ds_read_b64_tr_b16 a[96:97], v14 offset:38912\n"
      "v_mfma_f32_16x16x32_bf16 v[184:187], v[84:87], a[64:67], v[184:187]\n"
      "ds_read_b64_tr_b16 a[98:99], v14 offset:47104\n"
      "v_mfma_f32_16x16x32_bf16 v[184:187], v[88:91], a[68:71], v[184:187]\n"
      "ds_read_b64_tr_b16 a[100:101], v14 offset:55296\n"
      "v_mfma_f32_16x16x32_bf16 v[188:191], v[92:95], a[64:67], v[188:191]\n"
      "s_add_u32 m0, s23, 39552\n"
      "ds_read_b64_tr_b16 a[102:103], v14 offset:63488\n"
      "v_mfma_f32_16x16x32_bf16 v[188:191], v[96:99], a[68:71], v[188:191]\n"
      "buffer_load_dwordx4 v1, s[36:39], s52 offen offset:384 nt lds\n"
      "v_mfma_f32_16x16x32_bf16 v[192:195], v[100:103], a[64:67], v[192:195]\n"
      "v_add_u32 v1, 0x8000, v1\n"
      "v_mfma_f32_16x16x32_bf16 v[192:195], v[104:107], a[68:71], v[192:195]\n"
      "s_waitcnt lgkmcnt(12)\n"
      "ds_read_b64_tr_b16 a[64:65], v15 offset:38912\n"
      "v_mfma_f32_16x16x32_bf16 v[196:199], v[84:87], a[72:75], v[196:199]\n"
      "ds_read_b64_tr_b16 a[66:67], v15 offset:47104\n"
      "v_mfma_f32_16x16x32_bf16 v[196:199], v[88:91], a[76:79], v[196:199]\n"
      "buffer_load_dword v23, v22, s[60:63], 0 offen\n"
      "v_mfma_f32_16x16x32_bf16 v[200:203], v[92:95], a[72:75], v[200:203]\n"
      "ds_read_b64_tr_b16 a[68:69], v15 offset:55296\n"
      "v_add_u32 v22, 0x400, v22\n"
      "v_mfma_f32_16x16x32_bf16 v[200:203], v[96:99], a[76:79], v[200:203]\n"
      "ds_read_b64_tr_b16 a[70:71], v15 offset:63488\n"
      "v_mfma_f32_16x16x32_bf16 v[204:207], v[100:103], a[72:75], v[204:207]\n"
      "v_mfma_f32_16x16x32_bf16 v[204:207], v[104:107], a[76:79], v[204:207]\n"
      "s_waitcnt lgkmcnt(12)\n"
      "v_mfma_f32_16x16x32_bf16 v[208:211], v[84:87], a[80:83], v[208:211]\n"
      "v_mfma_f32_16x16x32_bf16 v[208:211], v[88:91], a[84:87], v[208:211]\n"
      "v_mfma_f32_16x16x32_bf16 v[212:215], v[92:95], a[80:83], v[212:215]\n"
      "s_cmp_eq_u32 s20, 14\n"
      "v_mfma_f32_16x16x32_bf16 v[212:215], v[96:99], a[84:87], v[212:215]\n"
      "s_cbranch_scc1 L_topD3_%=\n"
      "s_branch L_topN3_%=\n"
      "L_tail_%=:\n"
      "s_waitcnt vmcnt(0)\n"
      "v_mfma_f32_16x16x32_bf16 a[200:203], v[84:87], a[64:67], a[200:203]\n"
      "ds_read_b64_tr_b16 a[88:89], v15 offset:32768\n"
      "ds_read_b64_tr_b16 a[90:91], v15 offset:40960\n"
      "v_xor_b32 v3, 32, v20\n"
      "v_mfma_f32_16x16x32_bf16 a[200:203], v[88:91], a[68:71], a[200:203]\n"
      "s_waitcnt lgkmcnt(6)\n"
      "v_exp_f32 v79, v79\n"
      "v_and_b32 v4, 48, v0\n"
      "v_mfma_f32_16x16x32_bf16 a[212:215], v[84:87], a[72:75], a[212:215]\n"
      "ds_read_b64_tr_b16 a[92:93], v15 offset:49152\n"
      "ds_read_b64_tr_b16 a[94:95], v15 offset:57344\n"
      "v_cvt_pk_bf16_f32 v99, v78, v79\n"
      "v_mfma_f32_16x16x32_bf16 a[212:215], v[88:91], a[76:79], a[212:215]\n"
      "s_waitcnt lgkmcnt(4)\n"
      "v_exp_f32 v80, v80\n"
      "v_add_f32 v10, v10, v56\n"
      "v_mfma_f32_16x16x32_bf16 a[224:227], v[84:87], a[80:83], a[224:227]\n"
      "ds_read_b64_tr_b16 a[96:97], v12 offset:34816\n"
      "ds_read_b64_tr_b16 a[98:99], v12 offset:43008\n"
      "v_add_f32 v10, v10, v57\n"
      "v_mfma_f32_16x16x32_bf16 a[224:227], v[88:91], a[84:87], a[224:227]\n"
      "v_exp_f32 v81, v81\n"
      "v_add_f32 v10, v10, v58\n"
      "v_mfma_f32_16x16x32_bf16 a[204:207], v[92:95], a[64:67], a[204:207]\n"
      "ds_read_b64_tr_b16 a[100:101], v12 offset:51200\n"
      "ds_read_b64_tr_b16 a[102:103], v12 offset:59392\n"
      "v_cvt_pk_bf16_f32 v106, v80, v81\n"
      "v_mfma_f32_16x16x32_bf16 a[204:207], v[96:99], a[68:71], a[204:207]\n"
      "v_exp_f32 v82, v82\n"
      "v_add_f32 v10, v10, v59\n"
      "v_mfma_f32_16x16x32_bf16 a[216:219], v[92:95], a[72:75], a[216:219]\n"
      "ds_read_b64_tr_b16 a[0:1], v13 offset:34816\n"
      "ds_read_b64_tr_b16 a[2:3], v13 offset:43008\n"
      "v_add_f32 v6, v6, v60\n"
      "v_mfma_f32_16x16x32_bf16 a[216:219], v[96:99], a[76:79], a[216:219]\n"
      "v_exp_f32 v83, v83\n"
      "v_add_f32 v6, v6, v61\n"
      "v_mfma_f32_16x16x32_bf16 a[228:231], v[92:95], a[80:83], a[228:231]\n"
      "ds_read_b64_tr_b16 a[4:5], v13 offset:51200\n"
      "ds_read_b64_tr_b16 a[6:7], v13 offset:59392\n"
      "v_cvt_pk_bf16_f32 v107, v82, v83\n"
      "v_mfma_f32_16x16x32_bf16 a[228:231], v[96:99], a[84:87], a[228:231]\n"
      "ds_read_b64_tr_b16 a[8:9], v14 offset:34816\n"
      "ds_read_b64_tr_b16 a[10:11], v14 offset:43008\n"
      "v_add_f32 v6, v6, v62\n"
      "v_add_f32 v6, v6, v63\n"
      "v_add_f32 v8, v8, v64\n"
      "v_add_f32 v8, v8, v65\n"
      "v_add_f32 v8, v8, v66\n"
      "v_mfma_f32_16x16x32_bf16 a[208:211], v[100:103], a[64:67], a[208:211]\n"
      "ds_read_b64_tr_b16 a[12:13], v14 offset:51200\n"
      "ds_read_b64_tr_b16 a[14:15], v14 offset:59392\n"
      "v_add_f32 v8, v8, v67\n"
      "v_add_f32 v10, v10, v68\n"
      "v_add_f32 v10, v10, v69\n"
      "v_add_f32 v10, v10, v70\n"
      "v_add_f32 v10, v10, v71\n"
      "v_mfma_f32_16x16x32_bf16 a[208:211], v[104:107], a[68:71], a[208:211]\n"
      "ds_read_b64_tr_b16 a[16:17], v15 offset:34816\n"
      "ds_read_b64_tr_b16 a[18:19], v15 offset:43008\n"
      "v_add_f32 v6, v6, v72\n"
      "v_add_f32 v6, v6, v73\n"
      "v_add_f32 v6, v6, v74\n"
      "v_add_f32 v6, v6, v75\n"
      "v_add_f32 v8, v8, v76\n"
      "v_mfma_f32_16x16x32_bf16 a[220:223], v[100:103], a[72:75], a[220:223]\n"
      "ds_read_b64_tr_b16 a[20:21], v15 offset:51200\n"
      "ds_read_b64_tr_b16 a[22:23], v15 offset:59392\n"
      "v_add_f32 v8, v8, v77\n"
      "v_add_f32 v8, v8, v78\n"
      "v_add_f32 v8, v8, v79\n"
      "v_add_f32 v10, v10, v80\n"
      "v_add_f32 v10, v10, v81\n"
      "v_mfma_f32_16x16x32_bf16 a[220:223], v[104:107], a[76:79], a[220:223]\n"
      "ds_read_b64_tr_b16 a[24:25], v12 offset:36864\n"
      "ds_read_b64_tr_b16 a[26:27], v12 offset:45056\n"
      "v_add_f32 v10, v10, v82\n"
      "v_add_f32 v10, v10, v83\n"
      "v_mov_b32 v22, v6\n"
      "v_mov_b32 v23, v8\n"
      "v_mov_b32 v5, v10\n"
      "v_mfma_f32_16x16x32_bf16 a[232:235], v[100:103], a[80:83], a[232:235]\n"
      "ds_read_b64_tr_b16 a[28:29], v12 offset:53248\n"
      "ds_read_b64_tr_b16 a[30:31], v12 offset:61440\n"
      "s_nop 0\n"
      "v_permlane16_swap_b32 v6, v22\n"
      "v_permlane16_swap_b32 v8, v23\n"
      "v_permlane16_swap_b32 v10, v5\n"
      "v_add_f32 v6, v6, v22\n"
      "v_mfma_f32_16x16x32_bf16 a[232:235], v[104:107], a[84:87], a[232:235]\n"
      "s_waitcnt lgkmcnt(15)\n"
      "ds_read_b64_tr_b16 a[32:33], v13 offset:36864\n"
      "ds_read_b64_tr_b16 a[34:35], v13 offset:45056\n"
      "v_add_f32 v8, v8, v23\n"
      "v_add_f32 v10, v10, v5\n"
      "v_mov_b32 v22, v6\n"
      "v_mov_b32 v23, v8\n"
      "v_mov_b32 v5, v10\n"
      "v_mfma_f32_16x16x32_bf16 a[236:239], v[84:87], a[88:91], a[236:239]\n"
      "ds_read_b64_tr_b16 a[36:37], v13 offset:53248\n"
      "ds_read_b64_tr_b16 a[38:39], v13 offset:61440\n"
      "s_nop 0\n"
      "v_permlane32_swap_b32 v6, v22\n"
      "v_permlane32_swap_b32 v8, v23\n"
      "v_permlane32_swap_b32 v10, v5\n"
      "v_add_f32 v6, v6, v22\n"
      "v_mfma_f32_16x16x32_bf16 a[236:239], v[88:91], a[92:95], a[236:239]\n"
      "ds_read_b64_tr_b16 a[40:41], v14 offset:36864\n"
      "ds_read_b64_tr_b16 a[42:43], v14 offset:45056\n"
      "v_add_f32 v8, v8, v23\n"
      "v_add_f32 v10, v10, v5\n"
      "s_mov_b32 s29, 0x53800000\n"
      "v_cmp_nge_f32 vcc, s29, v6\n"
      "v_cmp_nge_f32 s[30:31], s29, v8\n"
      "v_mfma_f32_16x16x32_bf16 a[240:243], v[92:95], a[88:91], a[240:243]\n"
      "ds_read_b64_tr_b16 a[44:45], v14 offset:53248\n"
      "ds_read_b64_tr_b16 a[46:47], v14 offset:61440\n"
      "v_cmp_nge_f32 s[34:35], s29, v10\n"
      "v_rcp_f32 v6, v6\n"
      "v_rcp_f32 v8, v8\n"
      "v_rcp_f32 v10, v10\n"
      "v_mov_b32 v1, s23\n"
      "v_mfma_f32_16x16x32_bf16 a[240:243], v[96:99], a[92:95], a[240:243]\n"
      "ds_read_b64_tr_b16 a[48:49], v15 offset:36864\n"
      "ds_read_b64_tr_b16 a[50:51], v15 offset:45056\n"
      "s_or_b64 vcc, vcc, s[30:31]\n"
      "s_or_b64 vcc, vcc, s[34:35]\n"
      "s_cselect_b32 s29, 1, 0\n"
      "v_mov_b32 v2, s29\n"
      "v_mfma_f32_16x16x32_bf16 v[108:111], v[100:103], a[88:91], v[108:111]\n"
      "ds_bpermute_b32 v24, v4, v6 offset:0\n"
      "ds_bpermute_b32 v25, v4, v6 offset:4\n"
      "ds_bpermute_b32 v26, v4, v6 offset:8\n"
      "v_mfma_f32_16x16x32_bf16 v[108:111], v[104:107], a[92:95], v[108:111]\n"
      "s_waitcnt lgkmcnt(15)\n"
      "ds_bpermute_b32 v27, v4, v6 offset:12\n"
      "ds_bpermute_b32 v28, v4, v8 offset:0\n"
      "ds_bpermute_b32 v29, v4, v8 offset:4\n"
      "v_mfma_f32_16x16x32_bf16 v[112:115], v[84:87], a[96:99], v[112:115]\n"
      "ds_bpermute_b32 v30, v4, v8 offset:8\n"
      "ds_bpermute_b32 v31, v4, v8 offset:12\n"
      "ds_bpermute_b32 v32, v4, v10 offset:0\n"
      "v_mfma_f32_16x16x32_bf16 v[112:115], v[88:91], a[100:103], v[112:115]\n"
      "s_waitcnt lgkmcnt(15)\n"
      "ds_bpermute_b32 v33, v4, v10 offset:4\n"
      "ds_bpermute_b32 v34, v4, v10 offset:8\n"
      "ds_bpermute_b32 v35, v4, v10 offset:12\n"
      "v_mfma_f32_16x16x32_bf16 v[124:127], v[84:87], a[0:3], v[124:127]\n"
      "ds_write_b32 v1, v2 offset:6144\n"
      "s_waitcnt lgkmcnt(9)\n"
      "v_accvgpr_read_b32 v36, a200\n"
      "v_accvgpr_read_b32 v37, a201\n"
      "v_accvgpr_read_b32 v38, a202\n"
      "v_accvgpr_read_b32 v39, a203\n"
      "v_accvgpr_read_b32 v40, a212\n"
      "v_accvgpr_read_b32 v41, a213\n"
      "v_accvgpr_read_b32 v42, a214\n"
      "v_accvgpr_read_b32 v43, a215\n"
      "v_pk_mul_f32 v[36:37], v[36:37], v[24:25]\n"
      "v_pk_mul_f32 v[38:39], v[38:39], v[26:27]\n"
      "v_pk_mul_f32 v[40:41], v[40:41], v[24:25]\n"
      "v_pk_mul_f32 v[42:43], v[42:43], v[26:27]\n"
      "v_cvt_pk_bf16_f32 v36, v36, v40\n"
      "v_cvt_pk_bf16_f32 v37, v37, v41\n"
      "v_cvt_pk_bf16_f32 v38, v38, v42\n"
      "v_cvt_pk_bf16_f32 v39, v39, v43\n"
      "v_mfma_f32_16x16x32_bf16 v[124:127], v[88:91], a[4:7], v[124:127]\n"
      "s_waitcnt lgkmcnt(15)\n"
      "ds_read_b64_tr_b16 a[52:53], v15 offset:53248\n"
      "ds_read_b64_tr_b16 a[54:55], v15 offset:61440\n"
      "s_waitcnt lgkmcnt(7)\n"
      "v_accvgpr_read_b32 v52, a204\n"
      "v_accvgpr_read_b32 v53, a205\n"
      "v_accvgpr_read_b32 v54, a206\n"
      "v_accvgpr_read_b32 v55, a207\n"
      "v_accvgpr_read_b32 v56, a216\n"
      "v_accvgpr_read_b32 v57, a217\n"
      "v_accvgpr_read_b32 v58, a218\n"
      "v_accvgpr_read_b32 v59, a219\n"
      "v_pk_mul_f32 v[52:53], v[52:53], v[28:29]\n"
      "v_pk_mul_f32 v[54:55], v[54:55], v[30:31]\n"
      "v_pk_mul_f32 v[56:57], v[56:57], v[28:29]\n"
      "v_pk_mul_f32 v[58:59], v[58:59], v[30:31]\n"
      "v_cvt_pk_bf16_f32 v52, v52, v56\n"
      "v_cvt_pk_bf16_f32 v53, v53, v57\n"
      "v_cvt_pk_bf16_f32 v54, v54, v58\n"
      "v_cvt_pk_bf16_f32 v55, v55, v59\n"
      "v_mfma_f32_16x16x32_bf16 v[136:139], v[84:87], a[8:11], v[136:139]\n"
      "ds_read_b64_tr_b16 a[56:57], v12 offset:38912\n"
      "ds_read_b64_tr_b16 a[58:59], v12 offset:47104\n"
      "s_waitcnt lgkmcnt(5)\n"
      "v_accvgpr_read_b32 v68, a208\n"
      "v_accvgpr_read_b32 v69, a209\n"
      "v_accvgpr_read_b32 v70, a210\n"
      "v_accvgpr_read_b32 v71, a211\n"
      "v_accvgpr_read_b32 v72, a220\n"
      "v_accvgpr_read_b32 v73, a221\n"
      "v_accvgpr_read_b32 v74, a222\n"
      "v_accvgpr_read_b32 v75, a223\n"
      "v_pk_mul_f32 v[68:69], v[68:69], v[32:33]\n"
      "v_pk_mul_f32 v[70:71], v[70:71], v[34:35]\n"
      "v_pk_mul_f32 v[72:73], v[72:73], v[32:33]\n"
      "v_pk_mul_f32 v[74:75], v[74:75], v[34:35]\n"
      "v_cvt_pk_bf16_f32 v68, v68, v72\n"
      "v_cvt_pk_bf16_f32 v69, v69, v73\n"
      "v_cvt_pk_bf16_f32 v70, v70, v74\n"
      "v_cvt_pk_bf16_f32 v71, v71, v75\n"
      "v_mfma_f32_16x16x32_bf16 v[136:139], v[88:91], a[12:15], v[136:139]\n"
      "s_waitcnt lgkmcnt(15)\n"
      "ds_read_b64_tr_b16 a[60:61], v12 offset:55296\n"
      "ds_read_b64_tr_b16 a[62:63], v12 offset:63488\n"
      "v_accvgpr_read_b32 v44, a224\n"
      "v_accvgpr_read_b32 v45, a225\n"
      "v_accvgpr_read_b32 v46, a226\n"
      "v_accvgpr_read_b32 v47, a227\n"
      "v_accvgpr_read_b32 v48, a236\n"
      "v_accvgpr_read_b32 v49, a237\n"
      "v_accvgpr_read_b32 v50, a238\n"
      "v_accvgpr_read_b32 v51, a239\n"
      "v_pk_mul_f32 v[44:45], v[44:45], v[24:25]\n"
      "v_pk_mul_f32 v[46:47], v[46:47], v[26:27]\n"
      "v_pk_mul_f32 v[48:49], v[48:49], v[24:25]\n"
      "v_pk_mul_f32 v[50:51], v[50:51], v[26:27]\n"
      "v_cvt_pk_bf16_f32 v44, v44, v48\n"
      "v_cvt_pk_bf16_f32 v45, v45, v49\n"
      "v_cvt_pk_bf16_f32 v46, v46, v50\n"
      "v_cvt_pk_bf16_f32 v47, v47, v51\n"
      "v_mfma_f32_16x16x32_bf16 v[148:151], v[84:87], a[16:19], v[148:151]\n"
      "ds_write2st64_b32 v16, v36, v44 offset0:0 offset1:4\n"
      "ds_read_b64_tr_b16 a[64:65], v13 offset:38912\n"
      "v_accvgpr_read_b32 v60, a228\n"
      "v_accvgpr_read_b32 v61, a229\n"
      "v_accvgpr_read_b32 v62, a230\n"
      "v_accvgpr_read_b32 v63, a231\n"
      "v_accvgpr_read_b32 v64, a240\n"
      "v_accvgpr_read_b32 v65, a241\n"
      "v_accvgpr_read_b32 v66, a242\n"
      "v_accvgpr_read_b32 v67, a243\n"
      "v_pk_mul_f32 v[60:61], v[60:61], v[28:29]\n"
      "v_pk_mul_f32 v[62:63], v[62:63], v[30:31]\n"
      "v_pk_mul_f32 v[64:65], v[64:65], v[28:29]\n"
      "v_pk_mul_f32 v[66:67], v[66:67], v[30:31]\n"
      "v_cvt_pk_bf16_f32 v60, v60, v64\n"
      "v_cvt_pk_bf16_f32 v61, v61, v65\n"
      "v_cvt_pk_bf16_f32 v62, v62, v66\n"
      "v_cvt_pk_bf16_f32 v63, v63, v67\n"
      "v_mfma_f32_16x16x32_bf16 v[148:151], v[88:91], a[20:23], v[148:151]\n"
      "ds_write2st64_b32 v17, v37, v45 offset0:0 offset1:4\n"
      "ds_read_b64_tr_b16 a[66:67], v13 offset:47104\n"
      "v_mfma_f32_16x16x32_bf16 v[116:119], v[92:95], a[96:99], v[116:119]\n"
      "ds_write2st64_b32 v18, v38, v46 offset0:0 offset1:4\n"
      "ds_read_b64_tr_b16 a[68:69], v13 offset:55296\n"
      "v_mfma_f32_16x16x32_bf16 v[116:119], v[96:99], a[100:103], v[116:119]\n"
      "ds_write2st64_b32 v19, v39, v47 offset0:0 offset1:4\n"
      "ds_read_b64_tr_b16 a[70:71], v13 offset:63488\n"
      "v_accvgpr_read_b32 v76, a232\n"
      "v_accvgpr_read_b32 v77, a233\n"
      "v_accvgpr_read_b32 v78, a234\n"
      "v_accvgpr_read_b32 v79, a235\n"
      "v_pk_mul_f32 v[76:77], v[76:77], v[32:33]\n"
      "v_pk_mul_f32 v[78:79], v[78:79], v[34:35]\n"
      "v_pk_mul_f32 v[108:109], v[108:109], v[32:33]\n"
      "v_pk_mul_f32 v[110:111], v[110:111], v[34:35]\n"
      "v_cvt_pk_bf16_f32 v76, v76, v108\n"
      "v_cvt_pk_bf16_f32 v77, v77, v109\n"
      "v_cvt_pk_bf16_f32 v78, v78, v110\n"
      "v_cvt_pk_bf16_f32 v79, v79, v111\n"
      "v_mfma_f32_16x16x32_bf16 v[128:131], v[92:95], a[0:3], v[128:131]\n"
      "ds_read_b64_tr_b16 v[40:41], v20 offset:0\n"
      "ds_read_b64_tr_b16 v[42:43], v3 offset:0\n"
      "v_mfma_f32_16x16x32_bf16 v[128:131], v[96:99], a[4:7], v[128:131]\n"
      "ds_read_b64_tr_b16 v[48:49], v20 offset:512\n"
      "ds_read_b64_tr_b16 v[50:51], v3 offset:512\n"
      "v_mfma_f32_16x16x32_bf16 v[140:143], v[92:95], a[8:11], v[140:143]\n"
      "ds_write2st64_b32 v16, v52, v60 offset0:8 offset1:12\n"
      "ds_read_b64_tr_b16 a[72:73], v14 offset:38912\n"
      "v_mfma_f32_16x16x32_bf16 v[140:143], v[96:99], a[12:15], v[140:143]\n"
      "ds_write2st64_b32 v17, v53, v61 offset0:8 offset1:12\n"
      "ds_read_b64_tr_b16 a[74:75], v14 offset:47104\n"
      "v_mfma_f32_16x16x32_bf16 v[152:155], v[92:95], a[16:19], v[152:155]\n"
      "ds_write2st64_b32 v18, v54, v62 offset0:8 offset1:12\n"
      "s_waitcnt lgkmcnt(7)\n"
      "buffer_store_dwordx4 v[40:43], v21, s[48:51], 0 offen offset:0 nt\n"
      "v_mfma_f32_16x16x32_bf16 v[152:155], v[96:99], a[20:23], v[152:155]\n"
      "ds_write2st64_b32 v19, v55, v63 offset0:8 offset1:12\n"
      "ds_read_b64_tr_b16 a[76:77], v14 offset:55296\n"
      "v_pk_mul_f32 v[112:113], v[112:113], v[24:25]\n"
      "v_pk_mul_f32 v[114:115], v[114:115], v[26:27]\n"
      "v_pk_mul_f32 v[124:125], v[124:125], v[24:25]\n"
      "v_pk_mul_f32 v[126:127], v[126:127], v[26:27]\n"
      "v_cvt_pk_bf16_f32 v112, v112, v124\n"
      "v_cvt_pk_bf16_f32 v113, v113, v125\n"
      "v_cvt_pk_bf16_f32 v114, v114, v126\n"
      "v_cvt_pk_bf16_f32 v115, v115, v127\n"
      "v_mfma_f32_16x16x32_bf16 v[120:123], v[100:103], a[96:99], v[120:123]\n"
      "ds_read_b64_tr_b16 v[56:57], v20 offset:2048\n"
      "ds_read_b64_tr_b16 v[58:59], v3 offset:2048\n"
      "v_pk_mul_f32 v[136:137], v[136:137], v[24:25]\n"
      "v_pk_mul_f32 v[138:139], v[138:139], v[26:27]\n"
      "v_pk_mul_f32 v[148:149], v[148:149], v[24:25]\n"
      "v_pk_mul_f32 v[150:151], v[150:151], v[26:27]\n"
      "v_cvt_pk_bf16_f32 v136, v136, v148\n"
      "v_cvt_pk_bf16_f32 v137, v137, v149\n"
      "v_cvt_pk_bf16_f32 v138, v138, v150\n"
      "v_cvt_pk_bf16_f32 v139, v139, v151\n"
      "v_mfma_f32_16x16x32_bf16 v[120:123], v[104:107], a[100:103], v[120:123]\n"
      "ds_read_b64_tr_b16 v[64:65], v20 offset:2560\n"
      "s_waitcnt lgkmcnt(10)\n"
      "buffer_store_dwordx4 v[48:51], v21, s[48:51], s52 offen offset:0 nt\n"
      "v_mfma_f32_16x16x32_bf16 v[132:135], v[100:103], a[0:3], v[132:135]\n"
      "ds_read_b64_tr_b16 v[66:67], v3 offset:2560\n"
      "ds_write2st64_b32 v16, v68, v76 offset0:16 offset1:20\n"
      "v_mfma_f32_16x16x32_bf16 v[132:135], v[104:107], a[4:7], v[132:135]\n"
      "ds_write2st64_b32 v17, v69, v77 offset0:16 offset1:20\n"
      "ds_read_b64_tr_b16 a[78:79], v14 offset:63488\n"
      "v_mfma_f32_16x16x32_bf16 v[144:147], v[100:103], a[8:11], v[144:147]\n"
      "ds_write2st64_b32 v18, v70, v78 offset0:16 offset1:20\n"
      "s_waitcnt lgkmcnt(6)\n"
      "buffer_store_dwordx4 v[56:59], v21, s[48:51], s53 offen offset:0 nt\n"
      "v_mfma_f32_16x16x32_bf16 v[144:147], v[104:107], a[12:15], v[144:147]\n"
      "ds_write2st64_b32 v19, v71, v79 offset0:16 offset1:20\n"
      "ds_read_b64_tr_b16 a[80:81], v15 offset:38912\n"
      "v_pk_mul_f32 v[116:117], v[116:117], v[28:29]\n"
      "v_pk_mul_f32 v[118:119], v[118:119], v[30:31]\n"
      "v_pk_mul_f32 v[128:129], v[128:129], v[28:29]\n"
      "v_pk_mul_f32 v[130:131], v[130:131], v[30:31]\n"
      "v_cvt_pk_bf16_f32 v116, v116, v128\n"
      "v_cvt_pk_bf16_f32 v117, v117, v129\n"
      "v_cvt_pk_bf16_f32 v118, v118, v130\n"
      "v_cvt_pk_bf16_f32 v119, v119, v131\n"
      "v_mfma_f32_16x16x32_bf16 v[156:159], v[100:103], a[16:19], v[156:159]\n"
      "ds_read_b64_tr_b16 v[72:73], v20 offset:4096\n"
      "ds_read_b64_tr_b16 v[74:75], v3 offset:4096\n"
      "v_pk_mul_f32 v[140:141], v[140:141], v[28:29]\n"
      "v_pk_mul_f32 v[142:143], v[142:143], v[30:31]\n"
      "v_pk_mul_f32 v[152:153], v[152:153], v[28:29]\n"
      "v_pk_mul_f32 v[154:155], v[154:155], v[30:31]\n"
      "v_cvt_pk_bf16_f32 v140, v140, v152\n"
      "v_cvt_pk_bf16_f32 v141, v141, v153\n"
      "v_cvt_pk_bf16_f32 v142, v142, v154\n"
      "v_cvt_pk_bf16_f32 v143, v143, v155\n"
      "v_mfma_f32_16x16x32_bf16 v[156:159], v[104:107], a[20:23], v[156:159]\n"
      "s_waitcnt lgkmcnt(15)\n"
      "ds_read_b64_tr_b16 v[108:109], v20 offset:4608\n"
      "s_waitcnt lgkmcnt(9)\n"
      "buffer_store_dwordx4 v[64:67], v21, s[48:51], s54 offen offset:0 nt\n"
      "v_mfma_f32_16x16x32_bf16 v[160:163], v[84:87], a[24:27], v[160:163]\n"
      "ds_read_b64_tr_b16 v[110:111], v3 offset:4608\n"
      "ds_write2st64_b32 v16, v112, v136 offset0:0 offset1:4\n"
      "v_mfma_f32_16x16x32_bf16 v[160:163], v[88:91], a[28:31], v[160:163]\n"
      "s_waitcnt lgkmcnt(15)\n"
      "ds_write2st64_b32 v17, v113, v137 offset0:0 offset1:4\n"
      "ds_read_b64_tr_b16 a[82:83], v15 offset:47104\n"
      "v_mfma_f32_16x16x32_bf16 v[172:175], v[84:87], a[32:35], v[172:175]\n"
      "ds_write2st64_b32 v18, v114, v138 offset0:0 offset1:4\n"
      "s_waitcnt lgkmcnt(6)\n"
      "buffer_store_dwordx4 v[72:75], v21, s[48:51], s55 offen offset:0 nt\n"
      "v_mfma_f32_16x16x32_bf16 v[172:175], v[88:91], a[36:39], v[172:175]\n"
      "s_waitcnt lgkmcnt(15)\n"
      "ds_write2st64_b32 v19, v115, v139 offset0:0 offset1:4\n"
      "ds_read_b64_tr_b16 a[84:85], v15 offset:55296\n"
      "v_pk_mul_f32 v[120:121], v[120:121], v[32:33]\n"
      "v_pk_mul_f32 v[122:123], v[122:123], v[34:35]\n"
      "v_pk_mul_f32 v[132:133], v[132:133], v[32:33]\n"
      "v_pk_mul_f32 v[134:135], v[134:135], v[34:35]\n"
      "v_cvt_pk_bf16_f32 v120, v120, v132\n"
      "v_cvt_pk_bf16_f32 v121, v121, v133\n"
      "v_cvt_pk_bf16_f32 v122, v122, v134\n"
      "v_cvt_pk_bf16_f32 v123, v123, v135\n"
      "v_mfma_f32_16x16x32_bf16 v[184:187], v[84:87], a[40:43], v[184:187]\n"
      "ds_read_b64_tr_b16 v[124:125], v20 offset:0\n"
      "ds_read_b64_tr_b16 v[126:127], v3 offset:0\n"
      "v_pk_mul_f32 v[144:145], v[144:145], v[32:33]\n"
      "v_pk_mul_f32 v[146:147], v[146:147], v[34:35]\n"
      "v_pk_mul_f32 v[156:157], v[156:157], v[32:33]\n"
      "v_pk_mul_f32 v[158:159], v[158:159], v[34:35]\n"
      "v_cvt_pk_bf16_f32 v144, v144, v156\n"
      "v_cvt_pk_bf16_f32 v145, v145, v157\n"
      "v_cvt_pk_bf16_f32 v146, v146, v158\n"
      "v_cvt_pk_bf16_f32 v147, v147, v159\n"
      "v_mfma_f32_16x16x32_bf16 v[184:187], v[88:91], a[44:47], v[184:187]\n"
      "s_waitcnt lgkmcnt(15)\n"
      "ds_read_b64_tr_b16 v[148:149], v20 offset:512\n"
      "s_waitcnt lgkmcnt(9)\n"
      "buffer_store_dwordx4 v[108:111], v21, s[48:51], s56 offen offset:0 nt\n"
      "v_mfma_f32_16x16x32_bf16 v[196:199], v[84:87], a[48:51], v[196:199]\n"
      "ds_read_b64_tr_b16 v[150:151], v3 offset:512\n"
      "ds_write2st64_b32 v16, v116, v140 offset0:8 offset1:12\n"
      "v_mfma_f32_16x16x32_bf16 v[196:199], v[88:91], a[52:55], v[196:199]\n"
      "ds_write2st64_b32 v17, v117, v141 offset0:8 offset1:12\n"
      "ds_read_b64_tr_b16 a[86:87], v15 offset:63488\n"
      "v_mfma_f32_16x16x32_bf16 v[164:167], v[92:95], a[24:27], v[164:167]\n"
      "ds_write2st64_b32 v18, v118, v142 offset0:8 offset1:12\n"
      "s_waitcnt lgkmcnt(6)\n"
      "buffer_store_dwordx4 v[124:127], v21, s[48:51], 0 offen offset:128 nt\n"
      "v_mfma_f32_16x16x32_bf16 v[164:167], v[96:99], a[28:31], v[164:167]\n"
      "ds_write2st64_b32 v19, v119, v143 offset0:8 offset1:12\n"
      "v_pk_mul_f32 v[160:161], v[160:161], v[24:25]\n"
      "v_pk_mul_f32 v[162:163], v[162:163], v[26:27]\n"
      "v_pk_mul_f32 v[172:173], v[172:173], v[24:25]\n"
      "v_pk_mul_f32 v[174:175], v[174:175], v[26:27]\n"
      "v_cvt_pk_bf16_f32 v160, v160, v172\n"
      "v_cvt_pk_bf16_f32 v161, v161, v173\n"
      "v_cvt_pk_bf16_f32 v162, v162, v174\n"
      "v_cvt_pk_bf16_f32 v163, v163, v175\n"
      "v_mfma_f32_16x16x32_bf16 v[176:179], v[92:95], a[32:35], v[176:179]\n"
      "ds_read_b64_tr_b16 v[128:129], v20 offset:2048\n"
      "ds_read_b64_tr_b16 v[130:131], v3 offset:2048\n"
      "v_pk_mul_f32 v[184:185], v[184:185], v[24:25]\n"
      "v_pk_mul_f32 v[186:187], v[186:187], v[26:27]\n"
      "v_pk_mul_f32 v[196:197], v[196:197], v[24:25]\n"
      "v_pk_mul_f32 v[198:199], v[198:199], v[26:27]\n"
      "v_cvt_pk_bf16_f32 v184, v184, v196\n"
      "v_cvt_pk_bf16_f32 v185, v185, v197\n"
      "v_cvt_pk_bf16_f32 v186, v186, v198\n"
      "v_cvt_pk_bf16_f32 v187, v187, v199\n"
      "v_mfma_f32_16x16x32_bf16 v[176:179], v[96:99], a[36:39], v[176:179]\n"
      "ds_read_b64_tr_b16 v[152:153], v20 offset:2560\n"
      "s_waitcnt lgkmcnt(8)\n"
      "buffer_store_dwordx4 v[148:151], v21, s[48:51], s52 offen offset:128 nt\n"
      "v_mfma_f32_16x16x32_bf16 v[188:191], v[92:95], a[40:43], v[188:191]\n"
      "ds_read_b64_tr_b16 v[154:155], v3 offset:2560\n"
      "ds_write2st64_b32 v16, v120, v144 offset0:16 offset1:20\n"
      "v_mfma_f32_16x16x32_bf16 v[188:191], v[96:99], a[44:47], v[188:191]\n"
      "ds_write2st64_b32 v17, v121, v145 offset0:16 offset1:20\n"
      "v_mfma_f32_16x16x32_bf16 v[200:203], v[92:95], a[48:51], v[200:203]\n"
      "ds_write2st64_b32 v18, v122, v146 offset0:16 offset1:20\n"
      "s_waitcnt lgkmcnt(5)\n"
      "buffer_store_dwordx4 v[128:131], v21, s[48:51], s53 offen offset:128 nt\n"
      "v_mfma_f32_16x16x32_bf16 v[200:203], v[96:99], a[52:55], v[200:203]\n"
      "ds_write2st64_b32 v19, v123, v147 offset0:16 offset1:20\n"
      "v_pk_mul_f32 v[164:165], v[164:165], v[28:29]\n"
      "v_pk_mul_f32 v[166:167], v[166:167], v[30:31]\n"
      "v_pk_mul_f32 v[176:177], v[176:177], v[28:29]\n"
      "v_pk_mul_f32 v[178:179], v[178:179], v[30:31]\n"
      "v_cvt_pk_bf16_f32 v164, v164, v176\n"
      "v_cvt_pk_bf16_f32 v165, v165, v177\n"
      "v_cvt_pk_bf16_f32 v166, v166, v178\n"
      "v_cvt_pk_bf16_f32 v167, v167, v179\n"
      "v_mfma_f32_16x16x32_bf16 v[168:171], v[100:103], a[24:27], v[168:171]\n"
      "ds_read_b64_tr_b16 v[132:133], v20 offset:4096\n"
      "ds_read_b64_tr_b16 v[134:135], v3 offset:4096\n"
      "v_mfma_f32_16x16x32_bf16 v[168:171], v[104:107], a[28:31], v[168:171]\n"
      "ds_read_b64_tr_b16 v[156:157], v20 offset:4608\n"
      "s_waitcnt lgkmcnt(7)\n"
      "buffer_store_dwordx4 v[152:155], v21, s[48:51], s54 offen offset:128 nt\n"
      "v_mfma_f32_16x16x32_bf16 v[180:183], v[100:103], a[32:35], v[180:183]\n"
      "ds_read_b64_tr_b16 v[158:159], v3 offset:4608\n"
      "ds_write2st64_b32 v16, v160, v184 offset0:0 offset1:4\n"
      "v_pk_mul_f32 v[188:189], v[188:189], v[28:29]\n"
      "v_pk_mul_f32 v[190:191], v[190:191], v[30:31]\n"
      "v_pk_mul_f32 v[200:201], v[200:201], v[28:29]\n"
      "v_pk_mul_f32 v[202:203], v[202:203], v[30:31]\n"
      "v_cvt_pk_bf16_f32 v188, v188, v200\n"
      "v_cvt_pk_bf16_f32 v189, v189, v201\n"
      "v_cvt_pk_bf16_f32 v190, v190, v202\n"
      "v_cvt_pk_bf16_f32 v191, v191, v203\n"
      "v_mfma_f32_16x16x32_bf16 v[180:183], v[104:107], a[36:39], v[180:183]\n"
      "ds_write2st64_b32 v17, v161, v185 offset0:0 offset1:4\n"
      "v_mfma_f32_16x16x32_bf16 v[192:195], v[100:103], a[40:43], v[192:195]\n"
      "ds_write2st64_b32 v18, v162, v186 offset0:0 offset1:4\n"
      "s_waitcnt lgkmcnt(5)\n"
      "buffer_store_dwordx4 v[132:135], v21, s[48:51], s55 offen offset:128 nt\n"
      "v_mfma_f32_16x16x32_bf16 v[192:195], v[104:107], a[44:47], v[192:195]\n"
      "ds_write2st64_b32 v19, v163, v187 offset0:0 offset1:4\n"
      "v_mfma_f32_16x16x32_bf16 v[204:207], v[100:103], a[48:51], v[204:207]\n"
      "ds_read_b64_tr_b16 v[172:173], v20 offset:0\n"
      "ds_read_b64_tr_b16 v[174:175], v3 offset:0\n"
      "v_pk_mul_f32 v[168:169], v[168:169], v[32:33]\n"
      "v_pk_mul_f32 v[170:171], v[170:171], v[34:35]\n"
      "v_pk_mul_f32 v[180:181], v[180:181], v[32:33]\n"
      "v_pk_mul_f32 v[182:183], v[182:183], v[34:35]\n"
      "v_cvt_pk_bf16_f32 v168, v168, v180\n"
      "v_cvt_pk_bf16_f32 v169, v169, v181\n"
      "v_cvt_pk_bf16_f32 v170, v170, v182\n"
      "v_cvt_pk_bf16_f32 v171, v171, v183\n"
      "v_mfma_f32_16x16x32_bf16 v[204:207], v[104:107], a[52:55], v[204:207]\n"
      "s_waitcnt lgkmcnt(15)\n"
      "ds_read_b64_tr_b16 v[196:197], v20 offset:512\n"
      "s_waitcnt lgkmcnt(7)\n"
      "buffer_store_dwordx4 v[156:159], v21, s[48:51], s56 offen offset:128 nt\n"
      "v_mfma_f32_16x16x32_bf16 v[208:211], v[84:87], a[56:59], v[208:211]\n"
      "ds_read_b64_tr_b16 v[198:199], v3 offset:512\n"
      "ds_write2st64_b32 v16, v164, v188 offset0:8 offset1:12\n"
      "v_mfma_f32_16x16x32_bf16 v[208:211], v[88:91], a[60:63], v[208:211]\n"
      "s_waitcnt lgkmcnt(15)\n"
      "ds_write2st64_b32 v17, v165, v189 offset0:8 offset1:12\n"
      "v_mfma_f32_16x16x32_bf16 v[220:223], v[84:87], a[64:67], v[220:223]\n"
      "ds_write2st64_b32 v18, v166, v190 offset0:8 offset1:12\n"
      "s_waitcnt lgkmcnt(5)\n"
      "buffer_store_dwordx4 v[172:175], v21, s[48:51], 0 offen offset:256 nt\n"
      "v_pk_mul_f32 v[192:193], v[192:193], v[32:33]\n"
      "v_pk_mul_f32 v[194:195], v[194:195], v[34:35]\n"
      "v_pk_mul_f32 v[204:205], v[204:205], v[32:33]\n"
      "v_pk_mul_f32 v[206:207], v[206:207], v[34:35]\n"
      "v_cvt_pk_bf16_f32 v192, v192, v204\n"
      "v_cvt_pk_bf16_f32 v193, v193, v205\n"
      "v_cvt_pk_bf16_f32 v194, v194, v206\n"
      "v_cvt_pk_bf16_f32 v195, v195, v207\n"
      "v_mfma_f32_16x16x32_bf16 v[220:223], v[88:91], a[68:71], v[220:223]\n"
      "s_waitcnt lgkmcnt(15)\n"
      "ds_write2st64_b32 v19, v167, v191 offset0:8 offset1:12\n"
      "v_mfma_f32_16x16x32_bf16 v[232:235], v[84:87], a[72:75], v[232:235]\n"
      "ds_read_b64_tr_b16 v[176:177], v20 offset:2048\n"
      "ds_read_b64_tr_b16 v[178:179], v3 offset:2048\n"
      "v_mfma_f32_16x16x32_bf16 v[232:235], v[88:91], a[76:79], v[232:235]\n"
      "s_waitcnt lgkmcnt(15)\n"
      "ds_read_b64_tr_b16 v[200:201], v20 offset:2560\n"
      "s_waitcnt lgkmcnt(7)\n"
      "buffer_store_dwordx4 v[196:199], v21, s[48:51], s52 offen offset:256 nt\n"
      "v_mfma_f32_16x16x32_bf16 v[244:247], v[84:87], a[80:83], v[244:247]\n"
      "ds_read_b64_tr_b16 v[202:203], v3 offset:2560\n"
      "ds_write2st64_b32 v16, v168, v192 offset0:16 offset1:20\n"
      "v_pk_mul_f32 v[208:209], v[208:209], v[24:25]\n"
      "v_pk_mul_f32 v[210:211], v[210:211], v[26:27]\n"
      "v_pk_mul_f32 v[220:221], v[220:221], v[24:25]\n"
      "v_pk_mul_f32 v[222:223], v[222:223], v[26:27]\n"
      "v_cvt_pk_bf16_f32 v208, v208, v220\n"
      "v_cvt_pk_bf16_f32 v209, v209, v221\n"
      "v_cvt_pk_bf16_f32 v210, v210, v222\n"
      "v_cvt_pk_bf16_f32 v211, v211, v223\n"
      "v_mfma_f32_16x16x32_bf16 v[244:247], v[88:91], a[84:87], v[244:247]\n"
      "ds_write2st64_b32 v17, v169, v193 offset0:16 offset1:20\n"
      "v_mfma_f32_16x16x32_bf16 v[212:215], v[92:95], a[56:59], v[212:215]\n"
      "ds_write2st64_b32 v18, v170, v194 offset0:16 offset1:20\n"
      "s_waitcnt lgkmcnt(5)\n"
      "buffer_store_dwordx4 v[176:179], v21, s[48:51], s53 offen offset:256 nt\n"
      "v_mfma_f32_16x16x32_bf16 v[212:215], v[96:99], a[60:63], v[212:215]\n"
      "ds_write2st64_b32 v19, v171, v195 offset0:16 offset1:20\n"
      "v_mfma_f32_16x16x32_bf16 v[224:227], v[92:95], a[64:67], v[224:227]\n"
      "ds_read_b64_tr_b16 v[180:181], v20 offset:4096\n"
      "ds_read_b64_tr_b16 v[182:183], v3 offset:4096\n"
      "v_pk_mul_f32 v[232:233], v[232:233], v[24:25]\n"
      "v_pk_mul_f32 v[234:235], v[234:235], v[26:27]\n"
      "v_pk_mul_f32 v[244:245], v[244:245], v[24:25]\n"
      "v_pk_mul_f32 v[246:247], v[246:247], v[26:27]\n"
      "v_cvt_pk_bf16_f32 v232, v232, v244\n"
      "v_cvt_pk_bf16_f32 v233, v233, v245\n"
      "v_cvt_pk_bf16_f32 v234, v234, v246\n"
      "v_cvt_pk_bf16_f32 v235, v235, v247\n"
      "v_mfma_f32_16x16x32_bf16 v[224:227], v[96:99], a[68:71], v[224:227]\n"
      "ds_read_b64_tr_b16 v[204:205], v20 offset:4608\n"
      "s_waitcnt lgkmcnt(7)\n"
      "buffer_store_dwordx4 v[200:203], v21, s[48:51], s54 offen offset:256 nt\n"
      "v_mfma_f32_16x16x32_bf16 v[236:239], v[92:95], a[72:75], v[236:239]\n"
      "ds_read_b64_tr_b16 v[206:207], v3 offset:4608\n"
      "ds_write2st64_b32 v16, v208, v232 offset0:0 offset1:4\n"
      "v_mfma_f32_16x16x32_bf16 v[236:239], v[96:99], a[76:79], v[236:239]\n"
      "ds_write2st64_b32 v17, v209, v233 offset0:0 offset1:4\n"
      "v_mfma_f32_16x16x32_bf16 v[248:251], v[92:95], a[80:83], v[248:251]\n"
      "ds_write2st64_b32 v18, v210, v234 offset0:0 offset1:4\n"
      "s_waitcnt lgkmcnt(5)\n"
      "buffer_store_dwordx4 v[180:183], v21, s[48:51], s55 offen offset:256 nt\n"
      "v_pk_mul_f32 v[212:213], v[212:213], v[28:29]\n"
      "v_pk_mul_f32 v[214:215], v[214:215], v[30:31]\n"
      "v_pk_mul_f32 v[224:225], v[224:225], v[28:29]\n"
      "v_pk_mul_f32 v[226:227], v[226:227], v[30:31]\n"
      "v_cvt_pk_bf16_f32 v212, v212, v224\n"
      "v_cvt_pk_bf16_f32 v213, v213, v225\n"
      "v_cvt_pk_bf16_f32 v214, v214, v226\n"
      "v_cvt_pk_bf16_f32 v215, v215, v227\n"
      "v_mfma_f32_16x16x32_bf16 v[248:251], v[96:99], a[84:87], v[248:251]\n"
      "ds_write2st64_b32 v19, v211, v235 offset0:0 offset1:4\n"
      "v_mfma_f32_16x16x32_bf16 v[216:219], v[100:103], a[56:59], v[216:219]\n"
      "ds_read_b64_tr_b16 v[220:221], v20 offset:0\n"
      "ds_read_b64_tr_b16 v[222:223], v3 offset:0\n"
      "v_mfma_f32_16x16x32_bf16 v[216:219], v[104:107], a[60:63], v[216:219]\n"
      "ds_read_b64_tr_b16 v[244:245], v20 offset:512\n"
      "s_waitcnt lgkmcnt(7)\n"
      "buffer_store_dwordx4 v[204:207], v21, s[48:51], s56 offen offset:256 nt\n"
      "v_mfma_f32_16x16x32_bf16 v[228:231], v[100:103], a[64:67], v[228:231]\n"
      "ds_read_b64_tr_b16 v[246:247], v3 offset:512\n"
      "v_pk_mul_f32 v[236:237], v[236:237], v[28:29]\n"
      "v_pk_mul_f32 v[238:239], v[238:239], v[30:31]\n"
      "v_pk_mul_f32 v[248:249], v[248:249], v[28:29]\n"
      "v_pk_mul_f32 v[250:251], v[250:251], v[30:31]\n"
      "v_cvt_pk_bf16_f32 v236, v236, v248\n"
      "v_cvt_pk_bf16_f32 v237, v237, v249\n"
      "v_cvt_pk_bf16_f32 v238, v238, v250\n"
      "v_cvt_pk_bf16_f32 v239, v239, v251\n"
      "v_mfma_f32_16x16x32_bf16 v[228:231], v[104:107], a[68:71], v[228:231]\n"
      "ds_write2st64_b32 v16, v212, v236 offset0:8 offset1:12\n"
      "v_mfma_f32_16x16x32_bf16 v[240:243], v[100:103], a[72:75], v[240:243]\n"
      "ds_write2st64_b32 v17, v213, v237 offset0:8 offset1:12\n"
      "s_waitcnt lgkmcnt(4)\n"
      "buffer_store_dwordx4 v[220:223], v21, s[48:51], 0 offen offset:384 nt\n"
      "v_mfma_f32_16x16x32_bf16 v[240:243], v[104:107], a[76:79], v[240:243]\n"
      "ds_write2st64_b32 v18, v214, v238 offset0:8 offset1:12\n"
      "v_mfma_f32_16x16x32_bf16 v[252:255], v[100:103], a[80:83], v[252:255]\n"
      "ds_write2st64_b32 v19, v215, v239 offset0:8 offset1:12\n"
      "v_pk_mul_f32 v[216:217], v[216:217], v[32:33]\n"
      "v_pk_mul_f32 v[218:219], v[218:219], v[34:35]\n"
      "v_pk_mul_f32 v[228:229], v[228:229], v[32:33]\n"
      "v_pk_mul_f32 v[230:231], v[230:231], v[34:35]\n"
      "v_cvt_pk_bf16_f32 v216, v216, v228\n"
      "v_cvt_pk_bf16_f32 v217, v217, v229\n"
      "v_cvt_pk_bf16_f32 v218, v218, v230\n"
      "v_cvt_pk_bf16_f32 v219, v219, v231\n"
      "v_mfma_f32_16x16x32_bf16 v[252:255], v[104:107], a[84:87], v[252:255]\n"
      "ds_read_b64_tr_b16 v[224:225], v20 offset:2048\n"
      "s_waitcnt lgkmcnt(5)\n"
      "buffer_store_dwordx4 v[244:247], v21, s[48:51], s52 offen offset:384 nt\n"
      "ds_read_b64_tr_b16 v[226:227], v3 offset:2048\n"
      "ds_read_b64_tr_b16 v[248:249], v20 offset:2560\n"
      "ds_read_b64_tr_b16 v[250:251], v3 offset:2560\n"
      "s_nop 3\n"
      "v_pk_mul_f32 v[240:241], v[240:241], v[32:33]\n"
      "v_pk_mul_f32 v[242:243], v[242:243], v[34:35]\n"
      "v_pk_mul_f32 v[252:253], v[252:253], v[32:33]\n"
      "v_pk_mul_f32 v[254:255], v[254:255], v[34:35]\n"
      "v_cvt_pk_bf16_f32 v240, v240, v252\n"
      "v_cvt_pk_bf16_f32 v241, v241, v253\n"
      "v_cvt_pk_bf16_f32 v242, v242, v254\n"
      "v_cvt_pk_bf16_f32 v243, v243, v255\n"
      "ds_write2st64_b32 v16, v216, v240 offset0:16 offset1:20\n"
      "ds_write2st64_b32 v17, v217, v241 offset0:16 offset1:20\n"
      "s_waitcnt lgkmcnt(4)\n"
      "buffer_store_dwordx4 v[224:227], v21, s[48:51], s53 offen offset:384 nt\n"
      "ds_write2st64_b32 v18, v218, v242 offset0:16 offset1:20\n"
      "s_waitcnt lgkmcnt(3)\n"
      "buffer_store_dwordx4 v[248:251], v21, s[48:51], s54 offen offset:384 nt\n"
      "ds_write2st64_b32 v19, v219, v243 offset0:16 offset1:20\n"
      "ds_read_b64_tr_b16 v[228:229], v20 offset:4096\n"
      "ds_read_b64_tr_b16 v[230:231], v3 offset:4096\n"
      "ds_read_b64_tr_b16 v[252:253], v20 offset:4608\n"
      "ds_read_b64_tr_b16 v[254:255], v3 offset:4608\n"
      "s_nop 3\n"
      "s_nop 3\n"
      "s_waitcnt lgkmcnt(2)\n"
      "buffer_store_dwordx4 v[228:231], v21, s[48:51], s55 offen offset:384 nt\n"
      "s_waitcnt lgkmcnt(0)\n"
      "buffer_store_dwordx4 v[252:255], v21, s[48:51], s56 offen offset:384 nt\n"
      "v_mov_b32 v17, s21\n"
      "v_and_b32 v16, 3, v0\n"
      "v_lshlrev_b32 v16, 13, v16\n"
      "v_add_u32 v16, 6144, v16\n"
      "s_waitcnt lgkmcnt(0)\n"
      "s_barrier\n"
      "ds_read_b32 v16, v16\n"
      "s_waitcnt lgkmcnt(0)\n"
      "v_cmp_gt_u32 vcc, v16, v17\n"
      "s_nop 4\n"
      "s_cbranch_vccz L_done_%=\n"
      "s_waitcnt vmcnt(0)\n"
      "s_barrier\n"
      "s_mov_b32 s21, 1\n"
      "s_mov_b32 s20, 32\n"
      "v_and_b32 v16, 63, v0\n"
      "v_lshrrev_b32 v17, 3, v16\n"
      "v_and_b32 v5, 6, v17\n"
      "v_and_b32 v16, 7, v16\n"
      "v_xor_b32 v5, v5, v16\n"
      "v_lshlrev_b32 v5, 4, v5\n"
      "v_lshl_add_u32 v5, v17, 9, v5\n"
      "v_add_u32 v1, s23, v5\n"
      "v_mov_b32 v2, v1\n"
      "v_and_b32 v16, 63, v0\n"
      "v_lshrrev_b32 v17, 4, v16\n"
      "v_and_b32 v18, 6, v16\n"
      "v_xor_b32 v18, v18, v17\n"
      "v_lshlrev_b32 v18, 4, v18\n"
      "v_and_b32 v19, 15, v16\n"
      "v_lshl_add_u32 v3, v19, 7, v18\n"
      "v_xor_b32 v4, 64, v3\n"
      "v_lshrrev_b32 v18, 2, v16\n"
      "v_and_b32 v19, 6, v18\n"
      "v_bfe_u32 v17, v16, 1, 1\n"
      "v_xor_b32 v19, v19, v17\n"
      "v_lshlrev_b32 v19, 4, v19\n"
      "v_lshl_add_u32 v18, v18, 7, v19\n"
      "v_and_b32 v19, 1, v16\n"
      "v_lshl_add_u32 v18, v19, 3, v18\n"
      "v_add_u32 v12, 0x10000, v18\n"
      "v_xor_b32 v13, 32, v12\n"
      "v_xor_b32 v14, 64, v12\n"
      "v_xor_b32 v15, 96, v12\n"
      "L_max_%=:\n"
      "s_add_u32 m0, s23, 0\n"
      "s_nop 0\n"
      "buffer_load_dwordx4 v1, s[36:39], 0 offen offset:0 lds\n"
      "s_add_u32 m0, s23, 1024\n"
      "s_nop 0\n"
      "buffer_load_dwordx4 v1, s[36:39], s52 offen offset:0 lds\n"
      "s_add_u32 m0, s23, 1920\n"
      "s_nop 0\n"
      "buffer_load_dwordx4 v1, s[36:39], 0 offen offset:128 lds\n"
      "s_add_u32 m0, s23, 2944\n"
      "s_nop 0\n"
      "buffer_load_dwordx4 v1, s[36:39], s52 offen offset:128 lds\n"
      "s_add_u32 m0, s23, 3840\n"
      "s_nop 0\n"
      "buffer_load_dwordx4 v1, s[36:39], 0 offen offset:256 lds\n"
      "s_add_u32 m0, s23, 4864\n"
      "s_nop 0\n"
      "buffer_load_dwordx4 v1, s[36:39], s52 offen offset:256 lds\n"
      "s_add_u32 m0, s23, 5760\n"
      "s_nop 0\n"
      "buffer_load_dwordx4 v1, s[36:39], 0 offen offset:384 lds\n"
      "s_add_u32 m0, s23, 6784\n"
      "s_nop 0\n"
      "buffer_load_dwordx4 v1, s[36:39], s52 offen offset:384 lds\n"
      "v_add_u32 v1, 0x8000, v1\n"
      "s_waitcnt vmcnt(0)\n"
      "s_barrier\n"
      "ds_read_b128 a[0:3], v3 offset:0\n"
      "ds_read_b128 a[4:7], v4 offset:0\n"
      "ds_read_b128 a[8:11], v3 offset:2048\n"
      "ds_read_b128 a[12:15], v4 offset:2048\n"
      "ds_read_b128 a[16:19], v3 offset:4096\n"
      "ds_read_b128 a[20:23], v4 offset:4096\n"
      "ds_read_b128 a[24:27], v3 offset:6144\n"
      "ds_read_b128 a[28:31], v4 offset:6144\n"
      "s_waitcnt lgkmcnt(0)\n"
      "v_mfma_f32_16x16x32_bf16 v[36:39], a[0:3], a[104:107], 0\n"
      "v_mfma_f32_16x16x32_bf16 v[36:39], a[4:7], a[108:111], v[36:39]\n"
      "v_mfma_f32_16x16x32_bf16 v[36:39], a[8:11], a[112:115], v[36:39]\n"
      "v_mfma_f32_16x16x32_bf16 v[36:39], a[12:15], a[116:119], v[36:39]\n"
      "v_mfma_f32_16x16x32_bf16 v[36:39], a[16:19], a[120:123], v[36:39]\n"
      "v_mfma_f32_16x16x32_bf16 v[36:39], a[20:23], a[124:127], v[36:39]\n"
      "v_mfma_f32_16x16x32_bf16 v[36:39], a[24:27], a[128:131], v[36:39]\n"
      "v_mfma_f32_16x16x32_bf16 v[36:39], a[28:31], a[132:135], v[36:39]\n"
      "v_mfma_f32_16x16x32_bf16 v[40:43], a[0:3], a[136:139], 0\n"
      "v_mfma_f32_16x16x32_bf16 v[40:43], a[4:7], a[140:143], v[40:43]\n"
      "v_mfma_f32_16x16x32_bf16 v[40:43], a[8:11], a[144:147], v[40:43]\n"
      "v_mfma_f32_16x16x32_bf16 v[40:43], a[12:15], a[148:151], v[40:43]\n"
      "v_mfma_f32_16x16x32_bf16 v[40:43], a[16:19], a[152:155], v[40:43]\n"
      "v_mfma_f32_16x16x32_bf16 v[40:43], a[20:23], a[156:159], v[40:43]\n"
      "v_mfma_f32_16x16x32_bf16 v[40:43], a[24:27], a[160:163], v[40:43]\n"
      "v_mfma_f32_16x16x32_bf16 v[40:43], a[28:31], a[164:167], v[40:43]\n"
      "v_mfma_f32_16x16x32_bf16 v[44:47], a[0:3], a[168:171], 0\n"
      "v_mfma_f32_16x16x32_bf16 v[44:47], a[4:7], a[172:175], v[44:47]\n"
      "v_mfma_f32_16x16x32_bf16 v[44:47], a[8:11], a[176:179], v[44:47]\n"
      "v_mfma_f32_16x16x32_bf16 v[44:47], a[12:15], a[180:183], v[44:47]\n"
      "v_mfma_f32_16x16x32_bf16 v[44:47], a[16:19], a[184:187], v[44:47]\n"
      "v_mfma_f32_16x16x32_bf16 v[44:47], a[20:23], a[188:191], v[44:47]\n"
      "v_mfma_f32_16x16x32_bf16 v[44:47], a[24:27], a[192:195], v[44:47]\n"
      "v_mfma_f32_16x16x32_bf16 v[44:47], a[28:31], a[196:199], v[44:47]\n"
      "ds_read_b128 a[32:35], v3 offset:8192\n"
      "ds_read_b128 a[36:39], v4 offset:8192\n"
      "ds_read_b128 a[40:43], v3 offset:10240\n"
      "ds_read_b128 a[44:47], v4 offset:10240\n"
      "ds_read_b128 a[48:51], v3 offset:12288\n"
      "ds_read_b128 a[52:55], v4 offset:12288\n"
      "ds_read_b128 a[56:59], v3 offset:14336\n"
      "ds_read_b128 a[60:63], v4 offset:14336\n"
      "s_waitcnt lgkmcnt(0)\n"
      "v_mfma_f32_16x16x32_bf16 v[48:51], a[32:35], a[104:107], 0\n"
      "v_mfma_f32_16x16x32_bf16 v[48:51], a[36:39], a[108:111], v[48:51]\n"
      "v_mfma_f32_16x16x32_bf16 v[48:51], a[40:43], a[112:115], v[48:51]\n"
      "v_mfma_f32_16x16x32_bf16 v[48:51], a[44:47], a[116:119], v[48:51]\n"
      "v_mfma_f32_16x16x32_bf16 v[48:51], a[48:51], a[120:123], v[48:51]\n"
      "v_mfma_f32_16x16x32_bf16 v[48:51], a[52:55], a[124:127], v[48:51]\n"
      "v_mfma_f32_16x16x32_bf16 v[48:51], a[56:59], a[128:131], v[48:51]\n"
      "v_mfma_f32_16x16x32_bf16 v[48:51], a[60:63], a[132:135], v[48:51]\n"
      "v_mfma_f32_16x16x32_bf16 v[52:55], a[32:35], a[136:139], 0\n"
      "v_mfma_f32_16x16x32_bf16 v[52:55], a[36:39], a[140:143], v[52:55]\n"
      "v_mfma_f32_16x16x32_bf16 v[52:55], a[40:43], a[144:147], v[52:55]\n"
      "v_mfma_f32_16x16x32_bf16 v[52:55], a[44:47], a[148:151], v[52:55]\n"
      "v_mfma_f32_16x16x32_bf16 v[52:55], a[48:51], a[152:155], v[52:55]\n"
      "v_mfma_f32_16x16x32_bf16 v[52:55], a[52:55], a[156:159], v[52:55]\n"
      "v_mfma_f32_16x16x32_bf16 v[52:55], a[56:59], a[160:163], v[52:55]\n"
      "v_mfma_f32_16x16x32_bf16 v[52:55], a[60:63], a[164:167], v[52:55]\n"
      "v_mfma_f32_16x16x32_bf16 v[56:59], a[32:35], a[168:171], 0\n"
      "v_mfma_f32_16x16x32_bf16 v[56:59], a[36:39], a[172:175], v[56:59]\n"
      "v_mfma_f32_16x16x32_bf16 v[56:59], a[40:43], a[176:179], v[56:59]\n"
      "v_mfma_f32_16x16x32_bf16 v[56:59], a[44:47], a[180:183], v[56:59]\n"
      "v_mfma_f32_16x16x32_bf16 v[56:59], a[48:51], a[184:187], v[56:59]\n"
      "v_mfma_f32_16x16x32_bf16 v[56:59], a[52:55], a[188:191], v[56:59]\n"
      "v_mfma_f32_16x16x32_bf16 v[56:59], a[56:59], a[192:195], v[56:59]\n"
      "v_mfma_f32_16x16x32_bf16 v[56:59], a[60:63], a[196:199], v[56:59]\n"
      "ds_read_b128 a[0:3], v3 offset:16384\n"
      "ds_read_b128 a[4:7], v4 offset:16384\n"
      "ds_read_b128 a[8:11], v3 offset:18432\n"
      "ds_read_b128 a[12:15], v4 offset:18432\n"
      "ds_read_b128 a[16:19], v3 offset:20480\n"
      "ds_read_b128 a[20:23], v4 offset:20480\n"
      "ds_read_b128 a[24:27], v3 offset:22528\n"
      "ds_read_b128 a[28:31], v4 offset:22528\n"
      "s_waitcnt lgkmcnt(0)\n"
      "v_mfma_f32_16x16x32_bf16 v[60:63], a[0:3], a[104:107], 0\n"
      "v_mfma_f32_16x16x32_bf16 v[60:63], a[4:7], a[108:111], v[60:63]\n"
      "v_mfma_f32_16x16x32_bf16 v[60:63], a[8:11], a[112:115], v[60:63]\n"
      "v_mfma_f32_16x16x32_bf16 v[60:63], a[12:15], a[116:119], v[60:63]\n"
      "v_mfma_f32_16x16x32_bf16 v[60:63], a[16:19], a[120:123], v[60:63]\n"
      "v_mfma_f32_16x16x32_bf16 v[60:63], a[20:23], a[124:127], v[60:63]\n"
      "v_mfma_f32_16x16x32_bf16 v[60:63], a[24:27], a[128:131], v[60:63]\n"
      "v_mfma_f32_16x16x32_bf16 v[60:63], a[28:31], a[132:135], v[60:63]\n"
      "v_mfma_f32_16x16x32_bf16 v[64:67], a[0:3], a[136:139], 0\n"
      "v_mfma_f32_16x16x32_bf16 v[64:67], a[4:7], a[140:143], v[64:67]\n"
      "v_mfma_f32_16x16x32_bf16 v[64:67], a[8:11], a[144:147], v[64:67]\n"
      "v_mfma_f32_16x16x32_bf16 v[64:67], a[12:15], a[148:151], v[64:67]\n"
      "v_mfma_f32_16x16x32_bf16 v[64:67], a[16:19], a[152:155], v[64:67]\n"
      "v_mfma_f32_16x16x32_bf16 v[64:67], a[20:23], a[156:159], v[64:67]\n"
      "v_mfma_f32_16x16x32_bf16 v[64:67], a[24:27], a[160:163], v[64:67]\n"
      "v_mfma_f32_16x16x32_bf16 v[64:67], a[28:31], a[164:167], v[64:67]\n"
      "v_mfma_f32_16x16x32_bf16 v[68:71], a[0:3], a[168:171], 0\n"
      "v_mfma_f32_16x16x32_bf16 v[68:71], a[4:7], a[172:175], v[68:71]\n"
      "v_mfma_f32_16x16x32_bf16 v[68:71], a[8:11], a[176:179], v[68:71]\n"
      "v_mfma_f32_16x16x32_bf16 v[68:71], a[12:15], a[180:183], v[68:71]\n"
      "v_mfma_f32_16x16x32_bf16 v[68:71], a[16:19], a[184:187], v[68:71]\n"
      "v_mfma_f32_16x16x32_bf16 v[68:71], a[20:23], a[188:191], v[68:71]\n"
      "v_mfma_f32_16x16x32_bf16 v[68:71], a[24:27], a[192:195], v[68:71]\n"
      "v_mfma_f32_16x16x32_bf16 v[68:71], a[28:31], a[196:199], v[68:71]\n"
      "ds_read_b128 a[32:35], v3 offset:24576\n"
      "ds_read_b128 a[36:39], v4 offset:24576\n"
      "ds_read_b128 a[40:43], v3 offset:26624\n"
      "ds_read_b128 a[44:47], v4 offset:26624\n"
      "ds_read_b128 a[48:51], v3 offset:28672\n"
      "ds_read_b128 a[52:55], v4 offset:28672\n"
      "ds_read_b128 a[56:59], v3 offset:30720\n"
      "ds_read_b128 a[60:63], v4 offset:30720\n"
      "s_waitcnt lgkmcnt(0)\n"
      "v_mfma_f32_16x16x32_bf16 v[72:75], a[32:35], a[104:107], 0\n"
      "v_mfma_f32_16x16x32_bf16 v[72:75], a[36:39], a[108:111], v[72:75]\n"
      "v_mfma_f32_16x16x32_bf16 v[72:75], a[40:43], a[112:115], v[72:75]\n"
      "v_mfma_f32_16x16x32_bf16 v[72:75], a[44:47], a[116:119], v[72:75]\n"
      "v_mfma_f32_16x16x32_bf16 v[72:75], a[48:51], a[120:123], v[72:75]\n"
      "v_mfma_f32_16x16x32_bf16 v[72:75], a[52:55], a[124:127], v[72:75]\n"
      "v_mfma_f32_16x16x32_bf16 v[72:75], a[56:59], a[128:131], v[72:75]\n"
      "v_mfma_f32_16x16x32_bf16 v[72:75], a[60:63], a[132:135], v[72:75]\n"
      "v_mfma_f32_16x16x32_bf16 v[76:79], a[32:35], a[136:139], 0\n"
      "v_mfma_f32_16x16x32_bf16 v[76:79], a[36:39], a[140:143], v[76:79]\n"
      "v_mfma_f32_16x16x32_bf16 v[76:79], a[40:43], a[144:147], v[76:79]\n"
      "v_mfma_f32_16x16x32_bf16 v[76:79], a[44:47], a[148:151], v[76:79]\n"
      "v_mfma_f32_16x16x32_bf16 v[76:79], a[48:51], a[152:155], v[76:79]\n"
      "v_mfma_f32_16x16x32_bf16 v[76:79], a[52:55], a[156:159], v[76:79]\n"
      "v_mfma_f32_16x16x32_bf16 v[76:79], a[56:59], a[160:163], v[76:79]\n"
      "v_mfma_f32_16x16x32_bf16 v[76:79], a[60:63], a[164:167], v[76:79]\n"
      "v_mfma_f32_16x16x32_bf16 v[80:83], a[32:35], a[168:171], 0\n"
      "v_mfma_f32_16x16x32_bf16 v[80:83], a[36:39], a[172:175], v[80:83]\n"
      "v_mfma_f32_16x16x32_bf16 v[80:83], a[40:43], a[176:179], v[80:83]\n"
      "v_mfma_f32_16x16x32_bf16 v[80:83], a[44:47], a[180:183], v[80:83]\n"
      "v_mfma_f32_16x16x32_bf16 v[80:83], a[48:51], a[184:187], v[80:83]\n"
      "v_mfma_f32_16x16x32_bf16 v[80:83], a[52:55], a[188:191], v[80:83]\n"
      "v_mfma_f32_16x16x32_bf16 v[80:83], a[56:59], a[192:195], v[80:83]\n"
      "v_mfma_f32_16x16x32_bf16 v[80:83], a[60:63], a[196:199], v[80:83]\n"
      "s_nop 7\n"
      "v_max3_f32 v7, v7, v36, v37\n"
      "v_max3_f32 v7, v7, v38, v39\n"
      "v_max3_f32 v7, v7, v48, v49\n"
      "v_max3_f32 v7, v7, v50, v51\n"
      "v_max3_f32 v7, v7, v60, v61\n"
      "v_max3_f32 v7, v7, v62, v63\n"
      "v_max3_f32 v7, v7, v72, v73\n"
      "v_max3_f32 v7, v7, v74, v75\n"
      "v_max3_f32 v9, v9, v40, v41\n"
      "v_max3_f32 v9, v9, v42, v43\n"
      "v_max3_f32 v9, v9, v52, v53\n"
      "v_max3_f32 v9, v9, v54, v55\n"
      "v_max3_f32 v9, v9, v64, v65\n"
      "v_max3_f32 v9, v9, v66, v67\n"
      "v_max3_f32 v9, v9, v76, v77\n"
      "v_max3_f32 v9, v9, v78, v79\n"
      "v_max3_f32 v11, v11, v44, v45\n"
      "v_max3_f32 v11, v11, v46, v47\n"
      "v_max3_f32 v11, v11, v56, v57\n"
      "v_max3_f32 v11, v11, v58, v59\n"
      "v_max3_f32 v11, v11, v68, v69\n"
      "v_max3_f32 v11, v11, v70, v71\n"
      "v_max3_f32 v11, v11, v80, v81\n"
      "v_max3_f32 v11, v11, v82, v83\n"
      "s_barrier\n"
      "s_sub_u32 s20, s20, 1\n"
      "s_cmp_lg_u32 s20, 0\n"
      "s_cbranch_scc1 L_max_%=\n"
      "s_branch L_start_%=\n"
      "L_done_%=:\n"
      :
      : [args] "s"(args), [bid] "s"(bid), "{v0}"(tid)
      : "v1", "v2", "v3", "v4", "v5", "v6", "v7", "v8", "v9", "v10", "v11", "v12", "v13", "v14", "v15", "v16", "v17", "v18", "v19", "v20", "v21", "v22", "v23", "v24", "v25", "v26", "v27", "v28", "v29", "v30", "v31", "v32", "v33", "v34", "v35", "v36", "v37", "v38", "v39", "v40", "v41", "v42", "v43", "v44", "v45", "v46", "v47", "v48", "v49", "v50", "v51", "v52", "v53", "v54", "v55", "v56", "v57", "v58", "v59", "v60", "v61", "v62", "v63", "v64", "v65", "v66", "v67", "v68", "v69", "v70", "v71", "v72", "v73", "v74", "v75", "v76", "v77", "v78", "v79", "v80", "v81", "v82", "v83", "v84", "v85", "v86", "v87", "v88", "v89", "v90", "v91", "v92", "v93", "v94", "v95", "v96", "v97", "v98", "v99", "v100", "v101", "v102", "v103", "v104", "v105", "v106", "v107", "v108", "v109", "v110", "v111", "v112", "v113", "v114", "v115", "v116", "v117", "v118", "v119", "v120", "v121", "v122", "v123", "v124", "v125", "v126", "v127", "v128", "v129", "v130", "v131", "v132", "v133", "v134", "v135", "v136", "v137", "v138", "v139", "v140", "v141", "v142", "v143", "v144", "v145", "v146", "v147", "v148", "v149", "v150", "v151", "v152", "v153", "v154", "v155", "v156", "v157", "v158", "v159", "v160", "v161", "v162", "v163", "v164", "v165", "v166", "v167", "v168", "v169", "v170", "v171", "v172", "v173", "v174", "v175", "v176", "v177", "v178", "v179", "v180", "v181", "v182", "v183", "v184", "v185", "v186", "v187", "v188", "v189", "v190", "v191", "v192", "v193", "v194", "v195", "v196", "v197", "v198", "v199", "v200", "v201", "v202", "v203", "v204", "v205", "v206", "v207", "v208", "v209", "v210", "v211", "v212", "v213", "v214", "v215", "v216", "v217", "v218", "v219", "v220", "v221", "v222", "v223", "v224", "v225", "v226", "v227", "v228", "v229", "v230", "v231", "v232", "v233", "v234", "v235", "v236", "v237", "v238", "v239", "v240", "v241", "v242", "v243", "v244", "v245", "v246", "v247", "v248", "v249", "v250", "v251", "v252", "v253", "v254", "v255", "a0", "a1", "a2", "a3", "a4", "a5", "a6", "a7", "a8", "a9", "a10", "a11", "a12", "a13", "a14", "a15", "a16", "a17", "a18", "a19", "a20", "a21", "a22", "a23", "a24", "a25", "a26", "a27", "a28", "a29", "a30", "a31", "a32", "a33", "a34", "a35", "a36", "a37", "a38", "a39", "a40", "a41", "a42", "a43", "a44", "a45", "a46", "a47", "a48", "a49", "a50", "a51", "a52", "a53", "a54", "a55", "a56", "a57", "a58", "a59", "a60", "a61", "a62", "a63", "a64", "a65", "a66", "a67", "a68", "a69", "a70", "a71", "a72", "a73", "a74", "a75", "a76", "a77", "a78", "a79", "a80", "a81", "a82", "a83", "a84", "a85", "a86", "a87", "a88", "a89", "a90", "a91", "a92", "a93", "a94", "a95", "a96", "a97", "a98", "a99", "a100", "a101", "a102", "a103", "a104", "a105", "a106", "a107", "a108", "a109", "a110", "a111", "a112", "a113", "a114", "a115", "a116", "a117", "a118", "a119", "a120", "a121", "a122", "a123", "a124", "a125", "a126", "a127", "a128", "a129", "a130", "a131", "a132", "a133", "a134", "a135", "a136", "a137", "a138", "a139", "a140", "a141", "a142", "a143", "a144", "a145", "a146", "a147", "a148", "a149", "a150", "a151", "a152", "a153", "a154", "a155", "a156", "a157", "a158", "a159", "a160", "a161", "a162", "a163", "a164", "a165", "a166", "a167", "a168", "a169", "a170", "a171", "a172", "a173", "a174", "a175", "a176", "a177", "a178", "a179", "a180", "a181", "a182", "a183", "a184", "a185", "a186", "a187", "a188", "a189", "a190", "a191", "a192", "a193", "a194", "a195", "a196", "a197", "a198", "a199", "a200", "a201", "a202", "a203", "a204", "a205", "a206", "a207", "a208", "a209", "a210", "a211", "a212", "a213", "a214", "a215", "a216", "a217", "a218", "a219", "a220", "a221", "a222", "a223", "a224", "a225", "a226", "a227", "a228", "a229", "a230", "a231", "a232", "a233", "a234", "a235", "a236", "a237", "a238", "a239", "a240", "a241", "a242", "a243", "a244", "a245", "a246", "a247", "a248", "a249", "a250", "a251", "a252", "a253", "a254", "a255", "s8", "s9", "s10", "s11", "s12", "s13", "s14", "s15", "s16", "s17", "s18", "s19", "s20", "s21", "s22", "s23", "s24", "s25", "s26", "s27", "s28", "s29", "s30", "s31", "s34", "s35", "s36", "s37", "s38", "s39", "s40", "s41", "s42", "s43", "s44", "s45", "s46", "s47", "s48", "s49", "s50", "s51", "s52", "s53", "s54", "s55", "s56", "s57", "s58", "s59", "s60", "s61", "s62", "s63", "vcc", "scc", "memory");
}

extern "C" void kernel_launch(void* const* d_in, const int* in_sizes, int n_in, void* d_out, int out_size, void* d_ws, size_t ws_size,
                              hipStream_t stream) {
  attn_fwd_d256<<<256, 256, 0, stream>>>(d_in[0], d_in[1], d_in[2], d_out);
}
```
